```python
import torch

def _softmax(x, axis=-1):
    return torch.softmax(x, dim=axis)
def _gelu(x):
    return torch.nn.functional.gelu(x, approximate="tanh")
def _silu(x):
    return torch.nn.functional.silu(x)
def _sigmoid(x):
    return torch.sigmoid(x)
def _layernorm(x, axis=-1, eps=1e-5):
    m = x.mean(dim=axis, keepdim=True)
    v = x.var(dim=axis, keepdim=True, unbiased=False)
    return (x - m) / torch.sqrt(v + eps)
def _scatter_add(tgt, idx, src, axis=0):
    return tgt.clone().index_add_(axis, idx, src)
def _segment_sum(src, seg, n):
    out = torch.zeros((n,) + src.shape[1:], dtype=src.dtype)
    return out.index_add_(0, seg, src)
def _pad(x, pw):
    flat = tuple(v for pair in reversed(pw) for v in pair)
    return torch.nn.functional.pad(x, flat)

kernel_name = 'parallel_sum__mha_block__glu_gated_bilinear__prompt'

def setup_inputs():

    _w = (lambda a: (lambda t: t.to(torch.bfloat16) if t.is_floating_point() else t)(torch.as_tensor(a)))
    def _A_setup():
        B, S, D, H = 1, 2048, 640, 32
        return {
            "x": _w((lambda *s: torch.randn(*s, dtype=torch.bfloat16) * 0.05)(B, S, D)),
            "wqkv": _w((lambda *s: torch.randn(*s, dtype=torch.bfloat16) * 0.05)(D, 3 * D)),
            "wo": _w((lambda *s: torch.randn(*s, dtype=torch.bfloat16) * 0.05)(D, D)),
        }
    _A_in = _A_setup()

    def _B_setup():
        B, S, D, F = 1, 2048, 640, 1536
        return {
            "x": _w((lambda *s: torch.randn(*s, dtype=torch.bfloat16) * 0.05)(B, S, D)),
            "w_gate": _w((lambda *s: torch.randn(*s, dtype=torch.bfloat16) * 0.8)(D, F) * (D ** -0.5)),
            "w_up": _w((lambda *s: torch.randn(*s, dtype=torch.bfloat16) * 0.8)(D, F) * (D ** -0.5)),
            "w_down": _w((lambda *s: torch.randn(*s, dtype=torch.bfloat16) * 0.8)(F, D) * (F ** -0.5)),
        }
    _B_in = _B_setup()

    return {
        "A_x": _A_in["x"],
        "A_wqkv": _A_in["wqkv"],
        "A_wo": _A_in["wo"],
        "B_x": _B_in["x"],
        "B_w_gate": _B_in["w_gate"],
        "B_w_up": _B_in["w_up"],
        "B_w_down": _B_in["w_down"],
    }

def reference(A_x, A_wqkv, A_wo, B_x, B_w_gate, B_w_up, B_w_down):
    def _A_ref(x, wqkv, wo):
        B, S, D = x.shape
        H = 32
        Dh = D // H
        qkv = (lambda x, *d: x.permute(*d))((x @ wqkv).reshape(B, S, 3, H, Dh), 2, 0, 3, 1, 4)
        q, k, v = qkv[0], qkv[1], qkv[2]
        scores = torch.einsum("bhqd,bhkd->bhqk", q, k) * (Dh ** -0.5)
        p = _softmax(scores, axis=-1)
        o = torch.einsum("bhqk,bhkd->bhqd", p, v)
        return (lambda x, *d: x.permute(*d))(o, 0, 2, 1, 3).reshape(B, S, D) @ wo

    def _B_ref(x, w_gate, w_up, w_down):
        g = torch.einsum("bsd,df->bsf", x, w_gate)
        u = torch.einsum("bsd,df->bsf", x, w_up)
        h = g * u
        return torch.einsum("bsf,fd->bsd", h, w_down)

    x = A_x
    xn = _layernorm(x, axis=-1)
    return x + _A_ref(xn, A_wqkv, A_wo) + _B_ref(xn, B_w_gate, B_w_up, B_w_down)
```

```cpp
#include <hip/hip_runtime.h>
#include <stdint.h>

#define N_LAUNCH 1
#define STAMPS 0
#define ROT 0

typedef __attribute__((ext_vector_type(8))) __bf16 bf16x8;
typedef __attribute__((ext_vector_type(16))) __bf16 bf16x16;
typedef __attribute__((ext_vector_type(16))) float f32x16;
typedef __attribute__((ext_vector_type(8))) float f32x8;
typedef __attribute__((ext_vector_type(4))) float f32x4;
typedef __attribute__((ext_vector_type(4))) unsigned u32x4;
typedef __attribute__((ext_vector_type(2))) unsigned u32x2;
typedef __attribute__((ext_vector_type(4))) short s16x4;
typedef __attribute__((ext_vector_type(2))) __bf16 bf16x2;
typedef __attribute__((ext_vector_type(2))) float f32x2;
#define LAS __attribute__((address_space(3)))
#define GAS __attribute__((address_space(1)))
typedef LAS char* lptr;
#define DI __device__ __forceinline__
#define SBAR() __builtin_amdgcn_sched_barrier(0)
#define RLX_AGENT __ATOMIC_RELAXED, __HIP_MEMORY_SCOPE_AGENT
#define RLX_WG __ATOMIC_RELAXED, __HIP_MEMORY_SCOPE_WORKGROUP

constexpr unsigned WS_G = 0;
constexpr unsigned WS_F1 = 4096;
constexpr unsigned WS_FH = 8192;
constexpr unsigned WS_FW = 12288;
constexpr unsigned WS_FO = 16384;
constexpr unsigned WS_ST = 24576;
constexpr unsigned WS_STAMP = 49152;
constexpr unsigned WS_QKV = 1u << 20;
constexpr unsigned QKV_REC = 122880;
constexpr unsigned WS_AH = WS_QKV + 64 * QKV_REC;
constexpr unsigned AH_REC = 272 * 512;
constexpr unsigned WS_WT = WS_AH + 64 * AH_REC;
constexpr unsigned WS_END = WS_WT + 4 * 136 * 5120;

constexpr int L_X = 0, XSLOT = 16384;
constexpr int L_WQ = 65536, WQSLOT = 8192;
constexpr int L_WG = 98304, WGSLOT = 12352, WG_U = 6176;
constexpr int L_MU = 160064, L_RS = L_MU + 512, L_CSQ = L_RS + 512, L_CSG = L_CSQ + 512, L_CNT = L_CSG + 768;
constexpr int LDS_BYTES = L_CNT + 64;
constexpr int KVT = 3072, KVSLOT = 8 * KVT;
constexpr int kvslot_off(int s) { return s == 0 ? L_WQ : (s == 1 ? 0 : KVSLOT); }

constexpr unsigned SPIN_CAP = 1u << 18;

struct Params { const char* x; const char* wqkv; const char* wo; const char* wg; const char* wu; const char* wd; char* out; char* ws; };

DI f32x16 mfma32(bf16x8 a, bf16x8 b, f32x16 c) { return __builtin_amdgcn_mfma_f32_32x32x16_bf16(a, b, c, 0, 0, 0); }
DI f32x4 mfma16(bf16x8 a, bf16x8 b, f32x4 c) { return __builtin_amdgcn_mfma_f32_16x16x32_bf16(a, b, c, 0, 0, 0); }
DI bf16x8 lds_b128(lptr p) { return *(const LAS bf16x8*)p; }
DI f32x4 lds_f4(lptr p) { return *(const LAS f32x4*)p; }
DI bf16x8 lds_tr(lptr p, int second) {
  s16x4 lo = __builtin_amdgcn_ds_read_tr16_b64_v4i16((LAS s16x4*)p);
  s16x4 hi = __builtin_amdgcn_ds_read_tr16_b64_v4i16((LAS s16x4*)(p + second));
  return __builtin_bit_cast(bf16x8, __builtin_shufflevector(lo, hi, 0, 1, 2, 3, 4, 5, 6, 7));
}
DI unsigned pk2(float a, float b) { f32x2 v = {a, b}; return __builtin_bit_cast(unsigned, __builtin_convertvector(v, bf16x2)); }
DI float bf_round(float a) { return (float)(__bf16)a; }
DI void dma16(unsigned voff, const char* sbase, unsigned lds_dst) {
  asm volatile("s_mov_b32 m0, %2\n\ts_nop 0\n\tglobal_load_lds_dwordx4 %0, %1" :: "v"(voff), "s"(sbase), "s"(lds_dst) : "memory");
}
DI void dma16_sc1(unsigned voff, const char* sbase, unsigned lds_dst) {
  asm volatile("s_mov_b32 m0, %2\n\ts_nop 0\n\tglobal_load_lds_dwordx4 %0, %1 sc1" :: "v"(voff), "s"(sbase), "s"(lds_dst) : "memory");
}
template <int N> DI void vmwait() { asm volatile("s_waitcnt vmcnt(%0)" :: "n"(N) : "memory"); }
template <int N> DI void vmwait_bar() { asm volatile("s_waitcnt vmcnt(%0) lgkmcnt(0)\n\ts_barrier" :: "n"(N) : "memory"); }
DI void bar_only() { asm volatile("s_waitcnt lgkmcnt(0)\n\ts_barrier" ::: "memory"); }
DI void st16(__amdgpu_buffer_rsrc_t r, unsigned off, u32x4 v) { __builtin_amdgcn_raw_buffer_store_b128(v, r, off, 0, 16); }
DI u32x4 ld16(__amdgpu_buffer_rsrc_t r, unsigned off, unsigned soff) { return __builtin_amdgcn_raw_buffer_load_b128(r, off, soff, 16); }
DI void set_flag(char* ws, unsigned off, unsigned tag) { __hip_atomic_store((GAS unsigned*)(ws + off), tag, RLX_AGENT); }
DI void poll_flags(char* ws, unsigned off, bool active, unsigned tag) {
  for (unsigned spins = 0; spins < SPIN_CAP; ++spins) {
    unsigned v = tag;
    if (active) v = __hip_atomic_load((GAS unsigned*)(ws + off), RLX_AGENT);
    if (__all(v == tag)) break;
    __builtin_amdgcn_s_sleep(1);
  }
  asm volatile("" ::: "memory");
}
DI float wave_sum(float v) {
  v += __builtin_bit_cast(float, __builtin_amdgcn_update_dpp(0, __builtin_bit_cast(int, v), 0xB1, 0xf, 0xf, true));
  v += __builtin_bit_cast(float, __builtin_amdgcn_update_dpp(0, __builtin_bit_cast(int, v), 0x4E, 0xf, 0xf, true));
  v += __builtin_bit_cast(float, __builtin_amdgcn_update_dpp(0, __builtin_bit_cast(int, v), 0x141, 0xf, 0xf, true));
  v += __builtin_bit_cast(float, __builtin_amdgcn_update_dpp(0, __builtin_bit_cast(int, v), 0x140, 0xf, 0xf, true));
  const int b = __builtin_bit_cast(int, v);
  return (__builtin_bit_cast(float, __builtin_amdgcn_readlane(b, 0)) + __builtin_bit_cast(float, __builtin_amdgcn_readlane(b, 16))) +
         (__builtin_bit_cast(float, __builtin_amdgcn_readlane(b, 32)) + __builtin_bit_cast(float, __builtin_amdgcn_readlane(b, 48)));
}
#define STAMP(i) do {} while (0)

constexpr int q_grp(int s) { return ((s >= 1 && s <= 12) ? 2 : 0) + (s <= 15 ? 2 : 0); }
constexpr int q_issued(int s) { int n = 25; for (int i = 0; i < s; ++i) n += q_grp(i); return n; }
constexpr int q_endW(int s) { return s == 0 ? 6 : s == 1 ? 8 : s == 2 ? 14 : s == 3 ? 16 : q_issued(s - 4) + q_grp(s - 4); }
constexpr int q_endX(int c) { return c == 0 ? 4 : c == 1 ? 12 : c == 2 ? 20 : c == 3 ? 24 : q_issued(2 * (c - 4) + 2) + 2; }
constexpr int q_wait(int s) {
  if (s >= 19) return 0;
  int e = q_endW(s + 1); if (((s + 1) & 1) == 0 && q_endX((s + 1) / 2) > e) e = q_endX((s + 1) / 2);
  return q_issued(s) - e; }
constexpr int g_grp(int s) { return (s <= 13 ? 3 : 0) + (s == 13 ? 3 : 0); }
constexpr int g_issued(int s) { int n = 16; for (int i = 0; i < s; ++i) n += g_grp(i); return n; }
constexpr int g_endW(int s) { return s <= 4 ? 3 * (s + 1) : g_issued(s - 5) + 3; }
constexpr int g_wait(int s) { return g_issued(s) - g_endW(s + 1); }
constexpr int NLOCK = 14;

struct P1Ctx {
  lptr lds; unsigned lds0; int off; int lane, wr, wc;
  const char* xb; const char* wb; unsigned vx0, vx1, vw, vg0, vg1, vg2; int qw;
  lptr xa[4]; lptr wa[2];
};
DI int rot_chunk(int cl, int off) { int c = cl + off; return c >= 10 ? c - 10 : c; }

template <int C, int HF, int I> DI void q_dmaX1(const P1Ctx& c) {
  const char* b = c.xb + rot_chunk(C, c.off) * 128 + HF * 64 * 1280;
  dma16(I ? c.vx1 : c.vx0, b, c.lds0 + L_X + (C & 3) * XSLOT + HF * 8192 + c.qw * 2048 + I * 1024);
}
template <int C, int HF> DI void q_dmaX(const P1Ctx& c) { q_dmaX1<C, HF, 0>(c); q_dmaX1<C, HF, 1>(c); }
template <int S, int I> DI void q_dmaW1(const P1Ctx& c) {
  const char* b = c.wb + (size_t)(rot_chunk(S >> 1, c.off) * 64 + (S & 1) * 32 + I * 4) * 3840;
  dma16(c.vw, b, c.lds0 + L_WQ + (S & 3) * WQSLOT + c.qw * 2048 + I * 1024);
}
template <int S> DI void q_dmaW(const P1Ctx& c) { q_dmaW1<S, 0>(c); q_dmaW1<S, 1>(c); }
template <int S, int I> DI void q_dmaE(const P1Ctx& c) {
  if constexpr (S >= 0) {
    if constexpr (I < 2) { if constexpr (S >= 1 && S <= 12) q_dmaX1<(S - 1) / 2 + 4, (S - 1) & 1, I>(c); }
    else { if constexpr (S <= 15) q_dmaW1<S + 4, I - 2>(c); }
  }
}
constexpr int g_slot(int s) { return s == 19 ? L_X + 2 * XSLOT : L_WG + (s % 5) * WGSLOT; }
template <int S, int I> DI void g_dmaW1(const P1Ctx& c) {
  const char* b = c.wb + (size_t)(rot_chunk(S >> 1, c.off) * 64 + (S & 1) * 32) * 3072;
  dma16(I == 0 ? c.vg0 : I == 1 ? c.vg1 : c.vg2, b, c.lds0 + g_slot(S) + (c.qw >> 1) * WG_U + (c.qw & 1) * 3072 + I * 1024);
}
template <int S> DI void g_dmaW(const P1Ctx& c) { g_dmaW1<S, 0>(c); g_dmaW1<S, 1>(c); g_dmaW1<S, 2>(c); }
template <int S, int I> DI void g_dmaE(const P1Ctx& c) { if constexpr (S >= 0 && S <= 13) g_dmaW1<S + 5, I>(c); }
struct QFr { bf16x8 x[2], w[2]; };
struct GFr { bf16x8 x[2], w[3]; };
template <int S, int A, int R> DI bf16x8 rdX(const P1Ctx& c) { return lds_b128(c.xa[2 * (S & 1) + A] + ((S >> 1) & 3) * XSLOT + R * 4096); }
template <int S, int A, int T> DI bf16x8 q_rdW(const P1Ctx& c) { return lds_tr(c.wa[T] + (S & 3) * WQSLOT + A * 4096, 1024); }
template <int S, int A, int T> DI bf16x8 g_rdW(const P1Ctx& c) { return lds_tr((S == 19 ? c.wa[1] : c.wa[0] + (S % 5) * WGSLOT) + A * 3072 + T * 32, 768); }
template <int S, int A> DI void q_rd(QFr& f, const P1Ctx& c) { f.w[0] = q_rdW<S, A, 0>(c); f.x[0] = rdX<S, A, 0>(c); f.x[1] = rdX<S, A, 1>(c); f.w[1] = q_rdW<S, A, 1>(c); }
template <int S, int A> DI void g_rd(GFr& f, const P1Ctx& c) { f.w[0] = g_rdW<S, A, 0>(c); f.x[0] = rdX<S, A, 0>(c); f.x[1] = rdX<S, A, 1>(c); f.w[1] = g_rdW<S, A, 1>(c); f.w[2] = g_rdW<S, A, 2>(c); }
#define QMM(r, t) acc[r][t] = VT ? mfma32(u.x[r], u.w[t], acc[r][t]) : mfma32(u.w[t], u.x[r], acc[r][t])
template <bool VT, bool RD, int RS, int RA, int ES, int EI> DI void q_half(f32x16 (&acc)[2][2], f32x4 (&cs)[2], const QFr& u, QFr& n, bf16x8 sel, bool docs, const P1Ctx& c) {
  QMM(0, 0); if constexpr (RD) { n.w[0] = q_rdW<RS, RA, 0>(c); } SBAR();
  QMM(1, 0); if constexpr (RD) { n.x[0] = rdX<RS, RA, 0>(c); n.x[1] = rdX<RS, RA, 1>(c); } SBAR();
  QMM(0, 1); if constexpr (RD) { n.w[1] = q_rdW<RS, RA, 1>(c); } SBAR(); q_dmaE<ES, EI>(c); SBAR();
  QMM(1, 1); SBAR(); q_dmaE<ES, EI + 1>(c); SBAR();
  if (docs) { cs[0] = mfma16(sel, u.w[0], cs[0]); cs[1] = mfma16(sel, u.w[1], cs[1]); }
  SBAR();
}
template <int S, bool VT> DI void q_stage(f32x16 (&acc)[2][2], f32x4 (&cs)[2], QFr& fa, QFr& fb, bf16x8 sel, const P1Ctx& c) {
  q_half<VT, true, S, 1, S - 1, 2>(acc, cs, fa, fb, sel, c.wr == 0, c);
  vmwait_bar<q_wait(S)>();
  q_half<VT, (S < 19), S + 1, 0, S, 0>(acc, cs, fb, fa, sel, c.wr == 1, c);
  if constexpr (S < 19) q_stage<S + 1, VT>(acc, cs, fa, fb, sel, c);
}
#define GMM(r, t) acc[r][t] = mfma32(u.w[t], u.x[r], acc[r][t])
template <bool RD, int RS, int RA, int ES, int E0, int E1> DI void g_half(f32x16 (&acc)[2][3], f32x4 (&cs)[3], const GFr& u, GFr& n, bf16x8 sel, bool docs, const P1Ctx& c) {
  GMM(0, 0); if constexpr (RD) { n.w[0] = g_rdW<RS, RA, 0>(c); } SBAR();
  GMM(1, 0); if constexpr (RD) { n.x[0] = rdX<RS, RA, 0>(c); n.x[1] = rdX<RS, RA, 1>(c); } SBAR();
  GMM(0, 1); if constexpr (RD) { n.w[1] = g_rdW<RS, RA, 1>(c); } SBAR();
  GMM(1, 1); if constexpr (RD) { n.w[2] = g_rdW<RS, RA, 2>(c); } SBAR();
  GMM(0, 2); SBAR(); if constexpr (E0 >= 0) g_dmaE<ES, E0>(c); SBAR();
  GMM(1, 2); SBAR(); if constexpr (E1 >= 0) g_dmaE<ES, E1>(c); SBAR();
  if (docs) { cs[0] = mfma16(sel, u.w[0], cs[0]); cs[1] = mfma16(sel, u.w[1], cs[1]); cs[2] = mfma16(sel, u.w[2], cs[2]); }
  SBAR();
}
template <int S> DI void g_stage(f32x16 (&acc)[2][3], f32x4 (&cs)[3], GFr& fa, GFr& fb, bf16x8 sel, const P1Ctx& c) {
  g_half<true, S, 1, S - 1, (S == NLOCK ? -1 : 2), -1>(acc, cs, fa, fb, sel, c.wr == 0, c);
  if constexpr (S < NLOCK) vmwait_bar<g_wait(S)>();
  g_half<(S < 19 && S != NLOCK - 1), S + 1, 0, S, 0, 1>(acc, cs, fb, fa, sel, c.wr == 1, c);
  if constexpr (S == NLOCK - 1) { g_dmaE<S, 2>(c); g_dmaW<19>(c); }
  if constexpr (S < 19 && S != NLOCK - 1) g_stage<S + 1>(acc, cs, fa, fb, sel, c);
}
template <int NT> DI void cs_exchange(const f32x4 (&cs)[NT], lptr arr, lptr cnt, int lane) {
  if (lane < 16) {
#pragma unroll
    for (int t = 0; t < NT; ++t) {
      __hip_atomic_fetch_add((LAS float*)(arr + (t * 32 + lane) * 4), cs[t][0], RLX_WG);
      __hip_atomic_fetch_add((LAS float*)(arr + (t * 32 + 16 + lane) * 4), cs[t][1], RLX_WG);
    }
  }
  asm volatile("s_waitcnt lgkmcnt(0)" ::: "memory");
  if (lane == 0) __hip_atomic_fetch_add((LAS unsigned*)cnt, 1u, RLX_WG);
  for (unsigned spins = 0; spins < SPIN_CAP; ++spins) {
    if (__builtin_amdgcn_readfirstlane(__hip_atomic_load((LAS unsigned*)cnt, RLX_WG)) >= 2u) break;
    __builtin_amdgcn_s_sleep(0);
  }
  asm volatile("" ::: "memory");
}

constexpr float QSCALE = 0.22360679774997896f * 1.4426950408889634f;

template <int PH>
__global__ __launch_bounds__(512, 2) void fused(Params p) {
  extern __shared__ __attribute__((aligned(16))) char smem[];
  const lptr lds = (lptr)smem;
  const unsigned lds0 = (unsigned)(uintptr_t)lds;
  const int tid = threadIdx.x, lane = tid & 63;
  const int wid = __builtin_amdgcn_readfirstlane(tid >> 6);
  const int b = blockIdx.x, xcd = b & 7, j = b >> 3;
  const __amdgpu_buffer_rsrc_t wsr = __builtin_amdgcn_make_buffer_rsrc(p.ws, 0, WS_END, 0x00020000);
  const int r32 = lane & 31, hh = lane >> 5;
  STAMP(0);
  unsigned gword, pf; u32x4 sx4; unsigned sx1;
  {
    unsigned long long pc; unsigned clen;
    asm volatile("s_getpc_b64 %0\n.Lcode_a%c2:\n\ts_mov_b32 %1, .Lcode_z%c2-.Lcode_a%c2" : "=s"(pc), "=s"(clen) : "n"(PH));
    const char* xr = p.x + (size_t)(b * 8 + wid) * 1280;
    asm volatile("global_load_dword %0, %1, %2" : "=v"(pf) : "v"(min((unsigned)tid * 128u, clen) & ~3u), "s"(pc));
    asm volatile("global_load_dword %0, %1, %2 sc1" : "=v"(gword) : "v"(0), "s"(p.ws));
    asm volatile("global_load_dwordx4 %0, %1, %2" : "=v"(sx4) : "v"(lane * 16), "s"(xr));
    asm volatile("global_load_dword %0, %1, %2 offset:1024" : "=v"(sx1) : "v"(lane * 4), "s"(xr));
  }
  unsigned tag = 0;
  if constexpr (!(PH & 1)) {
    asm volatile("s_waitcnt vmcnt(0)" : "+v"(gword), "+v"(pf), "+v"(sx4), "+v"(sx1));
    tag = __builtin_amdgcn_readfirstlane(gword) * 1664525u + 1013904223u;
  }

  if constexpr (PH & 1) {
    const int rb = (xcd & 1) * 8 + (j & 7), cg = (xcd >> 1) * 4 + (j >> 3);
    const int role = wid >> 2;
    P1Ctx c; c.lds = lds; c.lds0 = lds0; c.lane = lane; c.qw = wid & 3; c.wr = (wid >> 1) & 1; c.wc = wid & 1;
    c.off = ROT ? ((j & 7) + 3 * (j >> 3)) % 10 : 0;
    const int cgq = cg < 15 ? cg : 14;
    const bool vtype = cgq >= 10;
    const int i16 = lane & 15, q4 = i16 >> 2, p4 = i16 & 3, gq = (lane >> 4) & 1;
    const int pperm = ((p4 & 1) << 1) | (p4 >> 1);
    if (role == 0) {
      const int r8 = lane >> 3, p8 = lane & 7;
      c.xb = p.x + (size_t)(128 * rb + 16 * c.qw) * 1280;
      c.vx0 = r8 * 1280 + ((p8 ^ (r8 >> 1)) << 4); c.vx1 = (8 + r8) * 1280 + ((p8 ^ (r8 >> 1) ^ 4) << 4);
      c.wb = p.wqkv + (size_t)(8 * c.qw) * 3840 + cgq * 256;
      c.vw = (lane >> 4) * 3840 + (((lane & 15) ^ ((lane >> 4) << 2)) << 4);
      q_dmaX<0, 0>(c); q_dmaX<0, 1>(c); q_dmaW<0>(c); q_dmaW<1>(c); q_dmaX<1, 0>(c); q_dmaX<1, 1>(c); q_dmaW<2>(c); q_dmaW<3>(c);
      q_dmaX<2, 0>(c); q_dmaX<2, 1>(c); q_dmaX<3, 0>(c); q_dmaX<3, 1>(c);
    } else {
      c.wb = ((c.qw >> 1) ? p.wu : p.wg) + (size_t)(16 * (c.qw & 1)) * 3072 + cg * 192;
      c.vg0 = (lane / 12) * 3072 + (lane % 12) * 16; c.vg1 = ((64 + lane) / 12) * 3072 + ((64 + lane) % 12) * 16; c.vg2 = ((128 + lane) / 12) * 3072 + ((128 + lane) % 12) * 16;
      g_dmaW<0>(c); g_dmaW<1>(c); g_dmaW<2>(c); g_dmaW<3>(c); g_dmaW<4>(c);
    }
    for (int i = tid; i < (L_CNT + 64 - L_CSQ) / 4; i += 512) *(LAS unsigned*)(lds + L_CSQ + i * 4) = 0u;
    if (role == 0) vmwait<24>(); else vmwait<15>();
    asm volatile("" : "+v"(gword), "+v"(pf), "+v"(sx4), "+v"(sx1));
    tag = __builtin_amdgcn_readfirstlane(gword) * 1664525u + 1013904223u;
    {
      float e[10];
#pragma unroll
      for (int i = 0; i < 4; ++i) { e[2 * i] = __builtin_bit_cast(float, sx4[i] << 16); e[2 * i + 1] = __builtin_bit_cast(float, sx4[i] & 0xffff0000u); }
      e[8] = __builtin_bit_cast(float, sx1 << 16); e[9] = __builtin_bit_cast(float, sx1 & 0xffff0000u);
      float s = 0.f;
#pragma unroll
      for (int i = 0; i < 10; ++i) s += e[i];
      const float mean = wave_sum(s) * (1.0f / 640.0f);
      float q = 0.f;
#pragma unroll
      for (int i = 0; i < 10; ++i) { const float d = e[i] - mean; q += d * d; }
      const float var = wave_sum(q) * (1.0f / 640.0f);
      const float sd = __builtin_sqrtf(bf_round(bf_round(var) + 1e-5f));
      const unsigned pay = pk2(mean, sd);
      if (lane == 0) __hip_atomic_store((GAS unsigned long long*)(p.ws + WS_ST + (b * 8 + wid) * 8), ((unsigned long long)tag << 32) | pay, RLX_AGENT);
    }
#pragma unroll
    for (int a = 0; a < 4; ++a) c.xa[a] = lds + L_X + ((64 * c.wr + r32) * 128 + (((2 * a + hh) ^ ((r32 >> 1) & 7)) << 4));
    bf16x8 sel;
    { const unsigned o = ((lane & 15) == ((lane >> 4) & 1)) ? 0x3f803f80u : 0u; u32x4 t = {o, o, o, o}; sel = __builtin_bit_cast(bf16x8, t); }
    STAMP(1);
    if (role == 0) {
      const int pp = vtype ? p4 : pperm;
#pragma unroll
      for (int t = 0; t < 2; ++t) c.wa[t] = lds + L_WQ + ((8 * hh + q4) * 256 + (((2 * c.wc + t) ^ q4) << 6) + gq * 32 + pp * 8);
      f32x16 acc[2][2] = {}; f32x4 cs[2] = {}; QFr fa, fb;
      vmwait_bar<25 - 6>();
      q_rd<0, 0>(fa, c);
      if (vtype) q_stage<0, true>(acc, cs, fa, fb, sel, c); else q_stage<0, false>(acc, cs, fa, fb, sel, c);
      STAMP(2);
      const int u = b * 4 + c.qw; const bool pt = u < 544;
      const int pks = u >> 2, pcg = u & 3;
      const int ptl = L_WQ + c.qw * 6144;
      if (pt) {
        const char* src = (pks < 40 ? p.wo + (size_t)pks * 16 * 1280 : p.wd + (size_t)(pks - 40) * 16 * 1280) + pcg * 320;
#pragma unroll
        for (int n = 0; n < 5; ++n) { const int o = 64 * n + lane; dma16((o / 20) * 1280 + (o % 20) * 16, src, lds0 + ptl + n * 1024); }
      }
      cs_exchange<2>(cs, lds + L_CSQ + c.wc * 256, lds + L_CNT + c.wc * 4, lane);
      if (cg < 15) {
        if (!vtype) {
          const float sc = cg < 5 ? QSCALE : 1.0f;
#pragma unroll
          for (int r = 0; r < 2; ++r) {
            const int row = 64 * c.wr + 32 * r + r32;
            const float mu = *(LAS float*)(lds + L_MU + row * 4), rs = *(LAS float*)(lds + L_RS + row * 4) * sc;
            const unsigned base = WS_QKV + (4 * rb + 2 * c.wr + r) * QKV_REC + (16 * cg + 8 * c.wc) * 512 + lane * 16;
#pragma unroll
            for (int t = 0; t < 2; ++t)
#pragma unroll
              for (int uu = 0; uu < 2; ++uu) {
                const f32x4 c0 = lds_f4(lds + L_CSQ + (c.wc * 64 + t * 32 + 16 * uu + 4 * hh) * 4), c1 = lds_f4(lds + L_CSQ + (c.wc * 64 + t * 32 + 16 * uu + 8 + 4 * hh) * 4);
                float y[8];
#pragma unroll
                for (int e = 0; e < 4; ++e) { y[e] = (acc[r][t][8 * uu + e] - mu * c0[e]) * rs; y[4 + e] = (acc[r][t][8 * uu + 4 + e] - mu * c1[e]) * rs; }
                st16(wsr, base + (4 * t + 2 * uu) * 512, u32x4{pk2(y[0], y[1]), pk2(y[2], y[3]), pk2(y[4], y[5]), pk2(y[6], y[7])});
              }
          }
        } else {
#pragma unroll
          for (int t = 0; t < 2; ++t) {
            const int col = 128 * (cg - 10) + 64 * c.wc + 32 * t + r32, head = col / 20, d = col - head * 20;
            const float csv = *(LAS float*)(lds + L_CSQ + (c.wc * 64 + t * 32 + r32) * 4);
#pragma unroll
            for (int r = 0; r < 2; ++r) {
              const unsigned base = WS_QKV + (4 * rb + 2 * c.wr + r) * QKV_REC + 81920 + head * 1280 + hh * 320 + d * 16;
#pragma unroll
              for (int s = 0; s < 2; ++s) {
                const int row0 = 64 * c.wr + 32 * r + 16 * s + 4 * hh;
                const f32x4 m0 = lds_f4(lds + L_MU + row0 * 4), m1 = lds_f4(lds + L_MU + (row0 + 8) * 4), r0 = lds_f4(lds + L_RS + row0 * 4), r1 = lds_f4(lds + L_RS + (row0 + 8) * 4);
                float y[8];
#pragma unroll
                for (int e = 0; e < 4; ++e) { y[e] = (acc[r][t][8 * s + e] - m0[e] * csv) * r0[e]; y[4 + e] = (acc[r][t][8 * s + 4 + e] - m1[e] * csv) * r1[e]; }
                st16(wsr, base + s * 640, u32x4{pk2(y[0], y[1]), pk2(y[2], y[3]), pk2(y[4], y[5]), pk2(y[6], y[7])});
              }
            }
          }
        }
      }
      vmwait<0>();
      if (cg < 15 && lane == 0) set_flag(p.ws, WS_F1 + ((2 * cg + c.wc) * 32 + 2 * rb + c.wr) * 4, tag);
      STAMP(3);
      if (pt) {
        lptr a = lds + ptl + ((8 * hh + q4) * 320 + gq * 32 + pperm * 8);
#pragma unroll
        for (int t = 0; t < 5; ++t) st16(wsr, WS_WT + (pcg * 136 + pks) * 5120 + t * 1024 + lane * 16, __builtin_bit_cast(u32x4, lds_tr(a + t * 64, 4 * 320)));
        vmwait<0>();
        if (lane == 0) set_flag(p.ws, WS_FW + (pcg * 160 + pks) * 4, tag);
      }
      STAMP(4);
    } else {
      const int lo = gq * WG_U + (8 * hh + q4) * 192 + c.wc * 96 + pperm * 8;
      c.wa[0] = lds + L_WG + lo; c.wa[1] = lds + g_slot(19) + lo;
      f32x16 acc[2][3] = {}; f32x4 cs[3] = {}; GFr fa, fb;
      vmwait_bar<16 - 3>();
      g_rd<0, 0>(fa, c);
      g_stage<0>(acc, cs, fa, fb, sel, c);
      if (c.qw < 2) {
        const int row = c.qw * 64 + lane; unsigned long long g = 0;
        for (unsigned spins = 0; spins < SPIN_CAP; ++spins) {
          g = __hip_atomic_load((GAS unsigned long long*)(p.ws + WS_ST + (128 * rb + row) * 8), RLX_AGENT);
          if (__all((unsigned)(g >> 32) == tag)) break;
          __builtin_amdgcn_s_sleep(1);
        }
        const unsigned pay = (unsigned)g;
        *(LAS float*)(lds + L_MU + row * 4) = __builtin_bit_cast(float, pay << 16);
        *(LAS float*)(lds + L_RS + row * 4) = 1.0f / __builtin_bit_cast(float, pay & 0xffff0000u);
      }
#pragma unroll
      for (int s = NLOCK; s < 19; ++s) bar_only();
      vmwait_bar<0>();
      STAMP(2);
      g_rd<NLOCK, 0>(fa, c);
      g_stage<NLOCK>(acc, cs, fa, fb, sel, c);
      STAMP(3);
      cs_exchange<3>(cs, lds + L_CSG + c.wc * 384, lds + L_CNT + 8 + c.wc * 4, lane);
#pragma unroll
      for (int r = 0; r < 2; ++r) {
        const int row = 64 * c.wr + 32 * r + r32;
        const float mu = *(LAS float*)(lds + L_MU + row * 4), rs = *(LAS float*)(lds + L_RS + row * 4), rs2 = rs * rs;
        const unsigned base = WS_AH + (4 * rb + 2 * c.wr + r) * AH_REC + (80 + 12 * cg + 6 * c.wc) * 512 + lane * 16;
#pragma unroll
        for (int t = 0; t < 3; ++t) {
          float y[8];
#pragma unroll
          for (int i = 0; i < 2; ++i) {
            const f32x4 cgv = lds_f4(lds + L_CSG + (c.wc * 96 + t * 32 + 8 * i + 4 * hh) * 4), cuv = lds_f4(lds + L_CSG + (c.wc * 96 + t * 32 + 16 + 8 * i + 4 * hh) * 4);
#pragma unroll
            for (int e = 0; e < 4; ++e) y[4 * i + e] = (acc[r][t][4 * i + e] - mu * cgv[e]) * (acc[r][t][8 + 4 * i + e] - mu * cuv[e]) * rs2;
          }
          st16(wsr, base + 2 * t * 512, u32x4{pk2(y[0], y[1]), pk2(y[2], y[3]), pk2(y[4], y[5]), pk2(y[6], y[7])});
        }
      }
      vmwait<0>();
      if (lane == 0) set_flag(p.ws, WS_FH + ((2 * rb + c.wr) * 32 + 2 * cg + c.wc) * 4, tag);
      STAMP(4);
    }
  }

  if constexpr (PH & 2) {
    const int head = xcd * 4 + (j & 3), qb = j >> 2, odd = head & 1, m5 = (head >> 1) * 5;
    {
      const int ty = lane >> 4, half = (lane >> 3) & 1;
      const int sg = (ty * 640 + head * 20 + (half ? 19 : 0)) >> 6;
      if (wid == 7) for (unsigned spins = 0; spins < SPIN_CAP; ++spins) {
        u32x4 v = {tag, tag, tag, tag};
        if (lane < 48) v = ld16(wsr, WS_F1 + sg * 128 + (lane & 7) * 16, 0);
        if (__all(v[0] == tag && v[1] == tag && v[2] == tag && v[3] == tag)) break;
        __builtin_amdgcn_s_sleep(8);
      }
      asm volatile("" ::: "memory");
    }
    STAMP(5);
    bar_only();
    unsigned vkv[3]; bool act[3];
#pragma unroll
    for (int n = 0; n < 3; ++n) {
      const int o = 64 * n + lane;
      if (o < 96) {
        const int kp = o >> 5, sl = o & 31, gp = odd ? (kp == 2 ? m5 + 2 : m5 + 3 + kp) : m5 + kp;
        const int key = (sl & ~12) | ((sl & 4) << 1) | ((sl & 8) >> 1);
        vkv[n] = (80 + gp) * 512 + key * 16; act[n] = true;
      } else {
        const int v = o - 96, blk = v / 24, i = v - blk * 24;
        const int dd = i < 16 ? (8 * ((i >> 2) & 1) + 4 * (i >> 3) + (i & 3) + (odd ? 4 : 0)) : (odd ? (i & 3) : 16 + (i & 3));
        vkv[n] = 81920 + head * 1280 + blk * 320 + dd * 16; act[n] = i < 20;
      }
    }
    const char* kvb = p.ws + WS_QKV + (size_t)wid * QKV_REC;
    auto kv_dma = [&](int chunk, int slotoff) {
      const char* s = kvb + (size_t)((chunk + qb) & 7) * 8 * QKV_REC; const unsigned d = __builtin_amdgcn_readfirstlane(lds0 + slotoff + wid * KVT);
      dma16_sc1(vkv[0], s, d);
      if (act[1]) dma16_sc1(vkv[1], s, d + 1024);
      if (act[2]) dma16_sc1(vkv[2], s, d + 2048);
    };
    const unsigned qrec = WS_QKV + (qb * 8 + wid) * QKV_REC;
    u32x4 q1u = ld16(wsr, qrec + ((odd ? m5 + 3 : m5) + hh) * 512 + r32 * 16, 0);
    u32x4 q2u = ld16(wsr, qrec + (m5 + 2) * 512 + r32 * 16, 0);
    if (lane < 12) { const u32x4 one = {0x3f803f80u, 0x3f803f80u, 0x3f803f80u, 0x3f803f80u};
      const int s = lane >> 2; *(LAS u32x4*)(lds + (s == 0 ? kvslot_off(0) : s == 1 ? kvslot_off(1) : kvslot_off(2)) + wid * KVT + 1536 + (lane & 3) * 384 + 320) = one; }
    if (hh || odd) { q2u[0] = 0u; q2u[1] = 0u; }
    if (hh || !odd) { q2u[2] = 0u; q2u[3] = 0u; }
    asm volatile("" :: "v"(q1u), "v"(q2u));
    const bf16x8 q1 = __builtin_bit_cast(bf16x8, q1u);
    const int ka1 = lane * 16, ka2 = hh ? 1536 + 320 : 1024 + r32 * 16, va = 1536 + hh * 384 + (r32 <= 20 ? r32 : (r32 < 28 ? 20 : 16)) * 16;
    const f32x16 zero = {};
    bool safe = false;
    f32x16 o;
    {
      kv_dma(0, kvslot_off(0)); kv_dma(1, kvslot_off(1));
      vmwait_bar<3>();
      STAMP(6);
      bf16x8 k1 = lds_b128(lds + kvslot_off(0) + ka1), k2 = lds_b128(lds + kvslot_off(0) + ka2);
      f32x16 sa = mfma32(k2, __builtin_bit_cast(bf16x8, q2u), mfma32(k1, q1, zero)), sb;
      float mx = sa[0];
#pragma unroll
      for (int r = 1; r < 16; ++r) mx = __builtin_fmaxf(mx, sa[r]);
      { auto rr = __builtin_amdgcn_permlane32_swap(__builtin_bit_cast(unsigned, mx), __builtin_bit_cast(unsigned, mx), false, false);
        mx = __builtin_fmaxf(__uint_as_float(rr[0]), __uint_as_float(rr[1])); }
      if (hh) q2u[0] = pk2(-mx, 0.f);
      const bf16x8 q2 = __builtin_bit_cast(bf16x8, q2u);
      sa = mfma32(k2, q2, mfma32(k1, q1, zero));
      o = zero;
      int nxt = kvslot_off(1), fre = kvslot_off(2), cur = kvslot_off(0);
      lptr ck1 = lds + cur + ka1, ck2 = lds + cur + ka2, cv = lds + cur + va;
      k1 = lds_b128(ck1 + KVT); k2 = lds_b128(ck2 + KVT);
      bf16x8 v0 = lds_b128(cv), v1 = lds_b128(cv + 768);
      f32x8 plo, phi = {};
#define PIN(x) asm volatile("" : "+v"(x))
#define EXP4(P, PB, S, SB) do { P[PB] = __builtin_amdgcn_exp2f(S[SB]); P[PB + 1] = __builtin_amdgcn_exp2f(S[SB + 1]); P[PB + 2] = __builtin_amdgcn_exp2f(S[SB + 2]); P[PB + 3] = __builtin_amdgcn_exp2f(S[SB + 3]); PIN(P); SBAR(); } while (0)
#define PV(V, P) o = __builtin_amdgcn_smfmac_f32_32x32x32_bf16(V, __builtin_bit_cast(bf16x16, P), o, 0xdddd, 0, 0)
#define TILE(SC, SN, K1P, K2P, V1P, V0P) do { \
        SN = mfma32(k1, q1, zero); SBAR(); k1 = lds_b128(K1P); SBAR(); EXP4(plo, 0, SC, 0); \
        PV(v1, phi); SBAR(); v1 = lds_b128(V1P); SBAR(); EXP4(plo, 4, SC, 4); \
        SN = mfma32(k2, q2, SN); SBAR(); k2 = lds_b128(K2P); SBAR(); EXP4(phi, 0, SC, 8); \
        PV(v0, plo); SBAR(); v0 = lds_b128(V0P); SBAR(); EXP4(phi, 4, SC, 12); } while (0)
      for (int ch = 0; ch < 8; ++ch) {
        lptr nk1 = lds + nxt + ka1, nk2 = lds + nxt + ka2, nv = lds + nxt + va;
        TILE(sa, sb, ck1 + 2 * KVT, ck2 + 2 * KVT, cv + 0 * KVT + 768, cv + 1 * KVT);
        TILE(sb, sa, ck1 + 3 * KVT, ck2 + 3 * KVT, cv + 1 * KVT + 768, cv + 2 * KVT);
        TILE(sa, sb, ck1 + 4 * KVT, ck2 + 4 * KVT, cv + 2 * KVT + 768, cv + 3 * KVT);
        TILE(sb, sa, ck1 + 5 * KVT, ck2 + 5 * KVT, cv + 3 * KVT + 768, cv + 4 * KVT);
        TILE(sa, sb, ck1 + 6 * KVT, ck2 + 6 * KVT, cv + 4 * KVT + 768, cv + 5 * KVT);
        TILE(sb, sa, ck1 + 7 * KVT, ck2 + 7 * KVT, cv + 5 * KVT + 768, cv + 6 * KVT);
        asm volatile("s_waitcnt vmcnt(0)\n\ts_barrier" ::: "memory");
        if (ch + 2 < 8) kv_dma(ch + 2, fre);
        TILE(sa, sb, nk1, nk2, cv + 6 * KVT + 768, cv + 7 * KVT);
        TILE(sb, sa, nk1 + KVT, nk2 + KVT, cv + 7 * KVT + 768, nv);
        ck1 = nk1; ck2 = nk2; cv = nv;
        const int t = cur; cur = nxt; nxt = fre; fre = t;
      }
      PV(v1, phi);
      float chk = 0.f;
#pragma unroll
      for (int r = 0; r < 13; ++r) chk += o[r] * 0.f;
      safe = chk == 0.f;
    }
    STAMP(7);
    (void)safe;
    const float l = hh ? o[8] : o[12], rl = 1.0f / l;
    const unsigned orec = WS_AH + (qb * 8 + wid) * AH_REC;
    st16(wsr, orec + (odd ? m5 + 3 : m5) * 512 + lane * 16, u32x4{pk2(o[0] * rl, o[1] * rl), pk2(o[2] * rl, o[3] * rl), pk2(o[4] * rl, o[5] * rl), pk2(o[6] * rl, o[7] * rl)});
    if (!hh) __builtin_amdgcn_raw_buffer_store_b64(u32x2{pk2(o[8] * rl, o[9] * rl), pk2(o[10] * rl, o[11] * rl)}, wsr, orec + (m5 + 2) * 512 + r32 * 16 + odd * 8, 0, 16);
    vmwait<0>();
    if (lane == 0) set_flag(p.ws, WS_FO + ((qb * 8 + wid) * 32 + head) * 4, tag);
    STAMP(8);
  }

  if constexpr (PH & 4) {
    const int rt = xcd * 8 + (j >> 2), cg4 = j & 3, kq = wid & 3, cls = wid >> 2;
    poll_flags(p.ws, lane < 8 ? WS_FH + ((rt >> 1) * 32 + 8 * kq + lane) * 4 : (lane < 18 ? WS_FW + (cg4 * 160 + 10 * kq + lane - 8) * 4 : WS_FW + (cg4 * 160 + 40 + 24 * kq + lane - 18) * 4), lane < 42, tag);
    STAMP(9);
    f32x16 acc[3] = {};
    const unsigned av = WS_AH + rt * AH_REC + lane * 16, wv = WS_WT + cg4 * 136 * 5120 + cls * 3072 + lane * 16;
    auto run = [&](int ks0, int n) {
      if (cls == 0) {
#pragma unroll 6
        for (int i = 0; i < n; ++i) { const int ks = ks0 + i;
          const bf16x8 a = __builtin_bit_cast(bf16x8, ld16(wsr, av, ks * 1024));
#pragma unroll
          for (int t = 0; t < 3; ++t) acc[t] = mfma32(__builtin_bit_cast(bf16x8, ld16(wsr, wv + t * 1024, ks * 5120)), a, acc[t]); }
      } else {
#pragma unroll 6
        for (int i = 0; i < n; ++i) { const int ks = ks0 + i;
          const bf16x8 a = __builtin_bit_cast(bf16x8, ld16(wsr, av, ks * 1024));
#pragma unroll
          for (int t = 0; t < 2; ++t) acc[t] = mfma32(__builtin_bit_cast(bf16x8, ld16(wsr, wv + t * 1024, ks * 5120)), a, acc[t]); }
      }
    };
    run(40 + 24 * kq, 24);
    STAMP(10);
    poll_flags(p.ws, WS_FO + (rt * 32 + 8 * kq + (lane & 7)) * 4, lane < 8, tag);
    STAMP(11);
    run(10 * kq, 10);
    STAMP(12);
    bar_only();
#pragma unroll
    for (int t = 0; t < 3; ++t) if (cls == 0 || t < 2) {
#pragma unroll
      for (int i = 0; i < 4; ++i) *(LAS f32x4*)(lds + kq * 20480 + ((cls * 3 + t) * 4 + i) * 1024 + lane * 16) = f32x4{acc[t][4 * i], acc[t][4 * i + 1], acc[t][4 * i + 2], acc[t][4 * i + 3]};
    }
    bar_only();
    for (int it = wid; it < 10; it += 8) {
      const int t = it >> 1, uu = it & 1;
      f32x4 s0 = {}, s1 = {};
#pragma unroll
      for (int k = 0; k < 4; ++k) { s0 += lds_f4(lds + k * 20480 + (t * 4 + 2 * uu) * 1024 + lane * 16); s1 += lds_f4(lds + k * 20480 + (t * 4 + 2 * uu + 1) * 1024 + lane * 16); }
      const size_t go = (size_t)(rt * 32 + r32) * 1280 + (cg4 * 160 + t * 32 + uu * 16 + hh * 8) * 2;
      const u32x4 xv = *(const GAS u32x4*)(p.x + go);
      float y[8];
#pragma unroll
      for (int e = 0; e < 4; ++e) { y[e] = s0[e]; y[4 + e] = s1[e]; }
#pragma unroll
      for (int e = 0; e < 4; ++e) { y[2 * e] += __builtin_bit_cast(float, xv[e] << 16); y[2 * e + 1] += __builtin_bit_cast(float, xv[e] & 0xffff0000u); }
      *(GAS u32x4*)(p.out + go) = u32x4{pk2(y[0], y[1]), pk2(y[2], y[3]), pk2(y[4], y[5]), pk2(y[6], y[7])};
    }
    if (b == 0 && tid == 0) set_flag(p.ws, WS_G, tag);
    STAMP(13);
  }
  asm volatile(".Lcode_z%c0:" :: "n"(PH));
}

extern "C" void kernel_launch(void* const* d_in, const int* in_sizes, int n_in, void* d_out, int out_size, void* d_ws, size_t ws_size, hipStream_t stream) {
  Params p;
  p.x = (const char*)d_in[0]; p.wqkv = (const char*)d_in[1]; p.wo = (const char*)d_in[2];
  p.wg = (const char*)d_in[4]; p.wu = (const char*)d_in[5]; p.wd = (const char*)d_in[6];
  p.out = (char*)d_out; p.ws = (char*)d_ws;
  (void)hipFuncSetAttribute((const void*)fused<7>, hipFuncAttributeMaxDynamicSharedMemorySize, LDS_BYTES);
  hipLaunchKernelGGL(fused<7>, dim3(256), dim3(512), LDS_BYTES, stream, p);
}
```

```cpp
#include <hip/hip_runtime.h>
#include <stdint.h>

#define STAMPS 0
#define TRACE 0
#define SPAN 0
#define ATR 0
#define PROBE -1
#define C1IN0 1
#define PB 0
#define POLL_BITS "sc1"
#define ROT 2

typedef __attribute__((ext_vector_type(8))) __bf16 bf16x8;
typedef __attribute__((ext_vector_type(16))) __bf16 bf16x16;
typedef __attribute__((ext_vector_type(16))) float f32x16;
typedef __attribute__((ext_vector_type(8))) float f32x8;
typedef __attribute__((ext_vector_type(4))) float f32x4;
typedef __attribute__((ext_vector_type(4))) unsigned u32x4;
typedef __attribute__((ext_vector_type(2))) unsigned u32x2;
typedef __attribute__((ext_vector_type(16))) unsigned u32x16;
typedef __attribute__((ext_vector_type(4))) short s16x4;
typedef __attribute__((ext_vector_type(2))) __bf16 bf16x2;
typedef __attribute__((ext_vector_type(2))) float f32x2;
#define LAS __attribute__((address_space(3)))
#define GAS __attribute__((address_space(1)))
typedef LAS char* lptr;
#define DI __device__ __forceinline__
#define SBAR() __builtin_amdgcn_sched_barrier(0)
#define PIN(x) asm volatile("" : "+v"(x))
#define RLX_AGENT __ATOMIC_RELAXED, __HIP_MEMORY_SCOPE_AGENT
#define RLX_WG __ATOMIC_RELAXED, __HIP_MEMORY_SCOPE_WORKGROUP

constexpr unsigned WS_G = 0;
constexpr unsigned WS_F1 = 802816, F1_STR = 4352;
constexpr unsigned WS_F2 = 8192;
constexpr unsigned WS_ST = 24576;
constexpr unsigned WS_HINT = 655360, HINT_STR = 4352;
constexpr unsigned WS_STAMP = 65536;
constexpr unsigned WS_CS = 1u << 20;
constexpr unsigned WS_QKV = 2u << 20;
constexpr unsigned QKV_REC = 122880;
constexpr unsigned WS_AH = WS_QKV + 64 * QKV_REC;
constexpr unsigned AH_REC = 272 * 512;
constexpr unsigned WS_WT = WS_AH + 64 * AH_REC;
constexpr unsigned WS_END = WS_WT + 8 * 68 * 5120;

constexpr int L_X = 0, XSLOT = 16384;
constexpr int L_WQ = 65536, WQSLOT = 8192;
constexpr int L_WG = 98304, WGSLOT = 12352, WG_U = 6176;
constexpr int L_MU = 160064, L_RS = L_MU + 512, L_CSQ = L_RS + 512, L_CSG = L_CSQ + 1024;
constexpr int L_JUNK = L_CSG, L_TAG = L_CSG + 1536;
constexpr int LDS_BYTES = 163840;
constexpr int KVT = 3072;
constexpr int kvs(int c) { return 16384 + ((c + 1) % 6) * 8 * KVT; }
constexpr int L_QST = 0, QSTR = 3328, L_TAB = 4 * QSTR, L_OX = kvs(4);

constexpr unsigned SPIN_CAP = 1u << 18;

struct Params { const char* x; const char* wqkv; const char* wo; const char* wg; const char* wu; const char* wd; char* out; char* ws; };

DI f32x16 mfma32(bf16x8 a, bf16x8 b, f32x16 c) { return __builtin_amdgcn_mfma_f32_32x32x16_bf16(a, b, c, 0, 0, 0); }
DI f32x16 mfma8(s16x4 a, s16x4 b, f32x16 c) { return __builtin_amdgcn_mfma_f32_32x32x8bf16_1k(a, b, c, 0, 0, 0); }
DI bf16x8 lds_b128(lptr p) { return *(const LAS bf16x8*)p; }
DI f32x4 lds_f4(lptr p) { return *(const LAS f32x4*)p; }
DI u32x4 lds_u4(lptr p) { return *(const LAS u32x4*)p; }
DI bf16x8 lds_tr(lptr p, int second) {
  s16x4 lo = __builtin_amdgcn_ds_read_tr16_b64_v4i16((LAS s16x4*)p);
  s16x4 hi = __builtin_amdgcn_ds_read_tr16_b64_v4i16((LAS s16x4*)(p + second));
  return __builtin_bit_cast(bf16x8, __builtin_shufflevector(lo, hi, 0, 1, 2, 3, 4, 5, 6, 7));
}
DI unsigned pk2(float a, float b) { f32x2 v = {a, b}; return __builtin_bit_cast(unsigned, __builtin_convertvector(v, bf16x2)); }
DI u32x4 pk8(const float (&y)[8]) { return u32x4{pk2(y[0], y[1]), pk2(y[2], y[3]), pk2(y[4], y[5]), pk2(y[6], y[7])}; }
DI float bf_round(float a) { return (float)(__bf16)a; }
DI void dma16(unsigned voff, const char* sbase, unsigned lds_dst) {
  asm volatile("s_mov_b32 m0, %2\n\ts_nop 0\n\tglobal_load_lds_dwordx4 %0, %1" :: "v"(voff), "s"(sbase), "s"(lds_dst) : "memory");
}
DI void dma16_sc1(unsigned voff, const char* sbase, unsigned lds_dst) {
  asm volatile("s_mov_b32 m0, %2\n\ts_nop 0\n\tglobal_load_lds_dwordx4 %0, %1 sc1" :: "v"(voff), "s"(sbase), "s"(lds_dst) : "memory");
}
template <int N> DI void vmwait() { asm volatile("s_waitcnt vmcnt(%0)" :: "n"(N) : "memory"); }
template <int N> DI void vmwait_bar() { asm volatile("s_waitcnt vmcnt(%0) lgkmcnt(0)\n\ts_barrier" :: "n"(N) : "memory"); }
DI void bar_only() { asm volatile("s_waitcnt lgkmcnt(0)\n\ts_barrier" ::: "memory"); }
DI void st16(__amdgpu_buffer_rsrc_t r, unsigned off, u32x4 v) { __builtin_amdgcn_raw_buffer_store_b128(v, r, off, 0, 16); }
DI u32x4 ld16(__amdgpu_buffer_rsrc_t r, unsigned off, unsigned soff) { return __builtin_amdgcn_raw_buffer_load_b128(r, off, soff, 16); }
DI bf16x8 ldf(__amdgpu_buffer_rsrc_t r, unsigned off, unsigned soff) { return __builtin_bit_cast(bf16x8, ld16(r, off, soff)); }
DI void set_flag(char* ws, unsigned off, unsigned tag) { __hip_atomic_store((GAS unsigned*)(ws + off), tag, RLX_AGENT); }
DI void poll_flags(char* ws, unsigned off, bool active, unsigned tag) {
#pragma unroll 1
  for (unsigned spins = 0; spins < SPIN_CAP; ++spins) {
    unsigned v = tag;
    if (active) v = __hip_atomic_load((GAS unsigned*)(ws + off), RLX_AGENT);
    if (__all(v == tag)) break;
    __builtin_amdgcn_s_sleep(2);
  }
  asm volatile("" ::: "memory");
}
template <int CTRL> DI float dpp(float v) { return __builtin_bit_cast(float, __builtin_amdgcn_update_dpp(0, __builtin_bit_cast(int, v), CTRL, 0xf, 0xf, true)); }
DI float half_sum(float v) {
  v += dpp<0xB1>(v); v += dpp<0x4E>(v); v += dpp<0x141>(v); v += dpp<0x140>(v);
  auto r = __builtin_amdgcn_permlane16_swap(__float_as_uint(v), __float_as_uint(v), false, false);
  return __uint_as_float(r[0]) + __uint_as_float(r[1]);
}
#define STAMP(i) do { if (PROBE >= 0 && ((i) == 0 || ((i) == PROBE && (i) >= 5))) asm volatile("s_memrealtime s[100:101]\n\ts_waitcnt lgkmcnt(0)" ::: "s100", "s101", "memory"); } while (0)

DI u32x4 make_srd(const char* base, unsigned bytes) { const unsigned long long a = (unsigned long long)base; return u32x4{(unsigned)a, (unsigned)(a >> 32) & 0xffffu, bytes, 0x00020000u}; }
template <int LOFF, int GOFF> DI void dma_b(unsigned voff, u32x4 srd, unsigned lbase, unsigned gbase) {
  unsigned t;
  asm volatile("s_add_u32 m0, %3, %5\n\ts_add_u32 %0, %4, %6\n\tbuffer_load_dwordx4 %1, %2, %0 offen lds"
               : "=&s"(t) : "v"(voff), "s"(srd), "s"(lbase), "s"(gbase), "n"(LOFF), "n"(GOFF) : "memory", "scc");
}
DI f32x4 cs_two(f32x4 acc, bf16x8 f0, bf16x8 f1, bf16x8 sel, int idx) {
  return __builtin_amdgcn_smfmac_f32_16x16x64_bf16(sel, __builtin_shufflevector(f0, f1, 0, 1, 2, 3, 4, 5, 6, 7, 8, 9, 10, 11, 12, 13, 14, 15), acc, idx, 0, 0);
}
DI u32x2 split3(float v) {
  const float hi = bf_round(v), r1 = v - hi, mid = bf_round(r1);
  return u32x2{pk2(hi, mid), pk2(r1 - mid, 0.f)};
}
#define P1_ASM \
  "s_cmp_lg_u32 s94, 0\n" \
  "s_cbranch_scc1 7f\n" \
  "s_waitcnt lgkmcnt(0)\n" \
  "s_barrier\n" \
  "ds_read_b64_tr_b16 v[232:233], v4 offset:0\n" \
  "ds_read_b64_tr_b16 v[234:235], v4 offset:1024\n" \
  "ds_read_b128 v[208:211], v0 offset:0\n" \
  "ds_read_b128 v[212:215], v0 offset:4096\n" \
  "ds_read_b64_tr_b16 v[236:237], v5 offset:0\n" \
  "ds_read_b64_tr_b16 v[238:239], v5 offset:1024\n" \
  "ds_read_b64_tr_b16 v[240:241], v6 offset:24704\n" \
  "ds_read_b64_tr_b16 v[242:243], v6 offset:25472\n" \
  "ds_read_b64_tr_b16 v[244:245], v6 offset:24736\n" \
  "ds_read_b64_tr_b16 v[246:247], v6 offset:25504\n" \
  "ds_read_b64_tr_b16 v[248:249], v6 offset:24768\n" \
  "ds_read_b64_tr_b16 v[250:251], v6 offset:25536\n" \
  "s_waitcnt lgkmcnt(9)\n" \
  "v_mfma_f32_32x32x16_bf16 v[112:127], v[232:235], v[208:211], 0\n" \
  "ds_read_b128 v[216:219], v1 offset:0\n" \
  "s_waitcnt lgkmcnt(9)\n" \
  "v_mfma_f32_32x32x16_bf16 v[144:159], v[232:235], v[212:215], 0\n" \
  "ds_read_b64_tr_b16 v[232:233], v4 offset:4096\n" \
  "ds_read_b64_tr_b16 v[234:235], v4 offset:5120\n" \
  "s_waitcnt lgkmcnt(9)\n" \
  "v_mfma_f32_32x32x16_bf16 v[128:143], v[236:239], v[208:211], 0\n" \
  "ds_read_b128 v[220:223], v1 offset:4096\n" \
  "v_mfma_f32_32x32x16_bf16 v[160:175], v[236:239], v[212:215], 0\n" \
  "ds_read_b64_tr_b16 v[236:237], v5 offset:4096\n" \
  "ds_read_b64_tr_b16 v[238:239], v5 offset:5120\n" \
  "s_waitcnt lgkmcnt(10)\n" \
  "v_mfma_f32_32x32x16_bf16 v[16:31], v[240:243], v[208:211], 0\n" \
  "v_mfma_f32_32x32x16_bf16 v[64:79], v[240:243], v[212:215], 0\n" \
  "ds_read_b64_tr_b16 v[240:241], v6 offset:27776\n" \
  "ds_read_b64_tr_b16 v[242:243], v6 offset:28544\n" \
  "s_waitcnt lgkmcnt(10)\n" \
  "v_mfma_f32_32x32x16_bf16 v[32:47], v[244:247], v[208:211], 0\n" \
  "v_mfma_f32_32x32x16_bf16 v[80:95], v[244:247], v[212:215], 0\n" \
  "ds_read_b64_tr_b16 v[244:245], v6 offset:27808\n" \
  "ds_read_b64_tr_b16 v[246:247], v6 offset:28576\n" \
  "s_waitcnt lgkmcnt(10)\n" \
  "v_mfma_f32_32x32x16_bf16 v[48:63], v[248:251], v[208:211], 0\n" \
  "v_mfma_f32_32x32x16_bf16 v[96:111], v[248:251], v[212:215], 0\n" \
  "ds_read_b64_tr_b16 v[248:249], v6 offset:27840\n" \
  "ds_read_b64_tr_b16 v[250:251], v6 offset:28608\n" \
  "s_waitcnt lgkmcnt(9)\n" \
  "v_mfma_f32_32x32x16_bf16 v[112:127], v[232:235], v[216:219], v[112:127]\n" \
  "s_waitcnt lgkmcnt(8)\n" \
  "v_mfma_f32_32x32x16_bf16 v[144:159], v[232:235], v[220:223], v[144:159]\n" \
  "s_waitcnt lgkmcnt(6)\n" \
  "v_mfma_f32_32x32x16_bf16 v[128:143], v[236:239], v[216:219], v[128:143]\n" \
  "v_mfma_f32_32x32x16_bf16 v[160:175], v[236:239], v[220:223], v[160:175]\n" \
  "s_waitcnt lgkmcnt(4)\n" \
  "v_mfma_f32_32x32x16_bf16 v[16:31], v[240:243], v[216:219], v[16:31]\n" \
  "s_waitcnt lgkmcnt(0)\n" \
  "s_barrier\n" \
  "ds_read_b64_tr_b16 v[232:233], v4 offset:8192\n" \
  "ds_read_b64_tr_b16 v[234:235], v4 offset:9216\n" \
  "v_mfma_f32_32x32x16_bf16 v[64:79], v[240:243], v[220:223], v[64:79]\n" \
  "ds_read_b128 v[208:211], v2 offset:0\n" \
  "ds_read_b128 v[212:215], v2 offset:4096\n" \
  "v_mfma_f32_32x32x16_bf16 v[32:47], v[244:247], v[216:219], v[32:47]\n" \
  "ds_read_b64_tr_b16 v[236:237], v5 offset:8192\n" \
  "ds_read_b64_tr_b16 v[238:239], v5 offset:9216\n" \
  "v_mfma_f32_32x32x16_bf16 v[80:95], v[244:247], v[220:223], v[80:95]\n" \
  "ds_read_b64_tr_b16 v[240:241], v6 offset:37056\n" \
  "ds_read_b64_tr_b16 v[242:243], v6 offset:37824\n" \
  "v_mfma_f32_32x32x16_bf16 v[48:63], v[248:251], v[216:219], v[48:63]\n" \
  "ds_read_b64_tr_b16 v[244:245], v6 offset:37088\n" \
  "ds_read_b64_tr_b16 v[246:247], v6 offset:37856\n" \
  "v_mfma_f32_32x32x16_bf16 v[96:111], v[248:251], v[220:223], v[96:111]\n" \
  "ds_read_b64_tr_b16 v[248:249], v6 offset:37120\n" \
  "ds_read_b64_tr_b16 v[250:251], v6 offset:37888\n" \
  "s_waitcnt lgkmcnt(9)\n" \
  "v_mfma_f32_32x32x16_bf16 v[112:127], v[232:235], v[208:211], v[112:127]\n" \
  "ds_read_b128 v[216:219], v3 offset:0\n" \
  "s_waitcnt lgkmcnt(9)\n" \
  "v_mfma_f32_32x32x16_bf16 v[144:159], v[232:235], v[212:215], v[144:159]\n" \
  "ds_read_b64_tr_b16 v[232:233], v4 offset:12288\n" \
  "ds_read_b64_tr_b16 v[234:235], v4 offset:13312\n" \
  "s_waitcnt lgkmcnt(9)\n" \
  "v_mfma_f32_32x32x16_bf16 v[128:143], v[236:239], v[208:211], v[128:143]\n" \
  "ds_read_b128 v[220:223], v3 offset:4096\n" \
  "v_mfma_f32_32x32x16_bf16 v[160:175], v[236:239], v[212:215], v[160:175]\n" \
  "ds_read_b64_tr_b16 v[236:237], v5 offset:12288\n" \
  "ds_read_b64_tr_b16 v[238:239], v5 offset:13312\n" \
  "s_waitcnt lgkmcnt(10)\n" \
  "v_mfma_f32_32x32x16_bf16 v[16:31], v[240:243], v[208:211], v[16:31]\n" \
  "v_mfma_f32_32x32x16_bf16 v[64:79], v[240:243], v[212:215], v[64:79]\n" \
  "ds_read_b64_tr_b16 v[240:241], v6 offset:40128\n" \
  "ds_read_b64_tr_b16 v[242:243], v6 offset:40896\n" \
  "s_waitcnt lgkmcnt(10)\n" \
  "v_mfma_f32_32x32x16_bf16 v[32:47], v[244:247], v[208:211], v[32:47]\n" \
  "v_mfma_f32_32x32x16_bf16 v[80:95], v[244:247], v[212:215], v[80:95]\n" \
  "ds_read_b64_tr_b16 v[244:245], v6 offset:40160\n" \
  "ds_read_b64_tr_b16 v[246:247], v6 offset:40928\n" \
  "s_waitcnt lgkmcnt(10)\n" \
  "v_mfma_f32_32x32x16_bf16 v[48:63], v[248:251], v[208:211], v[48:63]\n" \
  "v_mfma_f32_32x32x16_bf16 v[96:111], v[248:251], v[212:215], v[96:111]\n" \
  "ds_read_b64_tr_b16 v[248:249], v6 offset:40192\n" \
  "ds_read_b64_tr_b16 v[250:251], v6 offset:40960\n" \
  "s_waitcnt lgkmcnt(9)\n" \
  "v_mfma_f32_32x32x16_bf16 v[112:127], v[232:235], v[216:219], v[112:127]\n" \
  "s_waitcnt lgkmcnt(8)\n" \
  "v_mfma_f32_32x32x16_bf16 v[144:159], v[232:235], v[220:223], v[144:159]\n" \
  "s_waitcnt lgkmcnt(6)\n" \
  "v_mfma_f32_32x32x16_bf16 v[128:143], v[236:239], v[216:219], v[128:143]\n" \
  "v_mfma_f32_32x32x16_bf16 v[160:175], v[236:239], v[220:223], v[160:175]\n" \
  "s_waitcnt lgkmcnt(4)\n" \
  "v_mfma_f32_32x32x16_bf16 v[16:31], v[240:243], v[216:219], v[16:31]\n" \
  "s_waitcnt lgkmcnt(0)\n" \
  "s_barrier\n" \
  "ds_read_b64_tr_b16 v[232:233], v4 offset:16384\n" \
  "ds_read_b64_tr_b16 v[234:235], v4 offset:17408\n" \
  "v_mfma_f32_32x32x16_bf16 v[64:79], v[240:243], v[220:223], v[64:79]\n" \
  "ds_read_b128 v[208:211], v0 offset:16384\n" \
  "ds_read_b128 v[212:215], v0 offset:20480\n" \
  "v_mfma_f32_32x32x16_bf16 v[32:47], v[244:247], v[216:219], v[32:47]\n" \
  "ds_read_b64_tr_b16 v[236:237], v5 offset:16384\n" \
  "ds_read_b64_tr_b16 v[238:239], v5 offset:17408\n" \
  "v_mfma_f32_32x32x16_bf16 v[80:95], v[244:247], v[220:223], v[80:95]\n" \
  "ds_read_b64_tr_b16 v[240:241], v6 offset:49408\n" \
  "ds_read_b64_tr_b16 v[242:243], v6 offset:50176\n" \
  "v_mfma_f32_32x32x16_bf16 v[48:63], v[248:251], v[216:219], v[48:63]\n" \
  "ds_read_b64_tr_b16 v[244:245], v6 offset:49440\n" \
  "ds_read_b64_tr_b16 v[246:247], v6 offset:50208\n" \
  "v_mfma_f32_32x32x16_bf16 v[96:111], v[248:251], v[220:223], v[96:111]\n" \
  "ds_read_b64_tr_b16 v[248:249], v6 offset:49472\n" \
  "ds_read_b64_tr_b16 v[250:251], v6 offset:50240\n" \
  "s_waitcnt lgkmcnt(9)\n" \
  "v_mfma_f32_32x32x16_bf16 v[112:127], v[232:235], v[208:211], v[112:127]\n" \
  "ds_read_b128 v[216:219], v1 offset:16384\n" \
  "s_waitcnt lgkmcnt(9)\n" \
  "v_mfma_f32_32x32x16_bf16 v[144:159], v[232:235], v[212:215], v[144:159]\n" \
  "ds_read_b64_tr_b16 v[232:233], v4 offset:20480\n" \
  "ds_read_b64_tr_b16 v[234:235], v4 offset:21504\n" \
  "s_waitcnt lgkmcnt(9)\n" \
  "v_mfma_f32_32x32x16_bf16 v[128:143], v[236:239], v[208:211], v[128:143]\n" \
  "ds_read_b128 v[220:223], v1 offset:20480\n" \
  "v_mfma_f32_32x32x16_bf16 v[160:175], v[236:239], v[212:215], v[160:175]\n" \
  "ds_read_b64_tr_b16 v[236:237], v5 offset:20480\n" \
  "ds_read_b64_tr_b16 v[238:239], v5 offset:21504\n" \
  "s_waitcnt lgkmcnt(10)\n" \
  "v_mfma_f32_32x32x16_bf16 v[16:31], v[240:243], v[208:211], v[16:31]\n" \
  "v_mfma_f32_32x32x16_bf16 v[64:79], v[240:243], v[212:215], v[64:79]\n" \
  "ds_read_b64_tr_b16 v[240:241], v6 offset:52480\n" \
  "ds_read_b64_tr_b16 v[242:243], v6 offset:53248\n" \
  "s_waitcnt lgkmcnt(10)\n" \
  "v_mfma_f32_32x32x16_bf16 v[32:47], v[244:247], v[208:211], v[32:47]\n" \
  "v_mfma_f32_32x32x16_bf16 v[80:95], v[244:247], v[212:215], v[80:95]\n" \
  "ds_read_b64_tr_b16 v[244:245], v6 offset:52512\n" \
  "ds_read_b64_tr_b16 v[246:247], v6 offset:53280\n" \
  "s_waitcnt lgkmcnt(10)\n" \
  "v_mfma_f32_32x32x16_bf16 v[48:63], v[248:251], v[208:211], v[48:63]\n" \
  "v_mfma_f32_32x32x16_bf16 v[96:111], v[248:251], v[212:215], v[96:111]\n" \
  "ds_read_b64_tr_b16 v[248:249], v6 offset:52544\n" \
  "ds_read_b64_tr_b16 v[250:251], v6 offset:53312\n" \
  "s_waitcnt lgkmcnt(9)\n" \
  "v_mfma_f32_32x32x16_bf16 v[112:127], v[232:235], v[216:219], v[112:127]\n" \
  "s_waitcnt lgkmcnt(8)\n" \
  "v_mfma_f32_32x32x16_bf16 v[144:159], v[232:235], v[220:223], v[144:159]\n" \
  "s_waitcnt lgkmcnt(6)\n" \
  "v_mfma_f32_32x32x16_bf16 v[128:143], v[236:239], v[216:219], v[128:143]\n" \
  "v_mfma_f32_32x32x16_bf16 v[160:175], v[236:239], v[220:223], v[160:175]\n" \
  "s_waitcnt lgkmcnt(4)\n" \
  "v_mfma_f32_32x32x16_bf16 v[16:31], v[240:243], v[216:219], v[16:31]\n" \
  "s_waitcnt lgkmcnt(0)\n" \
  "s_barrier\n" \
  "ds_read_b64_tr_b16 v[232:233], v4 offset:24576\n" \
  "ds_read_b64_tr_b16 v[234:235], v4 offset:25600\n" \
  "v_mfma_f32_32x32x16_bf16 v[64:79], v[240:243], v[220:223], v[64:79]\n" \
  "ds_read_b128 v[208:211], v2 offset:16384\n" \
  "ds_read_b128 v[212:215], v2 offset:20480\n" \
  "v_mfma_f32_32x32x16_bf16 v[32:47], v[244:247], v[216:219], v[32:47]\n" \
  "ds_read_b64_tr_b16 v[236:237], v5 offset:24576\n" \
  "ds_read_b64_tr_b16 v[238:239], v5 offset:25600\n" \
  "v_mfma_f32_32x32x16_bf16 v[80:95], v[244:247], v[220:223], v[80:95]\n" \
  "ds_read_b64_tr_b16 v[240:241], v6 offset:0\n" \
  "ds_read_b64_tr_b16 v[242:243], v6 offset:768\n" \
  "v_mfma_f32_32x32x16_bf16 v[48:63], v[248:251], v[216:219], v[48:63]\n" \
  "ds_read_b64_tr_b16 v[244:245], v6 offset:32\n" \
  "ds_read_b64_tr_b16 v[246:247], v6 offset:800\n" \
  "v_mfma_f32_32x32x16_bf16 v[96:111], v[248:251], v[220:223], v[96:111]\n" \
  "ds_read_b64_tr_b16 v[248:249], v6 offset:64\n" \
  "ds_read_b64_tr_b16 v[250:251], v6 offset:832\n" \
  "s_waitcnt lgkmcnt(9)\n" \
  "v_mfma_f32_32x32x16_bf16 v[112:127], v[232:235], v[208:211], v[112:127]\n" \
  "ds_read_b128 v[216:219], v3 offset:16384\n" \
  "s_waitcnt lgkmcnt(9)\n" \
  "v_mfma_f32_32x32x16_bf16 v[144:159], v[232:235], v[212:215], v[144:159]\n" \
  "ds_read_b64_tr_b16 v[232:233], v4 offset:28672\n" \
  "ds_read_b64_tr_b16 v[234:235], v4 offset:29696\n" \
  "s_waitcnt lgkmcnt(9)\n" \
  "v_mfma_f32_32x32x16_bf16 v[128:143], v[236:239], v[208:211], v[128:143]\n" \
  "ds_read_b128 v[220:223], v3 offset:20480\n" \
  "v_mfma_f32_32x32x16_bf16 v[160:175], v[236:239], v[212:215], v[160:175]\n" \
  "ds_read_b64_tr_b16 v[236:237], v5 offset:28672\n" \
  "ds_read_b64_tr_b16 v[238:239], v5 offset:29696\n" \
  "s_waitcnt lgkmcnt(10)\n" \
  "v_mfma_f32_32x32x16_bf16 v[16:31], v[240:243], v[208:211], v[16:31]\n" \
  "v_mfma_f32_32x32x16_bf16 v[64:79], v[240:243], v[212:215], v[64:79]\n" \
  "ds_read_b64_tr_b16 v[240:241], v6 offset:3072\n" \
  "ds_read_b64_tr_b16 v[242:243], v6 offset:3840\n" \
  "s_waitcnt lgkmcnt(10)\n" \
  "v_mfma_f32_32x32x16_bf16 v[32:47], v[244:247], v[208:211], v[32:47]\n" \
  "v_mfma_f32_32x32x16_bf16 v[80:95], v[244:247], v[212:215], v[80:95]\n" \
  "ds_read_b64_tr_b16 v[244:245], v6 offset:3104\n" \
  "ds_read_b64_tr_b16 v[246:247], v6 offset:3872\n" \
  "s_waitcnt lgkmcnt(10)\n" \
  "v_mfma_f32_32x32x16_bf16 v[48:63], v[248:251], v[208:211], v[48:63]\n" \
  "v_mfma_f32_32x32x16_bf16 v[96:111], v[248:251], v[212:215], v[96:111]\n" \
  "ds_read_b64_tr_b16 v[248:249], v6 offset:3136\n" \
  "ds_read_b64_tr_b16 v[250:251], v6 offset:3904\n" \
  "s_waitcnt lgkmcnt(9)\n" \
  "v_mfma_f32_32x32x16_bf16 v[112:127], v[232:235], v[216:219], v[112:127]\n" \
  "s_waitcnt lgkmcnt(8)\n" \
  "v_mfma_f32_32x32x16_bf16 v[144:159], v[232:235], v[220:223], v[144:159]\n" \
  "s_waitcnt lgkmcnt(6)\n" \
  "v_mfma_f32_32x32x16_bf16 v[128:143], v[236:239], v[216:219], v[128:143]\n" \
  "v_mfma_f32_32x32x16_bf16 v[160:175], v[236:239], v[220:223], v[160:175]\n" \
  "s_waitcnt lgkmcnt(4)\n" \
  "v_mfma_f32_32x32x16_bf16 v[16:31], v[240:243], v[216:219], v[16:31]\n" \
  "s_waitcnt lgkmcnt(0)\n" \
  "s_barrier\n" \
  "ds_read_b64_tr_b16 v[232:233], v4 offset:0\n" \
  "ds_read_b64_tr_b16 v[234:235], v4 offset:1024\n" \
  "v_mfma_f32_32x32x16_bf16 v[64:79], v[240:243], v[220:223], v[64:79]\n" \
  "ds_read_b128 v[208:211], v0 offset:32768\n" \
  "ds_read_b128 v[212:215], v0 offset:36864\n" \
  "v_mfma_f32_32x32x16_bf16 v[32:47], v[244:247], v[216:219], v[32:47]\n" \
  "ds_read_b64_tr_b16 v[236:237], v5 offset:0\n" \
  "ds_read_b64_tr_b16 v[238:239], v5 offset:1024\n" \
  "v_mfma_f32_32x32x16_bf16 v[80:95], v[244:247], v[220:223], v[80:95]\n" \
  "ds_read_b64_tr_b16 v[240:241], v6 offset:12352\n" \
  "ds_read_b64_tr_b16 v[242:243], v6 offset:13120\n" \
  "v_mfma_f32_32x32x16_bf16 v[48:63], v[248:251], v[216:219], v[48:63]\n" \
  "ds_read_b64_tr_b16 v[244:245], v6 offset:12384\n" \
  "ds_read_b64_tr_b16 v[246:247], v6 offset:13152\n" \
  "v_mfma_f32_32x32x16_bf16 v[96:111], v[248:251], v[220:223], v[96:111]\n" \
  "ds_read_b64_tr_b16 v[248:249], v6 offset:12416\n" \
  "ds_read_b64_tr_b16 v[250:251], v6 offset:13184\n" \
  "s_waitcnt lgkmcnt(9)\n" \
  "v_mfma_f32_32x32x16_bf16 v[112:127], v[232:235], v[208:211], v[112:127]\n" \
  "ds_read_b128 v[216:219], v1 offset:32768\n" \
  "s_waitcnt lgkmcnt(9)\n" \
  "v_mfma_f32_32x32x16_bf16 v[144:159], v[232:235], v[212:215], v[144:159]\n" \
  "ds_read_b64_tr_b16 v[232:233], v4 offset:4096\n" \
  "ds_read_b64_tr_b16 v[234:235], v4 offset:5120\n" \
  "s_waitcnt lgkmcnt(9)\n" \
  "v_mfma_f32_32x32x16_bf16 v[128:143], v[236:239], v[208:211], v[128:143]\n" \
  "ds_read_b128 v[220:223], v1 offset:36864\n" \
  "v_mfma_f32_32x32x16_bf16 v[160:175], v[236:239], v[212:215], v[160:175]\n" \
  "ds_read_b64_tr_b16 v[236:237], v5 offset:4096\n" \
  "ds_read_b64_tr_b16 v[238:239], v5 offset:5120\n" \
  "s_waitcnt lgkmcnt(10)\n" \
  "v_mfma_f32_32x32x16_bf16 v[16:31], v[240:243], v[208:211], v[16:31]\n" \
  "v_mfma_f32_32x32x16_bf16 v[64:79], v[240:243], v[212:215], v[64:79]\n" \
  "ds_read_b64_tr_b16 v[240:241], v6 offset:15424\n" \
  "ds_read_b64_tr_b16 v[242:243], v6 offset:16192\n" \
  "s_waitcnt lgkmcnt(10)\n" \
  "v_mfma_f32_32x32x16_bf16 v[32:47], v[244:247], v[208:211], v[32:47]\n" \
  "v_mfma_f32_32x32x16_bf16 v[80:95], v[244:247], v[212:215], v[80:95]\n" \
  "ds_read_b64_tr_b16 v[244:245], v6 offset:15456\n" \
  "ds_read_b64_tr_b16 v[246:247], v6 offset:16224\n" \
  "s_waitcnt lgkmcnt(10)\n" \
  "v_mfma_f32_32x32x16_bf16 v[48:63], v[248:251], v[208:211], v[48:63]\n" \
  "v_mfma_f32_32x32x16_bf16 v[96:111], v[248:251], v[212:215], v[96:111]\n" \
  "ds_read_b64_tr_b16 v[248:249], v6 offset:15488\n" \
  "ds_read_b64_tr_b16 v[250:251], v6 offset:16256\n" \
  "s_waitcnt lgkmcnt(9)\n" \
  "v_mfma_f32_32x32x16_bf16 v[112:127], v[232:235], v[216:219], v[112:127]\n" \
  "s_waitcnt lgkmcnt(8)\n" \
  "v_mfma_f32_32x32x16_bf16 v[144:159], v[232:235], v[220:223], v[144:159]\n" \
  "s_waitcnt lgkmcnt(6)\n" \
  "v_mfma_f32_32x32x16_bf16 v[128:143], v[236:239], v[216:219], v[128:143]\n" \
  "v_mfma_f32_32x32x16_bf16 v[160:175], v[236:239], v[220:223], v[160:175]\n" \
  "s_waitcnt lgkmcnt(4)\n" \
  "v_mfma_f32_32x32x16_bf16 v[16:31], v[240:243], v[216:219], v[16:31]\n" \
  "s_waitcnt lgkmcnt(0)\n" \
  "s_barrier\n" \
  "ds_read_b64_tr_b16 v[232:233], v4 offset:8192\n" \
  "ds_read_b64_tr_b16 v[234:235], v4 offset:9216\n" \
  "v_mfma_f32_32x32x16_bf16 v[64:79], v[240:243], v[220:223], v[64:79]\n" \
  "ds_read_b128 v[208:211], v2 offset:32768\n" \
  "ds_read_b128 v[212:215], v2 offset:36864\n" \
  "v_mfma_f32_32x32x16_bf16 v[32:47], v[244:247], v[216:219], v[32:47]\n" \
  "ds_read_b64_tr_b16 v[236:237], v5 offset:8192\n" \
  "ds_read_b64_tr_b16 v[238:239], v5 offset:9216\n" \
  "v_mfma_f32_32x32x16_bf16 v[80:95], v[244:247], v[220:223], v[80:95]\n" \
  "ds_read_b64_tr_b16 v[240:241], v6 offset:24704\n" \
  "ds_read_b64_tr_b16 v[242:243], v6 offset:25472\n" \
  "v_mfma_f32_32x32x16_bf16 v[48:63], v[248:251], v[216:219], v[48:63]\n" \
  "ds_read_b64_tr_b16 v[244:245], v6 offset:24736\n" \
  "ds_read_b64_tr_b16 v[246:247], v6 offset:25504\n" \
  "v_mfma_f32_32x32x16_bf16 v[96:111], v[248:251], v[220:223], v[96:111]\n" \
  "ds_read_b64_tr_b16 v[248:249], v6 offset:24768\n" \
  "ds_read_b64_tr_b16 v[250:251], v6 offset:25536\n" \
  "s_waitcnt lgkmcnt(9)\n" \
  "v_mfma_f32_32x32x16_bf16 v[112:127], v[232:235], v[208:211], v[112:127]\n" \
  "ds_read_b128 v[216:219], v3 offset:32768\n" \
  "s_waitcnt lgkmcnt(9)\n" \
  "v_mfma_f32_32x32x16_bf16 v[144:159], v[232:235], v[212:215], v[144:159]\n" \
  "ds_read_b64_tr_b16 v[232:233], v4 offset:12288\n" \
  "ds_read_b64_tr_b16 v[234:235], v4 offset:13312\n" \
  "s_waitcnt lgkmcnt(9)\n" \
  "v_mfma_f32_32x32x16_bf16 v[128:143], v[236:239], v[208:211], v[128:143]\n" \
  "ds_read_b128 v[220:223], v3 offset:36864\n" \
  "v_mfma_f32_32x32x16_bf16 v[160:175], v[236:239], v[212:215], v[160:175]\n" \
  "ds_read_b64_tr_b16 v[236:237], v5 offset:12288\n" \
  "ds_read_b64_tr_b16 v[238:239], v5 offset:13312\n" \
  "s_waitcnt lgkmcnt(10)\n" \
  "v_mfma_f32_32x32x16_bf16 v[16:31], v[240:243], v[208:211], v[16:31]\n" \
  "v_mfma_f32_32x32x16_bf16 v[64:79], v[240:243], v[212:215], v[64:79]\n" \
  "ds_read_b64_tr_b16 v[240:241], v6 offset:27776\n" \
  "ds_read_b64_tr_b16 v[242:243], v6 offset:28544\n" \
  "s_waitcnt lgkmcnt(10)\n" \
  "v_mfma_f32_32x32x16_bf16 v[32:47], v[244:247], v[208:211], v[32:47]\n" \
  "v_mfma_f32_32x32x16_bf16 v[80:95], v[244:247], v[212:215], v[80:95]\n" \
  "ds_read_b64_tr_b16 v[244:245], v6 offset:27808\n" \
  "ds_read_b64_tr_b16 v[246:247], v6 offset:28576\n" \
  "s_waitcnt lgkmcnt(10)\n" \
  "v_mfma_f32_32x32x16_bf16 v[48:63], v[248:251], v[208:211], v[48:63]\n" \
  "v_mfma_f32_32x32x16_bf16 v[96:111], v[248:251], v[212:215], v[96:111]\n" \
  "ds_read_b64_tr_b16 v[248:249], v6 offset:27840\n" \
  "ds_read_b64_tr_b16 v[250:251], v6 offset:28608\n" \
  "s_waitcnt lgkmcnt(9)\n" \
  "v_mfma_f32_32x32x16_bf16 v[112:127], v[232:235], v[216:219], v[112:127]\n" \
  "s_waitcnt lgkmcnt(8)\n" \
  "v_mfma_f32_32x32x16_bf16 v[144:159], v[232:235], v[220:223], v[144:159]\n" \
  "s_waitcnt lgkmcnt(6)\n" \
  "v_mfma_f32_32x32x16_bf16 v[128:143], v[236:239], v[216:219], v[128:143]\n" \
  "v_mfma_f32_32x32x16_bf16 v[160:175], v[236:239], v[220:223], v[160:175]\n" \
  "s_waitcnt lgkmcnt(4)\n" \
  "v_mfma_f32_32x32x16_bf16 v[16:31], v[240:243], v[216:219], v[16:31]\n" \
  "s_waitcnt lgkmcnt(0)\n" \
  "s_barrier\n" \
  "ds_read_b64_tr_b16 v[232:233], v4 offset:16384\n" \
  "ds_read_b64_tr_b16 v[234:235], v4 offset:17408\n" \
  "v_mfma_f32_32x32x16_bf16 v[64:79], v[240:243], v[220:223], v[64:79]\n" \
  "ds_read_b128 v[208:211], v0 offset:49152\n" \
  "ds_read_b128 v[212:215], v0 offset:53248\n" \
  "v_mfma_f32_32x32x16_bf16 v[32:47], v[244:247], v[216:219], v[32:47]\n" \
  "ds_read_b64_tr_b16 v[236:237], v5 offset:16384\n" \
  "ds_read_b64_tr_b16 v[238:239], v5 offset:17408\n" \
  "v_mfma_f32_32x32x16_bf16 v[80:95], v[244:247], v[220:223], v[80:95]\n" \
  "ds_read_b64_tr_b16 v[240:241], v6 offset:37056\n" \
  "ds_read_b64_tr_b16 v[242:243], v6 offset:37824\n" \
  "v_mfma_f32_32x32x16_bf16 v[48:63], v[248:251], v[216:219], v[48:63]\n" \
  "ds_read_b64_tr_b16 v[244:245], v6 offset:37088\n" \
  "ds_read_b64_tr_b16 v[246:247], v6 offset:37856\n" \
  "v_mfma_f32_32x32x16_bf16 v[96:111], v[248:251], v[220:223], v[96:111]\n" \
  "ds_read_b64_tr_b16 v[248:249], v6 offset:37120\n" \
  "ds_read_b64_tr_b16 v[250:251], v6 offset:37888\n" \
  "s_waitcnt lgkmcnt(9)\n" \
  "v_mfma_f32_32x32x16_bf16 v[112:127], v[232:235], v[208:211], v[112:127]\n" \
  "ds_read_b128 v[216:219], v1 offset:49152\n" \
  "s_waitcnt lgkmcnt(9)\n" \
  "v_mfma_f32_32x32x16_bf16 v[144:159], v[232:235], v[212:215], v[144:159]\n" \
  "ds_read_b64_tr_b16 v[232:233], v4 offset:20480\n" \
  "ds_read_b64_tr_b16 v[234:235], v4 offset:21504\n" \
  "s_waitcnt lgkmcnt(9)\n" \
  "v_mfma_f32_32x32x16_bf16 v[128:143], v[236:239], v[208:211], v[128:143]\n" \
  "ds_read_b128 v[220:223], v1 offset:53248\n" \
  "v_mfma_f32_32x32x16_bf16 v[160:175], v[236:239], v[212:215], v[160:175]\n" \
  "ds_read_b64_tr_b16 v[236:237], v5 offset:20480\n" \
  "ds_read_b64_tr_b16 v[238:239], v5 offset:21504\n" \
  "s_waitcnt lgkmcnt(10)\n" \
  "v_mfma_f32_32x32x16_bf16 v[16:31], v[240:243], v[208:211], v[16:31]\n" \
  "v_mfma_f32_32x32x16_bf16 v[64:79], v[240:243], v[212:215], v[64:79]\n" \
  "ds_read_b64_tr_b16 v[240:241], v6 offset:40128\n" \
  "ds_read_b64_tr_b16 v[242:243], v6 offset:40896\n" \
  "s_waitcnt lgkmcnt(10)\n" \
  "v_mfma_f32_32x32x16_bf16 v[32:47], v[244:247], v[208:211], v[32:47]\n" \
  "v_mfma_f32_32x32x16_bf16 v[80:95], v[244:247], v[212:215], v[80:95]\n" \
  "ds_read_b64_tr_b16 v[244:245], v6 offset:40160\n" \
  "ds_read_b64_tr_b16 v[246:247], v6 offset:40928\n" \
  "s_waitcnt lgkmcnt(10)\n" \
  "v_mfma_f32_32x32x16_bf16 v[48:63], v[248:251], v[208:211], v[48:63]\n" \
  "v_mfma_f32_32x32x16_bf16 v[96:111], v[248:251], v[212:215], v[96:111]\n" \
  "ds_read_b64_tr_b16 v[248:249], v6 offset:40192\n" \
  "ds_read_b64_tr_b16 v[250:251], v6 offset:40960\n" \
  "s_waitcnt lgkmcnt(9)\n" \
  "v_mfma_f32_32x32x16_bf16 v[112:127], v[232:235], v[216:219], v[112:127]\n" \
  "s_waitcnt lgkmcnt(8)\n" \
  "v_mfma_f32_32x32x16_bf16 v[144:159], v[232:235], v[220:223], v[144:159]\n" \
  "s_waitcnt lgkmcnt(6)\n" \
  "v_mfma_f32_32x32x16_bf16 v[128:143], v[236:239], v[216:219], v[128:143]\n" \
  "v_mfma_f32_32x32x16_bf16 v[160:175], v[236:239], v[220:223], v[160:175]\n" \
  "s_waitcnt lgkmcnt(4)\n" \
  "v_mfma_f32_32x32x16_bf16 v[16:31], v[240:243], v[216:219], v[16:31]\n" \
  "s_waitcnt lgkmcnt(0)\n" \
  "s_barrier\n" \
  "ds_read_b64_tr_b16 v[232:233], v4 offset:24576\n" \
  "ds_read_b64_tr_b16 v[234:235], v4 offset:25600\n" \
  "v_mfma_f32_32x32x16_bf16 v[64:79], v[240:243], v[220:223], v[64:79]\n" \
  "ds_read_b128 v[208:211], v2 offset:49152\n" \
  "ds_read_b128 v[212:215], v2 offset:53248\n" \
  "v_mfma_f32_32x32x16_bf16 v[32:47], v[244:247], v[216:219], v[32:47]\n" \
  "ds_read_b64_tr_b16 v[236:237], v5 offset:24576\n" \
  "ds_read_b64_tr_b16 v[238:239], v5 offset:25600\n" \
  "v_mfma_f32_32x32x16_bf16 v[80:95], v[244:247], v[220:223], v[80:95]\n" \
  "ds_read_b64_tr_b16 v[240:241], v6 offset:49408\n" \
  "ds_read_b64_tr_b16 v[242:243], v6 offset:50176\n" \
  "v_mfma_f32_32x32x16_bf16 v[48:63], v[248:251], v[216:219], v[48:63]\n" \
  "ds_read_b64_tr_b16 v[244:245], v6 offset:49440\n" \
  "ds_read_b64_tr_b16 v[246:247], v6 offset:50208\n" \
  "v_mfma_f32_32x32x16_bf16 v[96:111], v[248:251], v[220:223], v[96:111]\n" \
  "ds_read_b64_tr_b16 v[248:249], v6 offset:49472\n" \
  "ds_read_b64_tr_b16 v[250:251], v6 offset:50240\n" \
  "s_waitcnt lgkmcnt(9)\n" \
  "v_mfma_f32_32x32x16_bf16 v[112:127], v[232:235], v[208:211], v[112:127]\n" \
  "ds_read_b128 v[216:219], v3 offset:49152\n" \
  "s_waitcnt lgkmcnt(9)\n" \
  "v_mfma_f32_32x32x16_bf16 v[144:159], v[232:235], v[212:215], v[144:159]\n" \
  "ds_read_b64_tr_b16 v[232:233], v4 offset:28672\n" \
  "ds_read_b64_tr_b16 v[234:235], v4 offset:29696\n" \
  "s_waitcnt lgkmcnt(9)\n" \
  "v_mfma_f32_32x32x16_bf16 v[128:143], v[236:239], v[208:211], v[128:143]\n" \
  "ds_read_b128 v[220:223], v3 offset:53248\n" \
  "v_mfma_f32_32x32x16_bf16 v[160:175], v[236:239], v[212:215], v[160:175]\n" \
  "ds_read_b64_tr_b16 v[236:237], v5 offset:28672\n" \
  "ds_read_b64_tr_b16 v[238:239], v5 offset:29696\n" \
  "s_waitcnt lgkmcnt(10)\n" \
  "v_mfma_f32_32x32x16_bf16 v[16:31], v[240:243], v[208:211], v[16:31]\n" \
  "v_mfma_f32_32x32x16_bf16 v[64:79], v[240:243], v[212:215], v[64:79]\n" \
  "ds_read_b64_tr_b16 v[240:241], v6 offset:52480\n" \
  "ds_read_b64_tr_b16 v[242:243], v6 offset:53248\n" \
  "s_waitcnt lgkmcnt(10)\n" \
  "v_mfma_f32_32x32x16_bf16 v[32:47], v[244:247], v[208:211], v[32:47]\n" \
  "v_mfma_f32_32x32x16_bf16 v[80:95], v[244:247], v[212:215], v[80:95]\n" \
  "ds_read_b64_tr_b16 v[244:245], v6 offset:52512\n" \
  "ds_read_b64_tr_b16 v[246:247], v6 offset:53280\n" \
  "s_waitcnt lgkmcnt(10)\n" \
  "v_mfma_f32_32x32x16_bf16 v[48:63], v[248:251], v[208:211], v[48:63]\n" \
  "v_mfma_f32_32x32x16_bf16 v[96:111], v[248:251], v[212:215], v[96:111]\n" \
  "ds_read_b64_tr_b16 v[248:249], v6 offset:52544\n" \
  "ds_read_b64_tr_b16 v[250:251], v6 offset:53312\n" \
  "s_waitcnt lgkmcnt(9)\n" \
  "v_mfma_f32_32x32x16_bf16 v[112:127], v[232:235], v[216:219], v[112:127]\n" \
  "s_waitcnt lgkmcnt(8)\n" \
  "v_mfma_f32_32x32x16_bf16 v[144:159], v[232:235], v[220:223], v[144:159]\n" \
  "s_waitcnt lgkmcnt(6)\n" \
  "v_mfma_f32_32x32x16_bf16 v[128:143], v[236:239], v[216:219], v[128:143]\n" \
  "v_mfma_f32_32x32x16_bf16 v[160:175], v[236:239], v[220:223], v[160:175]\n" \
  "s_waitcnt lgkmcnt(4)\n" \
  "v_mfma_f32_32x32x16_bf16 v[16:31], v[240:243], v[216:219], v[16:31]\n" \
  "s_waitcnt lgkmcnt(0)\n" \
  "s_barrier\n" \
  "ds_read_b64_tr_b16 v[232:233], v4 offset:0\n" \
  "ds_read_b64_tr_b16 v[234:235], v4 offset:1024\n" \
  "v_mfma_f32_32x32x16_bf16 v[64:79], v[240:243], v[220:223], v[64:79]\n" \
  "ds_read_b128 v[208:211], v0 offset:0\n" \
  "ds_read_b128 v[212:215], v0 offset:4096\n" \
  "v_mfma_f32_32x32x16_bf16 v[32:47], v[244:247], v[216:219], v[32:47]\n" \
  "ds_read_b64_tr_b16 v[236:237], v5 offset:0\n" \
  "ds_read_b64_tr_b16 v[238:239], v5 offset:1024\n" \
  "v_mfma_f32_32x32x16_bf16 v[80:95], v[244:247], v[220:223], v[80:95]\n" \
  "ds_read_b64_tr_b16 v[240:241], v6 offset:0\n" \
  "ds_read_b64_tr_b16 v[242:243], v6 offset:768\n" \
  "v_mfma_f32_32x32x16_bf16 v[48:63], v[248:251], v[216:219], v[48:63]\n" \
  "ds_read_b64_tr_b16 v[244:245], v6 offset:32\n" \
  "ds_read_b64_tr_b16 v[246:247], v6 offset:800\n" \
  "v_mfma_f32_32x32x16_bf16 v[96:111], v[248:251], v[220:223], v[96:111]\n" \
  "ds_read_b64_tr_b16 v[248:249], v6 offset:64\n" \
  "ds_read_b64_tr_b16 v[250:251], v6 offset:832\n" \
  "s_waitcnt lgkmcnt(9)\n" \
  "v_mfma_f32_32x32x16_bf16 v[112:127], v[232:235], v[208:211], v[112:127]\n" \
  "ds_read_b128 v[216:219], v1 offset:0\n" \
  "s_waitcnt lgkmcnt(9)\n" \
  "v_mfma_f32_32x32x16_bf16 v[144:159], v[232:235], v[212:215], v[144:159]\n" \
  "ds_read_b64_tr_b16 v[232:233], v4 offset:4096\n" \
  "ds_read_b64_tr_b16 v[234:235], v4 offset:5120\n" \
  "s_waitcnt lgkmcnt(9)\n" \
  "v_mfma_f32_32x32x16_bf16 v[128:143], v[236:239], v[208:211], v[128:143]\n" \
  "ds_read_b128 v[220:223], v1 offset:4096\n" \
  "v_mfma_f32_32x32x16_bf16 v[160:175], v[236:239], v[212:215], v[160:175]\n" \
  "ds_read_b64_tr_b16 v[236:237], v5 offset:4096\n" \
  "ds_read_b64_tr_b16 v[238:239], v5 offset:5120\n" \
  "s_waitcnt lgkmcnt(10)\n" \
  "v_mfma_f32_32x32x16_bf16 v[16:31], v[240:243], v[208:211], v[16:31]\n" \
  "v_mfma_f32_32x32x16_bf16 v[64:79], v[240:243], v[212:215], v[64:79]\n" \
  "ds_read_b64_tr_b16 v[240:241], v6 offset:3072\n" \
  "ds_read_b64_tr_b16 v[242:243], v6 offset:3840\n" \
  "s_waitcnt lgkmcnt(10)\n" \
  "v_mfma_f32_32x32x16_bf16 v[32:47], v[244:247], v[208:211], v[32:47]\n" \
  "v_mfma_f32_32x32x16_bf16 v[80:95], v[244:247], v[212:215], v[80:95]\n" \
  "ds_read_b64_tr_b16 v[244:245], v6 offset:3104\n" \
  "ds_read_b64_tr_b16 v[246:247], v6 offset:3872\n" \
  "s_waitcnt lgkmcnt(10)\n" \
  "v_mfma_f32_32x32x16_bf16 v[48:63], v[248:251], v[208:211], v[48:63]\n" \
  "v_mfma_f32_32x32x16_bf16 v[96:111], v[248:251], v[212:215], v[96:111]\n" \
  "ds_read_b64_tr_b16 v[248:249], v6 offset:3136\n" \
  "ds_read_b64_tr_b16 v[250:251], v6 offset:3904\n" \
  "s_waitcnt lgkmcnt(9)\n" \
  "v_mfma_f32_32x32x16_bf16 v[112:127], v[232:235], v[216:219], v[112:127]\n" \
  "s_waitcnt lgkmcnt(8)\n" \
  "v_mfma_f32_32x32x16_bf16 v[144:159], v[232:235], v[220:223], v[144:159]\n" \
  "s_waitcnt lgkmcnt(6)\n" \
  "v_mfma_f32_32x32x16_bf16 v[128:143], v[236:239], v[216:219], v[128:143]\n" \
  "v_mfma_f32_32x32x16_bf16 v[160:175], v[236:239], v[220:223], v[160:175]\n" \
  "s_waitcnt lgkmcnt(4)\n" \
  "v_mfma_f32_32x32x16_bf16 v[16:31], v[240:243], v[216:219], v[16:31]\n" \
  "s_waitcnt lgkmcnt(0)\n" \
  "s_barrier\n" \
  "ds_read_b64_tr_b16 v[232:233], v4 offset:8192\n" \
  "ds_read_b64_tr_b16 v[234:235], v4 offset:9216\n" \
  "v_mfma_f32_32x32x16_bf16 v[64:79], v[240:243], v[220:223], v[64:79]\n" \
  "ds_read_b128 v[208:211], v2 offset:0\n" \
  "ds_read_b128 v[212:215], v2 offset:4096\n" \
  "v_mfma_f32_32x32x16_bf16 v[32:47], v[244:247], v[216:219], v[32:47]\n" \
  "ds_read_b64_tr_b16 v[236:237], v5 offset:8192\n" \
  "ds_read_b64_tr_b16 v[238:239], v5 offset:9216\n" \
  "v_mfma_f32_32x32x16_bf16 v[80:95], v[244:247], v[220:223], v[80:95]\n" \
  "ds_read_b64_tr_b16 v[240:241], v6 offset:12352\n" \
  "ds_read_b64_tr_b16 v[242:243], v6 offset:13120\n" \
  "v_mfma_f32_32x32x16_bf16 v[48:63], v[248:251], v[216:219], v[48:63]\n" \
  "ds_read_b64_tr_b16 v[244:245], v6 offset:12384\n" \
  "ds_read_b64_tr_b16 v[246:247], v6 offset:13152\n" \
  "v_mfma_f32_32x32x16_bf16 v[96:111], v[248:251], v[220:223], v[96:111]\n" \
  "ds_read_b64_tr_b16 v[248:249], v6 offset:12416\n" \
  "ds_read_b64_tr_b16 v[250:251], v6 offset:13184\n" \
  "s_waitcnt lgkmcnt(9)\n" \
  "v_mfma_f32_32x32x16_bf16 v[112:127], v[232:235], v[208:211], v[112:127]\n" \
  "ds_read_b128 v[216:219], v3 offset:0\n" \
  "s_waitcnt lgkmcnt(9)\n" \
  "v_mfma_f32_32x32x16_bf16 v[144:159], v[232:235], v[212:215], v[144:159]\n" \
  "ds_read_b64_tr_b16 v[232:233], v4 offset:12288\n" \
  "ds_read_b64_tr_b16 v[234:235], v4 offset:13312\n" \
  "s_waitcnt lgkmcnt(9)\n" \
  "v_mfma_f32_32x32x16_bf16 v[128:143], v[236:239], v[208:211], v[128:143]\n" \
  "ds_read_b128 v[220:223], v3 offset:4096\n" \
  "v_mfma_f32_32x32x16_bf16 v[160:175], v[236:239], v[212:215], v[160:175]\n" \
  "ds_read_b64_tr_b16 v[236:237], v5 offset:12288\n" \
  "ds_read_b64_tr_b16 v[238:239], v5 offset:13312\n" \
  "s_waitcnt lgkmcnt(10)\n" \
  "v_mfma_f32_32x32x16_bf16 v[16:31], v[240:243], v[208:211], v[16:31]\n" \
  "v_mfma_f32_32x32x16_bf16 v[64:79], v[240:243], v[212:215], v[64:79]\n" \
  "ds_read_b64_tr_b16 v[240:241], v6 offset:15424\n" \
  "ds_read_b64_tr_b16 v[242:243], v6 offset:16192\n" \
  "s_waitcnt lgkmcnt(10)\n" \
  "v_mfma_f32_32x32x16_bf16 v[32:47], v[244:247], v[208:211], v[32:47]\n" \
  "v_mfma_f32_32x32x16_bf16 v[80:95], v[244:247], v[212:215], v[80:95]\n" \
  "ds_read_b64_tr_b16 v[244:245], v6 offset:15456\n" \
  "ds_read_b64_tr_b16 v[246:247], v6 offset:16224\n" \
  "s_waitcnt lgkmcnt(10)\n" \
  "v_mfma_f32_32x32x16_bf16 v[48:63], v[248:251], v[208:211], v[48:63]\n" \
  "v_mfma_f32_32x32x16_bf16 v[96:111], v[248:251], v[212:215], v[96:111]\n" \
  "ds_read_b64_tr_b16 v[248:249], v6 offset:15488\n" \
  "ds_read_b64_tr_b16 v[250:251], v6 offset:16256\n" \
  "s_waitcnt lgkmcnt(9)\n" \
  "v_mfma_f32_32x32x16_bf16 v[112:127], v[232:235], v[216:219], v[112:127]\n" \
  "s_waitcnt lgkmcnt(8)\n" \
  "v_mfma_f32_32x32x16_bf16 v[144:159], v[232:235], v[220:223], v[144:159]\n" \
  "s_waitcnt lgkmcnt(6)\n" \
  "v_mfma_f32_32x32x16_bf16 v[128:143], v[236:239], v[216:219], v[128:143]\n" \
  "v_mfma_f32_32x32x16_bf16 v[160:175], v[236:239], v[220:223], v[160:175]\n" \
  "s_waitcnt lgkmcnt(4)\n" \
  "v_mfma_f32_32x32x16_bf16 v[16:31], v[240:243], v[216:219], v[16:31]\n" \
  "s_waitcnt lgkmcnt(0)\n" \
  "s_barrier\n" \
  "ds_read_b64_tr_b16 v[232:233], v4 offset:16384\n" \
  "ds_read_b64_tr_b16 v[234:235], v4 offset:17408\n" \
  "v_mfma_f32_32x32x16_bf16 v[64:79], v[240:243], v[220:223], v[64:79]\n" \
  "ds_read_b128 v[176:179], v0 offset:16384\n" \
  "ds_read_b128 v[180:183], v0 offset:20480\n" \
  "v_mfma_f32_32x32x16_bf16 v[32:47], v[244:247], v[216:219], v[32:47]\n" \
  "ds_read_b64_tr_b16 v[236:237], v5 offset:16384\n" \
  "ds_read_b64_tr_b16 v[238:239], v5 offset:17408\n" \
  "v_mfma_f32_32x32x16_bf16 v[80:95], v[244:247], v[220:223], v[80:95]\n" \
  "ds_read_b64_tr_b16 v[252:253], v4 offset:20480\n" \
  "ds_read_b64_tr_b16 v[254:255], v4 offset:21504\n" \
  "v_mfma_f32_32x32x16_bf16 v[48:63], v[248:251], v[216:219], v[48:63]\n" \
  "ds_read_b128 v[184:187], v1 offset:16384\n" \
  "ds_read_b128 v[188:191], v1 offset:20480\n" \
  "v_mfma_f32_32x32x16_bf16 v[96:111], v[248:251], v[220:223], v[96:111]\n" \
  "ds_read_b64_tr_b16 v[240:241], v5 offset:20480\n" \
  "ds_read_b64_tr_b16 v[242:243], v5 offset:21504\n" \
  "s_waitcnt lgkmcnt(9)\n" \
  "v_mfma_f32_32x32x16_bf16 v[112:127], v[232:235], v[176:179], v[112:127]\n" \
  "ds_read_b64_tr_b16 v[244:245], v4 offset:24576\n" \
  "ds_read_b64_tr_b16 v[246:247], v4 offset:25600\n" \
  "s_waitcnt lgkmcnt(10)\n" \
  "v_mfma_f32_32x32x16_bf16 v[144:159], v[232:235], v[180:183], v[144:159]\n" \
  "ds_read_b128 v[192:195], v2 offset:16384\n" \
  "ds_read_b128 v[196:199], v2 offset:20480\n" \
  "s_waitcnt lgkmcnt(10)\n" \
  "v_mfma_f32_32x32x16_bf16 v[128:143], v[236:239], v[176:179], v[128:143]\n" \
  "ds_read_b64_tr_b16 v[248:249], v5 offset:24576\n" \
  "ds_read_b64_tr_b16 v[250:251], v5 offset:25600\n" \
  "v_mfma_f32_32x32x16_bf16 v[160:175], v[236:239], v[180:183], v[160:175]\n" \
  "s_waitcnt lgkmcnt(9)\n" \
  "v_mfma_f32_32x32x16_bf16 v[112:127], v[252:255], v[184:187], v[112:127]\n" \
  "ds_read_b64_tr_b16 v[232:233], v4 offset:28672\n" \
  "ds_read_b64_tr_b16 v[234:235], v4 offset:29696\n" \
  "s_waitcnt lgkmcnt(10)\n" \
  "v_mfma_f32_32x32x16_bf16 v[144:159], v[252:255], v[188:191], v[144:159]\n" \
  "ds_read_b128 v[200:203], v3 offset:16384\n" \
  "ds_read_b128 v[204:207], v3 offset:20480\n" \
  "s_waitcnt lgkmcnt(10)\n" \
  "v_mfma_f32_32x32x16_bf16 v[128:143], v[240:243], v[184:187], v[128:143]\n" \
  "ds_read_b64_tr_b16 v[236:237], v5 offset:28672\n" \
  "ds_read_b64_tr_b16 v[238:239], v5 offset:29696\n" \
  "v_mfma_f32_32x32x16_bf16 v[160:175], v[240:243], v[188:191], v[160:175]\n" \
  "s_waitcnt lgkmcnt(9)\n" \
  "v_mfma_f32_32x32x16_bf16 v[112:127], v[244:247], v[192:195], v[112:127]\n" \
  "s_barrier\n" \
  "ds_read_b64_tr_b16 v[252:253], v4 offset:0\n" \
  "ds_read_b64_tr_b16 v[254:255], v4 offset:1024\n" \
  "s_waitcnt lgkmcnt(10)\n" \
  "v_mfma_f32_32x32x16_bf16 v[144:159], v[244:247], v[196:199], v[144:159]\n" \
  "ds_read_b128 v[208:211], v0 offset:32768\n" \
  "ds_read_b128 v[212:215], v0 offset:36864\n" \
  "s_waitcnt lgkmcnt(10)\n" \
  "v_mfma_f32_32x32x16_bf16 v[128:143], v[248:251], v[192:195], v[128:143]\n" \
  "ds_read_b64_tr_b16 v[240:241], v5 offset:0\n" \
  "ds_read_b64_tr_b16 v[242:243], v5 offset:1024\n" \
  "v_mfma_f32_32x32x16_bf16 v[160:175], v[248:251], v[196:199], v[160:175]\n" \
  "s_waitcnt lgkmcnt(9)\n" \
  "v_mfma_f32_32x32x16_bf16 v[112:127], v[232:235], v[200:203], v[112:127]\n" \
  "ds_read_b64_tr_b16 v[244:245], v4 offset:4096\n" \
  "ds_read_b64_tr_b16 v[246:247], v4 offset:5120\n" \
  "s_waitcnt lgkmcnt(10)\n" \
  "v_mfma_f32_32x32x16_bf16 v[144:159], v[232:235], v[204:207], v[144:159]\n" \
  "ds_read_b128 v[216:219], v1 offset:32768\n" \
  "ds_read_b128 v[220:223], v1 offset:36864\n" \
  "s_waitcnt lgkmcnt(10)\n" \
  "v_mfma_f32_32x32x16_bf16 v[128:143], v[236:239], v[200:203], v[128:143]\n" \
  "ds_read_b64_tr_b16 v[248:249], v5 offset:4096\n" \
  "ds_read_b64_tr_b16 v[250:251], v5 offset:5120\n" \
  "v_mfma_f32_32x32x16_bf16 v[160:175], v[236:239], v[204:207], v[160:175]\n" \
  "s_waitcnt lgkmcnt(9)\n" \
  "v_mfma_f32_32x32x16_bf16 v[112:127], v[252:255], v[208:211], v[112:127]\n" \
  "ds_read_b64_tr_b16 v[232:233], v4 offset:32768\n" \
  "ds_read_b64_tr_b16 v[234:235], v4 offset:33792\n" \
  "s_waitcnt lgkmcnt(10)\n" \
  "v_mfma_f32_32x32x16_bf16 v[144:159], v[252:255], v[212:215], v[144:159]\n" \
  "ds_read_b128 v[224:227], v2 offset:32768\n" \
  "ds_read_b128 v[228:231], v2 offset:36864\n" \
  "s_waitcnt lgkmcnt(10)\n" \
  "v_mfma_f32_32x32x16_bf16 v[128:143], v[240:243], v[208:211], v[128:143]\n" \
  "ds_read_b64_tr_b16 v[236:237], v5 offset:32768\n" \
  "ds_read_b64_tr_b16 v[238:239], v5 offset:33792\n" \
  "v_mfma_f32_32x32x16_bf16 v[160:175], v[240:243], v[212:215], v[160:175]\n" \
  "s_waitcnt lgkmcnt(9)\n" \
  "v_mfma_f32_32x32x16_bf16 v[112:127], v[244:247], v[216:219], v[112:127]\n" \
  "ds_read_b64_tr_b16 v[252:253], v4 offset:36864\n" \
  "ds_read_b64_tr_b16 v[254:255], v4 offset:37888\n" \
  "s_waitcnt lgkmcnt(10)\n" \
  "v_mfma_f32_32x32x16_bf16 v[144:159], v[244:247], v[220:223], v[144:159]\n" \
  "ds_read_b128 v[208:211], v3 offset:32768\n" \
  "ds_read_b128 v[212:215], v3 offset:36864\n" \
  "s_waitcnt lgkmcnt(10)\n" \
  "v_mfma_f32_32x32x16_bf16 v[128:143], v[248:251], v[216:219], v[128:143]\n" \
  "ds_read_b64_tr_b16 v[240:241], v5 offset:36864\n" \
  "ds_read_b64_tr_b16 v[242:243], v5 offset:37888\n" \
  "v_mfma_f32_32x32x16_bf16 v[160:175], v[248:251], v[220:223], v[160:175]\n" \
  "s_waitcnt lgkmcnt(9)\n" \
  "v_mfma_f32_32x32x16_bf16 v[112:127], v[232:235], v[224:227], v[112:127]\n" \
  "s_barrier\n" \
  "ds_read_b64_tr_b16 v[244:245], v4 offset:8192\n" \
  "ds_read_b64_tr_b16 v[246:247], v4 offset:9216\n" \
  "s_waitcnt lgkmcnt(10)\n" \
  "v_mfma_f32_32x32x16_bf16 v[144:159], v[232:235], v[228:231], v[144:159]\n" \
  "ds_read_b128 v[216:219], v0 offset:49152\n" \
  "ds_read_b128 v[220:223], v0 offset:53248\n" \
  "s_waitcnt lgkmcnt(10)\n" \
  "v_mfma_f32_32x32x16_bf16 v[128:143], v[236:239], v[224:227], v[128:143]\n" \
  "ds_read_b64_tr_b16 v[248:249], v5 offset:8192\n" \
  "ds_read_b64_tr_b16 v[250:251], v5 offset:9216\n" \
  "v_mfma_f32_32x32x16_bf16 v[160:175], v[236:239], v[228:231], v[160:175]\n" \
  "s_waitcnt lgkmcnt(9)\n" \
  "v_mfma_f32_32x32x16_bf16 v[112:127], v[252:255], v[208:211], v[112:127]\n" \
  "ds_read_b64_tr_b16 v[232:233], v4 offset:12288\n" \
  "ds_read_b64_tr_b16 v[234:235], v4 offset:13312\n" \
  "s_waitcnt lgkmcnt(10)\n" \
  "v_mfma_f32_32x32x16_bf16 v[144:159], v[252:255], v[212:215], v[144:159]\n" \
  "ds_read_b128 v[224:227], v1 offset:49152\n" \
  "ds_read_b128 v[228:231], v1 offset:53248\n" \
  "s_waitcnt lgkmcnt(10)\n" \
  "v_mfma_f32_32x32x16_bf16 v[128:143], v[240:243], v[208:211], v[128:143]\n" \
  "ds_read_b64_tr_b16 v[236:237], v5 offset:12288\n" \
  "ds_read_b64_tr_b16 v[238:239], v5 offset:13312\n" \
  "v_mfma_f32_32x32x16_bf16 v[160:175], v[240:243], v[212:215], v[160:175]\n" \
  "s_waitcnt lgkmcnt(9)\n" \
  "v_mfma_f32_32x32x16_bf16 v[112:127], v[244:247], v[216:219], v[112:127]\n" \
  "ds_read_b64_tr_b16 v[252:253], v4 offset:40960\n" \
  "ds_read_b64_tr_b16 v[254:255], v4 offset:41984\n" \
  "s_waitcnt lgkmcnt(10)\n" \
  "v_mfma_f32_32x32x16_bf16 v[144:159], v[244:247], v[220:223], v[144:159]\n" \
  "ds_read_b128 v[208:211], v2 offset:49152\n" \
  "ds_read_b128 v[212:215], v2 offset:53248\n" \
  "s_waitcnt lgkmcnt(10)\n" \
  "v_mfma_f32_32x32x16_bf16 v[128:143], v[248:251], v[216:219], v[128:143]\n" \
  "ds_read_b64_tr_b16 v[240:241], v5 offset:40960\n" \
  "ds_read_b64_tr_b16 v[242:243], v5 offset:41984\n" \
  "v_mfma_f32_32x32x16_bf16 v[160:175], v[248:251], v[220:223], v[160:175]\n" \
  "s_waitcnt lgkmcnt(9)\n" \
  "v_mfma_f32_32x32x16_bf16 v[112:127], v[232:235], v[224:227], v[112:127]\n" \
  "ds_read_b64_tr_b16 v[244:245], v4 offset:45056\n" \
  "ds_read_b64_tr_b16 v[246:247], v4 offset:46080\n" \
  "s_waitcnt lgkmcnt(10)\n" \
  "v_mfma_f32_32x32x16_bf16 v[144:159], v[232:235], v[228:231], v[144:159]\n" \
  "ds_read_b128 v[216:219], v3 offset:49152\n" \
  "ds_read_b128 v[220:223], v3 offset:53248\n" \
  "s_waitcnt lgkmcnt(10)\n" \
  "v_mfma_f32_32x32x16_bf16 v[128:143], v[236:239], v[224:227], v[128:143]\n" \
  "ds_read_b64_tr_b16 v[248:249], v5 offset:45056\n" \
  "ds_read_b64_tr_b16 v[250:251], v5 offset:46080\n" \
  "v_mfma_f32_32x32x16_bf16 v[160:175], v[236:239], v[228:231], v[160:175]\n" \
  "s_waitcnt lgkmcnt(9)\n" \
  "v_mfma_f32_32x32x16_bf16 v[112:127], v[252:255], v[208:211], v[112:127]\n" \
  "s_barrier\n" \
  "ds_read_b64_tr_b16 v[232:233], v4 offset:49152\n" \
  "ds_read_b64_tr_b16 v[234:235], v4 offset:50176\n" \
  "s_waitcnt lgkmcnt(10)\n" \
  "v_mfma_f32_32x32x16_bf16 v[144:159], v[252:255], v[212:215], v[144:159]\n" \
  "ds_read_b128 v[224:227], v0 offset:0\n" \
  "ds_read_b128 v[228:231], v0 offset:4096\n" \
  "s_waitcnt lgkmcnt(10)\n" \
  "v_mfma_f32_32x32x16_bf16 v[128:143], v[240:243], v[208:211], v[128:143]\n" \
  "ds_read_b64_tr_b16 v[236:237], v5 offset:49152\n" \
  "ds_read_b64_tr_b16 v[238:239], v5 offset:50176\n" \
  "v_mfma_f32_32x32x16_bf16 v[160:175], v[240:243], v[212:215], v[160:175]\n" \
  "s_waitcnt lgkmcnt(9)\n" \
  "v_mfma_f32_32x32x16_bf16 v[112:127], v[244:247], v[216:219], v[112:127]\n" \
  "ds_read_b64_tr_b16 v[252:253], v4 offset:53248\n" \
  "ds_read_b64_tr_b16 v[254:255], v4 offset:54272\n" \
  "s_waitcnt lgkmcnt(10)\n" \
  "v_mfma_f32_32x32x16_bf16 v[144:159], v[244:247], v[220:223], v[144:159]\n" \
  "ds_read_b128 v[208:211], v1 offset:0\n" \
  "ds_read_b128 v[212:215], v1 offset:4096\n" \
  "s_waitcnt lgkmcnt(10)\n" \
  "v_mfma_f32_32x32x16_bf16 v[128:143], v[248:251], v[216:219], v[128:143]\n" \
  "ds_read_b64_tr_b16 v[240:241], v5 offset:53248\n" \
  "ds_read_b64_tr_b16 v[242:243], v5 offset:54272\n" \
  "v_mfma_f32_32x32x16_bf16 v[160:175], v[248:251], v[220:223], v[160:175]\n" \
  "s_waitcnt lgkmcnt(9)\n" \
  "v_mfma_f32_32x32x16_bf16 v[112:127], v[232:235], v[224:227], v[112:127]\n" \
  "ds_read_b64_tr_b16 v[244:245], v4 offset:16384\n" \
  "ds_read_b64_tr_b16 v[246:247], v4 offset:17408\n" \
  "s_waitcnt lgkmcnt(10)\n" \
  "v_mfma_f32_32x32x16_bf16 v[144:159], v[232:235], v[228:231], v[144:159]\n" \
  "ds_read_b128 v[216:219], v2 offset:0\n" \
  "ds_read_b128 v[220:223], v2 offset:4096\n" \
  "s_waitcnt lgkmcnt(10)\n" \
  "v_mfma_f32_32x32x16_bf16 v[128:143], v[236:239], v[224:227], v[128:143]\n" \
  "ds_read_b64_tr_b16 v[248:249], v5 offset:16384\n" \
  "ds_read_b64_tr_b16 v[250:251], v5 offset:17408\n" \
  "v_mfma_f32_32x32x16_bf16 v[160:175], v[236:239], v[228:231], v[160:175]\n" \
  "s_waitcnt lgkmcnt(9)\n" \
  "v_mfma_f32_32x32x16_bf16 v[112:127], v[252:255], v[208:211], v[112:127]\n" \
  "ds_read_b64_tr_b16 v[232:233], v4 offset:20480\n" \
  "ds_read_b64_tr_b16 v[234:235], v4 offset:21504\n" \
  "s_waitcnt lgkmcnt(10)\n" \
  "v_mfma_f32_32x32x16_bf16 v[144:159], v[252:255], v[212:215], v[144:159]\n" \
  "ds_read_b128 v[224:227], v3 offset:0\n" \
  "ds_read_b128 v[228:231], v3 offset:4096\n" \
  "s_waitcnt lgkmcnt(10)\n" \
  "v_mfma_f32_32x32x16_bf16 v[128:143], v[240:243], v[208:211], v[128:143]\n" \
  "ds_read_b64_tr_b16 v[236:237], v5 offset:20480\n" \
  "ds_read_b64_tr_b16 v[238:239], v5 offset:21504\n" \
  "v_mfma_f32_32x32x16_bf16 v[160:175], v[240:243], v[212:215], v[160:175]\n" \
  "s_waitcnt lgkmcnt(9)\n" \
  "v_mfma_f32_32x32x16_bf16 v[112:127], v[244:247], v[216:219], v[112:127]\n" \
  "s_barrier\n" \
  "ds_read_b64_tr_b16 v[252:253], v4 offset:24576\n" \
  "ds_read_b64_tr_b16 v[254:255], v4 offset:25600\n" \
  "s_waitcnt lgkmcnt(10)\n" \
  "v_mfma_f32_32x32x16_bf16 v[144:159], v[244:247], v[220:223], v[144:159]\n" \
  "ds_read_b128 v[208:211], v0 offset:16384\n" \
  "ds_read_b128 v[212:215], v0 offset:20480\n" \
  "s_waitcnt lgkmcnt(10)\n" \
  "v_mfma_f32_32x32x16_bf16 v[128:143], v[248:251], v[216:219], v[128:143]\n" \
  "ds_read_b64_tr_b16 v[240:241], v5 offset:24576\n" \
  "ds_read_b64_tr_b16 v[242:243], v5 offset:25600\n" \
  "v_mfma_f32_32x32x16_bf16 v[160:175], v[248:251], v[220:223], v[160:175]\n" \
  "s_waitcnt lgkmcnt(9)\n" \
  "v_mfma_f32_32x32x16_bf16 v[112:127], v[232:235], v[224:227], v[112:127]\n" \
  "ds_read_b64_tr_b16 v[244:245], v4 offset:28672\n" \
  "ds_read_b64_tr_b16 v[246:247], v4 offset:29696\n" \
  "s_waitcnt lgkmcnt(10)\n" \
  "v_mfma_f32_32x32x16_bf16 v[144:159], v[232:235], v[228:231], v[144:159]\n" \
  "ds_read_b128 v[216:219], v1 offset:16384\n" \
  "ds_read_b128 v[220:223], v1 offset:20480\n" \
  "s_waitcnt lgkmcnt(10)\n" \
  "v_mfma_f32_32x32x16_bf16 v[128:143], v[236:239], v[224:227], v[128:143]\n" \
  "ds_read_b64_tr_b16 v[248:249], v5 offset:28672\n" \
  "ds_read_b64_tr_b16 v[250:251], v5 offset:29696\n" \
  "v_mfma_f32_32x32x16_bf16 v[160:175], v[236:239], v[228:231], v[160:175]\n" \
  "s_waitcnt lgkmcnt(9)\n" \
  "v_mfma_f32_32x32x16_bf16 v[112:127], v[252:255], v[208:211], v[112:127]\n" \
  "ds_read_b64_tr_b16 v[232:233], v4 offset:0\n" \
  "ds_read_b64_tr_b16 v[234:235], v4 offset:1024\n" \
  "s_waitcnt lgkmcnt(10)\n" \
  "v_mfma_f32_32x32x16_bf16 v[144:159], v[252:255], v[212:215], v[144:159]\n" \
  "ds_read_b128 v[224:227], v2 offset:16384\n" \
  "ds_read_b128 v[228:231], v2 offset:20480\n" \
  "s_waitcnt lgkmcnt(10)\n" \
  "v_mfma_f32_32x32x16_bf16 v[128:143], v[240:243], v[208:211], v[128:143]\n" \
  "ds_read_b64_tr_b16 v[236:237], v5 offset:0\n" \
  "ds_read_b64_tr_b16 v[238:239], v5 offset:1024\n" \
  "v_mfma_f32_32x32x16_bf16 v[160:175], v[240:243], v[212:215], v[160:175]\n" \
  "s_waitcnt lgkmcnt(9)\n" \
  "v_mfma_f32_32x32x16_bf16 v[112:127], v[244:247], v[216:219], v[112:127]\n" \
  "ds_read_b64_tr_b16 v[252:253], v4 offset:4096\n" \
  "ds_read_b64_tr_b16 v[254:255], v4 offset:5120\n" \
  "s_waitcnt lgkmcnt(10)\n" \
  "v_mfma_f32_32x32x16_bf16 v[144:159], v[244:247], v[220:223], v[144:159]\n" \
  "ds_read_b128 v[208:211], v3 offset:16384\n" \
  "ds_read_b128 v[212:215], v3 offset:20480\n" \
  "s_waitcnt lgkmcnt(10)\n" \
  "v_mfma_f32_32x32x16_bf16 v[128:143], v[248:251], v[216:219], v[128:143]\n" \
  "ds_read_b64_tr_b16 v[240:241], v5 offset:4096\n" \
  "ds_read_b64_tr_b16 v[242:243], v5 offset:5120\n" \
  "v_mfma_f32_32x32x16_bf16 v[160:175], v[248:251], v[220:223], v[160:175]\n" \
  "s_waitcnt lgkmcnt(9)\n" \
  "v_mfma_f32_32x32x16_bf16 v[112:127], v[232:235], v[224:227], v[112:127]\n" \
  "v_mov_b32 v220, 0\n" \
  "v_mov_b32 v221, 0\n" \
  "v_mov_b32 v222, 0\n" \
  "v_mov_b32 v223, 0\n" \
  "s_waitcnt lgkmcnt(0)\n" \
  "s_barrier\n" \
  "ds_read_b64 v[216:217], v9 offset:0\n" \
  "ds_read_b64 v[218:219], v9 offset:256\n" \
  "v_mfma_f32_32x32x16_bf16 v[144:159], v[232:235], v[228:231], v[144:159]\n" \
  "s_mov_b32 exec_hi, 0\n" \
  "ds_read2_b32 v[220:221], v11 offset0:0 offset1:0\n" \
  "ds_read2_b32 v[222:223], v11 offset0:32 offset1:32\n" \
  "s_mov_b32 exec_hi, -1\n" \
  "v_mfma_f32_32x32x16_bf16 v[128:143], v[236:239], v[224:227], v[128:143]\n" \
  "ds_read_b64_tr_b16 v[244:245], v6 offset:24704\n" \
  "ds_read_b64_tr_b16 v[246:247], v6 offset:25472\n" \
  "v_mfma_f32_32x32x16_bf16 v[160:175], v[236:239], v[228:231], v[160:175]\n" \
  "ds_read_b64_tr_b16 v[248:249], v6 offset:24736\n" \
  "ds_read_b64_tr_b16 v[250:251], v6 offset:25504\n" \
  "v_mfma_f32_32x32x16_bf16 v[112:127], v[252:255], v[208:211], v[112:127]\n" \
  "ds_read_b64_tr_b16 v[232:233], v6 offset:24768\n" \
  "ds_read_b64_tr_b16 v[234:235], v6 offset:25536\n" \
  "v_mfma_f32_32x32x16_bf16 v[144:159], v[252:255], v[212:215], v[144:159]\n" \
  "ds_read_b32 v14, v11 offset:512\n" \
  "ds_read_b32 v15, v11 offset:640\n" \
  "v_mfma_f32_32x32x16_bf16 v[128:143], v[240:243], v[208:211], v[128:143]\n" \
  "v_mfma_f32_32x32x16_bf16 v[160:175], v[240:243], v[212:215], v[160:175]\n" \
  "s_waitcnt lgkmcnt(9)\n" \
  "v_mfma_f32_32x32x8_bf16 v[112:127], v[216:217], v[220:221], v[112:127]\n" \
  "v_mfma_f32_32x32x8_bf16 v[128:143], v[218:219], v[220:221], v[128:143]\n" \
  "s_waitcnt lgkmcnt(8)\n" \
  "v_mfma_f32_32x32x8_bf16 v[144:159], v[216:217], v[222:223], v[144:159]\n" \
  "v_mfma_f32_32x32x8_bf16 v[160:175], v[218:219], v[222:223], v[160:175]\n" \
  "v_subrev_u32 v9, 32768, v6\n" \
  "s_waitcnt lgkmcnt(6)\n" \
  "v_mfma_f32_32x32x16_bf16 v[16:31], v[244:247], v[176:179], v[16:31]\n" \
  "ds_read_b64_tr_b16 v[236:237], v6 offset:27776\n" \
  "ds_read_b64_tr_b16 v[238:239], v6 offset:28544\n" \
  "v_mfma_f32_32x32x16_bf16 v[64:79], v[244:247], v[180:183], v[64:79]\n" \
  "s_waitcnt lgkmcnt(6)\n" \
  "v_mfma_f32_32x32x16_bf16 v[32:47], v[248:251], v[176:179], v[32:47]\n" \
  "ds_read_b64_tr_b16 v[252:253], v6 offset:27808\n" \
  "ds_read_b64_tr_b16 v[254:255], v6 offset:28576\n" \
  "v_mfma_f32_32x32x16_bf16 v[80:95], v[248:251], v[180:183], v[80:95]\n" \
  "ds_read_b64_tr_b16 v[240:241], v6 offset:27840\n" \
  "ds_read_b64_tr_b16 v[242:243], v6 offset:28608\n" \
  "s_waitcnt lgkmcnt(7)\n" \
  "v_mul_f32 v4, s95, v14\n" \
  "s_waitcnt lgkmcnt(6)\n" \
  "v_mul_f32 v5, s95, v15\n" \
  "v_mul_f32 v112, v112, v4\n" \
  "v_mfma_f32_32x32x16_bf16 v[48:63], v[232:235], v[176:179], v[48:63]\n" \
  "v_mul_f32 v113, v113, v4\n" \
  "v_mul_f32 v114, v114, v4\n" \
  "v_mul_f32 v115, v115, v4\n" \
  "v_mul_f32 v116, v116, v4\n" \
  "v_mul_f32 v117, v117, v4\n" \
  "v_mfma_f32_32x32x16_bf16 v[96:111], v[232:235], v[180:183], v[96:111]\n" \
  "v_mul_f32 v118, v118, v4\n" \
  "v_mul_f32 v119, v119, v4\n" \
  "v_cvt_pk_bf16_f32 v112, v112, v113\n" \
  "v_cvt_pk_bf16_f32 v113, v114, v115\n" \
  "v_cvt_pk_bf16_f32 v114, v116, v117\n" \
  "s_waitcnt lgkmcnt(4)\n" \
  "v_mfma_f32_32x32x16_bf16 v[16:31], v[236:239], v[184:187], v[16:31]\n" \
  "ds_read_b64_tr_b16 v[244:245], v6 offset:37056\n" \
  "ds_read_b64_tr_b16 v[246:247], v6 offset:37824\n" \
  "v_cvt_pk_bf16_f32 v115, v118, v119\n" \
  "buffer_store_dwordx4 v[112:115], v7, %5, s92 offen offset:0 sc1\n" \
  "v_mfma_f32_32x32x16_bf16 v[64:79], v[236:239], v[188:191], v[64:79]\n" \
  "v_mul_f32 v128, v128, v4\n" \
  "v_mul_f32 v129, v129, v4\n" \
  "v_mul_f32 v130, v130, v4\n" \
  "v_mul_f32 v131, v131, v4\n" \
  "v_mul_f32 v132, v132, v4\n" \
  "s_waitcnt lgkmcnt(4)\n" \
  "v_mfma_f32_32x32x16_bf16 v[32:47], v[252:255], v[184:187], v[32:47]\n" \
  "ds_read_b64_tr_b16 v[248:249], v6 offset:37088\n" \
  "ds_read_b64_tr_b16 v[250:251], v6 offset:37856\n" \
  "v_mul_f32 v133, v133, v4\n" \
  "v_mul_f32 v134, v134, v4\n" \
  "v_mul_f32 v135, v135, v4\n" \
  "v_mfma_f32_32x32x16_bf16 v[80:95], v[252:255], v[188:191], v[80:95]\n" \
  "ds_read_b64_tr_b16 v[232:233], v6 offset:37120\n" \
  "ds_read_b64_tr_b16 v[234:235], v6 offset:37888\n" \
  "v_cvt_pk_bf16_f32 v128, v128, v129\n" \
  "v_cvt_pk_bf16_f32 v129, v130, v131\n" \
  "v_cvt_pk_bf16_f32 v130, v132, v133\n" \
  "s_waitcnt lgkmcnt(6)\n" \
  "v_mfma_f32_32x32x16_bf16 v[48:63], v[240:243], v[184:187], v[48:63]\n" \
  "v_cvt_pk_bf16_f32 v131, v134, v135\n" \
  "buffer_store_dwordx4 v[128:131], v7, %5, s92 offen offset:2048 sc1\n" \
  "v_mfma_f32_32x32x16_bf16 v[96:111], v[240:243], v[188:191], v[96:111]\n" \
  "v_mul_f32 v120, v120, v4\n" \
  "v_mul_f32 v121, v121, v4\n" \
  "v_mul_f32 v122, v122, v4\n" \
  "v_mul_f32 v123, v123, v4\n" \
  "v_mul_f32 v124, v124, v4\n" \
  "s_waitcnt lgkmcnt(4)\n" \
  "v_mfma_f32_32x32x16_bf16 v[16:31], v[244:247], v[192:195], v[16:31]\n" \
  "ds_read_b64_tr_b16 v[236:237], v6 offset:40128\n" \
  "ds_read_b64_tr_b16 v[238:239], v6 offset:40896\n" \
  "v_mul_f32 v125, v125, v4\n" \
  "v_mul_f32 v126, v126, v4\n" \
  "v_mul_f32 v127, v127, v4\n" \
  "v_mfma_f32_32x32x16_bf16 v[64:79], v[244:247], v[196:199], v[64:79]\n" \
  "v_cvt_pk_bf16_f32 v120, v120, v121\n" \
  "v_cvt_pk_bf16_f32 v121, v122, v123\n" \
  "v_cvt_pk_bf16_f32 v122, v124, v125\n" \
  "v_cvt_pk_bf16_f32 v123, v126, v127\n" \
  "buffer_store_dwordx4 v[120:123], v7, %5, s92 offen offset:1024 sc1\n" \
  "s_waitcnt lgkmcnt(4)\n" \
  "v_mfma_f32_32x32x16_bf16 v[32:47], v[248:251], v[192:195], v[32:47]\n" \
  "ds_read_b64_tr_b16 v[252:253], v6 offset:40160\n" \
  "ds_read_b64_tr_b16 v[254:255], v6 offset:40928\n" \
  "v_mfma_f32_32x32x16_bf16 v[80:95], v[248:251], v[196:199], v[80:95]\n" \
  "ds_read_b64_tr_b16 v[240:241], v6 offset:40192\n" \
  "ds_read_b64_tr_b16 v[242:243], v6 offset:40960\n" \
  "v_mul_f32 v136, v136, v4\n" \
  "v_mul_f32 v137, v137, v4\n" \
  "v_mul_f32 v138, v138, v4\n" \
  "s_waitcnt lgkmcnt(6)\n" \
  "v_mfma_f32_32x32x16_bf16 v[48:63], v[232:235], v[192:195], v[48:63]\n" \
  "v_mul_f32 v139, v139, v4\n" \
  "v_mul_f32 v140, v140, v4\n" \
  "v_mul_f32 v141, v141, v4\n" \
  "v_mul_f32 v142, v142, v4\n" \
  "v_mul_f32 v143, v143, v4\n" \
  "v_mfma_f32_32x32x16_bf16 v[96:111], v[232:235], v[196:199], v[96:111]\n" \
  "v_cvt_pk_bf16_f32 v136, v136, v137\n" \
  "v_cvt_pk_bf16_f32 v137, v138, v139\n" \
  "v_cvt_pk_bf16_f32 v138, v140, v141\n" \
  "v_cvt_pk_bf16_f32 v139, v142, v143\n" \
  "buffer_store_dwordx4 v[136:139], v7, %5, s92 offen offset:3072 sc1\n" \
  "s_waitcnt lgkmcnt(4)\n" \
  "v_mfma_f32_32x32x16_bf16 v[16:31], v[236:239], v[200:203], v[16:31]\n" \
  "ds_read_b64_tr_b16 v[244:245], v6 offset:49408\n" \
  "ds_read_b64_tr_b16 v[246:247], v6 offset:50176\n" \
  "v_mfma_f32_32x32x16_bf16 v[64:79], v[236:239], v[204:207], v[64:79]\n" \
  "ds_read_b128 v[208:211], v0 offset:32768\n" \
  "ds_read_b128 v[212:215], v0 offset:36864\n" \
  "v_mul_f32 v144, v144, v5\n" \
  "v_mul_f32 v145, v145, v5\n" \
  "v_mul_f32 v146, v146, v5\n" \
  "s_waitcnt lgkmcnt(6)\n" \
  "v_mfma_f32_32x32x16_bf16 v[32:47], v[252:255], v[200:203], v[32:47]\n" \
  "ds_read_b64_tr_b16 v[248:249], v6 offset:49440\n" \
  "ds_read_b64_tr_b16 v[250:251], v6 offset:50208\n" \
  "v_mul_f32 v147, v147, v5\n" \
  "v_mul_f32 v148, v148, v5\n" \
  "v_mul_f32 v149, v149, v5\n" \
  "v_mfma_f32_32x32x16_bf16 v[80:95], v[252:255], v[204:207], v[80:95]\n" \
  "ds_read_b64_tr_b16 v[232:233], v6 offset:49472\n" \
  "ds_read_b64_tr_b16 v[234:235], v6 offset:50240\n" \
  "v_mul_f32 v150, v150, v5\n" \
  "v_mul_f32 v151, v151, v5\n" \
  "v_cvt_pk_bf16_f32 v144, v144, v145\n" \
  "s_waitcnt lgkmcnt(8)\n" \
  "v_mfma_f32_32x32x16_bf16 v[48:63], v[240:243], v[200:203], v[48:63]\n" \
  "v_cvt_pk_bf16_f32 v145, v146, v147\n" \
  "v_cvt_pk_bf16_f32 v146, v148, v149\n" \
  "v_cvt_pk_bf16_f32 v147, v150, v151\n" \
  "buffer_store_dwordx4 v[144:147], v7, %5, s93 offen offset:0 sc1\n" \
  "v_mfma_f32_32x32x16_bf16 v[96:111], v[240:243], v[204:207], v[96:111]\n" \
  "v_mul_f32 v160, v160, v5\n" \
  "v_mul_f32 v161, v161, v5\n" \
  "v_mul_f32 v162, v162, v5\n" \
  "v_mul_f32 v163, v163, v5\n" \
  "v_mul_f32 v164, v164, v5\n" \
  "s_waitcnt lgkmcnt(5)\n" \
  "v_mfma_f32_32x32x16_bf16 v[16:31], v[244:247], v[208:211], v[16:31]\n" \
  "ds_read_b64_tr_b16 v[236:237], v6 offset:52480\n" \
  "ds_read_b64_tr_b16 v[238:239], v6 offset:53248\n" \
  "v_mul_f32 v165, v165, v5\n" \
  "v_mul_f32 v166, v166, v5\n" \
  "v_mul_f32 v167, v167, v5\n" \
  "s_waitcnt lgkmcnt(6)\n" \
  "v_mfma_f32_32x32x16_bf16 v[64:79], v[244:247], v[212:215], v[64:79]\n" \
  "ds_read_b128 v[224:227], v1 offset:32768\n" \
  "ds_read_b128 v[228:231], v1 offset:36864\n" \
  "v_cvt_pk_bf16_f32 v160, v160, v161\n" \
  "v_cvt_pk_bf16_f32 v161, v162, v163\n" \
  "v_cvt_pk_bf16_f32 v162, v164, v165\n" \
  "s_waitcnt lgkmcnt(6)\n" \
  "v_mfma_f32_32x32x16_bf16 v[32:47], v[248:251], v[208:211], v[32:47]\n" \
  "ds_read_b64_tr_b16 v[252:253], v6 offset:52512\n" \
  "ds_read_b64_tr_b16 v[254:255], v6 offset:53280\n" \
  "v_cvt_pk_bf16_f32 v163, v166, v167\n" \
  "buffer_store_dwordx4 v[160:163], v7, %5, s93 offen offset:2048 sc1\n" \
  "v_mfma_f32_32x32x16_bf16 v[80:95], v[248:251], v[212:215], v[80:95]\n" \
  "ds_read_b64_tr_b16 v[240:241], v6 offset:52544\n" \
  "ds_read_b64_tr_b16 v[242:243], v6 offset:53312\n" \
  "v_mul_f32 v152, v152, v5\n" \
  "v_mul_f32 v153, v153, v5\n" \
  "v_mul_f32 v154, v154, v5\n" \
  "s_waitcnt lgkmcnt(8)\n" \
  "v_mfma_f32_32x32x16_bf16 v[48:63], v[232:235], v[208:211], v[48:63]\n" \
  "v_mul_f32 v155, v155, v5\n" \
  "v_mul_f32 v156, v156, v5\n" \
  "v_mul_f32 v157, v157, v5\n" \
  "v_mul_f32 v158, v158, v5\n" \
  "v_mul_f32 v159, v159, v5\n" \
  "v_mfma_f32_32x32x16_bf16 v[96:111], v[232:235], v[212:215], v[96:111]\n" \
  "v_cvt_pk_bf16_f32 v152, v152, v153\n" \
  "v_cvt_pk_bf16_f32 v153, v154, v155\n" \
  "v_cvt_pk_bf16_f32 v154, v156, v157\n" \
  "v_cvt_pk_bf16_f32 v155, v158, v159\n" \
  "buffer_store_dwordx4 v[152:155], v7, %5, s93 offen offset:1024 sc1\n" \
  "s_waitcnt lgkmcnt(5)\n" \
  "v_mfma_f32_32x32x16_bf16 v[16:31], v[236:239], v[224:227], v[16:31]\n" \
  "s_barrier\n" \
  "ds_read_b64_tr_b16 v[244:245], v6 offset:0\n" \
  "ds_read_b64_tr_b16 v[246:247], v6 offset:768\n" \
  "s_waitcnt lgkmcnt(6)\n" \
  "v_mfma_f32_32x32x16_bf16 v[64:79], v[236:239], v[228:231], v[64:79]\n" \
  "ds_read_b128 v[208:211], v2 offset:32768\n" \
  "ds_read_b128 v[212:215], v2 offset:36864\n" \
  "v_mul_f32 v168, v168, v5\n" \
  "v_mul_f32 v169, v169, v5\n" \
  "v_mul_f32 v170, v170, v5\n" \
  "s_waitcnt lgkmcnt(6)\n" \
  "v_mfma_f32_32x32x16_bf16 v[32:47], v[252:255], v[224:227], v[32:47]\n" \
  "ds_read_b64_tr_b16 v[248:249], v6 offset:32\n" \
  "ds_read_b64_tr_b16 v[250:251], v6 offset:800\n" \
  "v_mul_f32 v171, v171, v5\n" \
  "v_mul_f32 v172, v172, v5\n" \
  "v_mul_f32 v173, v173, v5\n" \
  "v_mfma_f32_32x32x16_bf16 v[80:95], v[252:255], v[228:231], v[80:95]\n" \
  "ds_read_b64_tr_b16 v[232:233], v6 offset:64\n" \
  "ds_read_b64_tr_b16 v[234:235], v6 offset:832\n" \
  "v_mul_f32 v174, v174, v5\n" \
  "v_mul_f32 v175, v175, v5\n" \
  "v_cvt_pk_bf16_f32 v168, v168, v169\n" \
  "s_waitcnt lgkmcnt(8)\n" \
  "v_mfma_f32_32x32x16_bf16 v[48:63], v[240:243], v[224:227], v[48:63]\n" \
  "v_cvt_pk_bf16_f32 v169, v170, v171\n" \
  "v_cvt_pk_bf16_f32 v170, v172, v173\n" \
  "v_cvt_pk_bf16_f32 v171, v174, v175\n" \
  "buffer_store_dwordx4 v[168:171], v7, %5, s93 offen offset:3072 sc1\n" \
  "s_branch 8f\n" \
  "7:\n" \
  "s_waitcnt lgkmcnt(0)\n" \
  "s_barrier\n" \
  "ds_read_b64_tr_b16 v[232:233], v4 offset:0\n" \
  "ds_read_b64_tr_b16 v[234:235], v4 offset:1024\n" \
  "ds_read_b128 v[208:211], v0 offset:0\n" \
  "ds_read_b128 v[212:215], v0 offset:4096\n" \
  "ds_read_b64_tr_b16 v[236:237], v5 offset:0\n" \
  "ds_read_b64_tr_b16 v[238:239], v5 offset:1024\n" \
  "ds_read_b64_tr_b16 v[240:241], v6 offset:24704\n" \
  "ds_read_b64_tr_b16 v[242:243], v6 offset:25472\n" \
  "ds_read_b64_tr_b16 v[244:245], v6 offset:24736\n" \
  "ds_read_b64_tr_b16 v[246:247], v6 offset:25504\n" \
  "ds_read_b64_tr_b16 v[248:249], v6 offset:24768\n" \
  "ds_read_b64_tr_b16 v[250:251], v6 offset:25536\n" \
  "s_waitcnt lgkmcnt(9)\n" \
  "v_mfma_f32_32x32x16_bf16 v[112:127], v[208:211], v[232:235], 0\n" \
  "ds_read_b128 v[216:219], v1 offset:0\n" \
  "s_waitcnt lgkmcnt(9)\n" \
  "v_mfma_f32_32x32x16_bf16 v[144:159], v[212:215], v[232:235], 0\n" \
  "ds_read_b64_tr_b16 v[232:233], v4 offset:4096\n" \
  "ds_read_b64_tr_b16 v[234:235], v4 offset:5120\n" \
  "s_waitcnt lgkmcnt(9)\n" \
  "v_mfma_f32_32x32x16_bf16 v[128:143], v[208:211], v[236:239], 0\n" \
  "ds_read_b128 v[220:223], v1 offset:4096\n" \
  "v_mfma_f32_32x32x16_bf16 v[160:175], v[212:215], v[236:239], 0\n" \
  "ds_read_b64_tr_b16 v[236:237], v5 offset:4096\n" \
  "ds_read_b64_tr_b16 v[238:239], v5 offset:5120\n" \
  "s_waitcnt lgkmcnt(10)\n" \
  "v_mfma_f32_32x32x16_bf16 v[16:31], v[240:243], v[208:211], 0\n" \
  "v_mfma_f32_32x32x16_bf16 v[64:79], v[240:243], v[212:215], 0\n" \
  "ds_read_b64_tr_b16 v[240:241], v6 offset:27776\n" \
  "ds_read_b64_tr_b16 v[242:243], v6 offset:28544\n" \
  "s_waitcnt lgkmcnt(10)\n" \
  "v_mfma_f32_32x32x16_bf16 v[32:47], v[244:247], v[208:211], 0\n" \
  "v_mfma_f32_32x32x16_bf16 v[80:95], v[244:247], v[212:215], 0\n" \
  "ds_read_b64_tr_b16 v[244:245], v6 offset:27808\n" \
  "ds_read_b64_tr_b16 v[246:247], v6 offset:28576\n" \
  "s_waitcnt lgkmcnt(10)\n" \
  "v_mfma_f32_32x32x16_bf16 v[48:63], v[248:251], v[208:211], 0\n" \
  "v_mfma_f32_32x32x16_bf16 v[96:111], v[248:251], v[212:215], 0\n" \
  "ds_read_b64_tr_b16 v[248:249], v6 offset:27840\n" \
  "ds_read_b64_tr_b16 v[250:251], v6 offset:28608\n" \
  "s_waitcnt lgkmcnt(9)\n" \
  "v_mfma_f32_32x32x16_bf16 v[112:127], v[216:219], v[232:235], v[112:127]\n" \
  "s_waitcnt lgkmcnt(8)\n" \
  "v_mfma_f32_32x32x16_bf16 v[144:159], v[220:223], v[232:235], v[144:159]\n" \
  "s_waitcnt lgkmcnt(6)\n" \
  "v_mfma_f32_32x32x16_bf16 v[128:143], v[216:219], v[236:239], v[128:143]\n" \
  "v_mfma_f32_32x32x16_bf16 v[160:175], v[220:223], v[236:239], v[160:175]\n" \
  "s_waitcnt lgkmcnt(4)\n" \
  "v_mfma_f32_32x32x16_bf16 v[16:31], v[240:243], v[216:219], v[16:31]\n" \
  "s_waitcnt lgkmcnt(0)\n" \
  "s_barrier\n" \
  "ds_read_b64_tr_b16 v[232:233], v4 offset:8192\n" \
  "ds_read_b64_tr_b16 v[234:235], v4 offset:9216\n" \
  "v_mfma_f32_32x32x16_bf16 v[64:79], v[240:243], v[220:223], v[64:79]\n" \
  "ds_read_b128 v[208:211], v2 offset:0\n" \
  "ds_read_b128 v[212:215], v2 offset:4096\n" \
  "v_mfma_f32_32x32x16_bf16 v[32:47], v[244:247], v[216:219], v[32:47]\n" \
  "ds_read_b64_tr_b16 v[236:237], v5 offset:8192\n" \
  "ds_read_b64_tr_b16 v[238:239], v5 offset:9216\n" \
  "v_mfma_f32_32x32x16_bf16 v[80:95], v[244:247], v[220:223], v[80:95]\n" \
  "ds_read_b64_tr_b16 v[240:241], v6 offset:37056\n" \
  "ds_read_b64_tr_b16 v[242:243], v6 offset:37824\n" \
  "v_mfma_f32_32x32x16_bf16 v[48:63], v[248:251], v[216:219], v[48:63]\n" \
  "ds_read_b64_tr_b16 v[244:245], v6 offset:37088\n" \
  "ds_read_b64_tr_b16 v[246:247], v6 offset:37856\n" \
  "v_mfma_f32_32x32x16_bf16 v[96:111], v[248:251], v[220:223], v[96:111]\n" \
  "ds_read_b64_tr_b16 v[248:249], v6 offset:37120\n" \
  "ds_read_b64_tr_b16 v[250:251], v6 offset:37888\n" \
  "s_waitcnt lgkmcnt(9)\n" \
  "v_mfma_f32_32x32x16_bf16 v[112:127], v[208:211], v[232:235], v[112:127]\n" \
  "ds_read_b128 v[216:219], v3 offset:0\n" \
  "s_waitcnt lgkmcnt(9)\n" \
  "v_mfma_f32_32x32x16_bf16 v[144:159], v[212:215], v[232:235], v[144:159]\n" \
  "ds_read_b64_tr_b16 v[232:233], v4 offset:12288\n" \
  "ds_read_b64_tr_b16 v[234:235], v4 offset:13312\n" \
  "s_waitcnt lgkmcnt(9)\n" \
  "v_mfma_f32_32x32x16_bf16 v[128:143], v[208:211], v[236:239], v[128:143]\n" \
  "ds_read_b128 v[220:223], v3 offset:4096\n" \
  "v_mfma_f32_32x32x16_bf16 v[160:175], v[212:215], v[236:239], v[160:175]\n" \
  "ds_read_b64_tr_b16 v[236:237], v5 offset:12288\n" \
  "ds_read_b64_tr_b16 v[238:239], v5 offset:13312\n" \
  "s_waitcnt lgkmcnt(10)\n" \
  "v_mfma_f32_32x32x16_bf16 v[16:31], v[240:243], v[208:211], v[16:31]\n" \
  "v_mfma_f32_32x32x16_bf16 v[64:79], v[240:243], v[212:215], v[64:79]\n" \
  "ds_read_b64_tr_b16 v[240:241], v6 offset:40128\n" \
  "ds_read_b64_tr_b16 v[242:243], v6 offset:40896\n" \
  "s_waitcnt lgkmcnt(10)\n" \
  "v_mfma_f32_32x32x16_bf16 v[32:47], v[244:247], v[208:211], v[32:47]\n" \
  "v_mfma_f32_32x32x16_bf16 v[80:95], v[244:247], v[212:215], v[80:95]\n" \
  "ds_read_b64_tr_b16 v[244:245], v6 offset:40160\n" \
  "ds_read_b64_tr_b16 v[246:247], v6 offset:40928\n" \
  "s_waitcnt lgkmcnt(10)\n" \
  "v_mfma_f32_32x32x16_bf16 v[48:63], v[248:251], v[208:211], v[48:63]\n" \
  "v_mfma_f32_32x32x16_bf16 v[96:111], v[248:251], v[212:215], v[96:111]\n" \
  "ds_read_b64_tr_b16 v[248:249], v6 offset:40192\n" \
  "ds_read_b64_tr_b16 v[250:251], v6 offset:40960\n" \
  "s_waitcnt lgkmcnt(9)\n" \
  "v_mfma_f32_32x32x16_bf16 v[112:127], v[216:219], v[232:235], v[112:127]\n" \
  "s_waitcnt lgkmcnt(8)\n" \
  "v_mfma_f32_32x32x16_bf16 v[144:159], v[220:223], v[232:235], v[144:159]\n" \
  "s_waitcnt lgkmcnt(6)\n" \
  "v_mfma_f32_32x32x16_bf16 v[128:143], v[216:219], v[236:239], v[128:143]\n" \
  "v_mfma_f32_32x32x16_bf16 v[160:175], v[220:223], v[236:239], v[160:175]\n" \
  "s_waitcnt lgkmcnt(4)\n" \
  "v_mfma_f32_32x32x16_bf16 v[16:31], v[240:243], v[216:219], v[16:31]\n" \
  "s_waitcnt lgkmcnt(0)\n" \
  "s_barrier\n" \
  "ds_read_b64_tr_b16 v[232:233], v4 offset:16384\n" \
  "ds_read_b64_tr_b16 v[234:235], v4 offset:17408\n" \
  "v_mfma_f32_32x32x16_bf16 v[64:79], v[240:243], v[220:223], v[64:79]\n" \
  "ds_read_b128 v[208:211], v0 offset:16384\n" \
  "ds_read_b128 v[212:215], v0 offset:20480\n" \
  "v_mfma_f32_32x32x16_bf16 v[32:47], v[244:247], v[216:219], v[32:47]\n" \
  "ds_read_b64_tr_b16 v[236:237], v5 offset:16384\n" \
  "ds_read_b64_tr_b16 v[238:239], v5 offset:17408\n" \
  "v_mfma_f32_32x32x16_bf16 v[80:95], v[244:247], v[220:223], v[80:95]\n" \
  "ds_read_b64_tr_b16 v[240:241], v6 offset:49408\n" \
  "ds_read_b64_tr_b16 v[242:243], v6 offset:50176\n" \
  "v_mfma_f32_32x32x16_bf16 v[48:63], v[248:251], v[216:219], v[48:63]\n" \
  "ds_read_b64_tr_b16 v[244:245], v6 offset:49440\n" \
  "ds_read_b64_tr_b16 v[246:247], v6 offset:50208\n" \
  "v_mfma_f32_32x32x16_bf16 v[96:111], v[248:251], v[220:223], v[96:111]\n" \
  "ds_read_b64_tr_b16 v[248:249], v6 offset:49472\n" \
  "ds_read_b64_tr_b16 v[250:251], v6 offset:50240\n" \
  "s_waitcnt lgkmcnt(9)\n" \
  "v_mfma_f32_32x32x16_bf16 v[112:127], v[208:211], v[232:235], v[112:127]\n" \
  "ds_read_b128 v[216:219], v1 offset:16384\n" \
  "s_waitcnt lgkmcnt(9)\n" \
  "v_mfma_f32_32x32x16_bf16 v[144:159], v[212:215], v[232:235], v[144:159]\n" \
  "ds_read_b64_tr_b16 v[232:233], v4 offset:20480\n" \
  "ds_read_b64_tr_b16 v[234:235], v4 offset:21504\n" \
  "s_waitcnt lgkmcnt(9)\n" \
  "v_mfma_f32_32x32x16_bf16 v[128:143], v[208:211], v[236:239], v[128:143]\n" \
  "ds_read_b128 v[220:223], v1 offset:20480\n" \
  "v_mfma_f32_32x32x16_bf16 v[160:175], v[212:215], v[236:239], v[160:175]\n" \
  "ds_read_b64_tr_b16 v[236:237], v5 offset:20480\n" \
  "ds_read_b64_tr_b16 v[238:239], v5 offset:21504\n" \
  "s_waitcnt lgkmcnt(10)\n" \
  "v_mfma_f32_32x32x16_bf16 v[16:31], v[240:243], v[208:211], v[16:31]\n" \
  "v_mfma_f32_32x32x16_bf16 v[64:79], v[240:243], v[212:215], v[64:79]\n" \
  "ds_read_b64_tr_b16 v[240:241], v6 offset:52480\n" \
  "ds_read_b64_tr_b16 v[242:243], v6 offset:53248\n" \
  "s_waitcnt lgkmcnt(10)\n" \
  "v_mfma_f32_32x32x16_bf16 v[32:47], v[244:247], v[208:211], v[32:47]\n" \
  "v_mfma_f32_32x32x16_bf16 v[80:95], v[244:247], v[212:215], v[80:95]\n" \
  "ds_read_b64_tr_b16 v[244:245], v6 offset:52512\n" \
  "ds_read_b64_tr_b16 v[246:247], v6 offset:53280\n" \
  "s_waitcnt lgkmcnt(10)\n" \
  "v_mfma_f32_32x32x16_bf16 v[48:63], v[248:251], v[208:211], v[48:63]\n" \
  "v_mfma_f32_32x32x16_bf16 v[96:111], v[248:251], v[212:215], v[96:111]\n" \
  "ds_read_b64_tr_b16 v[248:249], v6 offset:52544\n" \
  "ds_read_b64_tr_b16 v[250:251], v6 offset:53312\n" \
  "s_waitcnt lgkmcnt(9)\n" \
  "v_mfma_f32_32x32x16_bf16 v[112:127], v[216:219], v[232:235], v[112:127]\n" \
  "s_waitcnt lgkmcnt(8)\n" \
  "v_mfma_f32_32x32x16_bf16 v[144:159], v[220:223], v[232:235], v[144:159]\n" \
  "s_waitcnt lgkmcnt(6)\n" \
  "v_mfma_f32_32x32x16_bf16 v[128:143], v[216:219], v[236:239], v[128:143]\n" \
  "v_mfma_f32_32x32x16_bf16 v[160:175], v[220:223], v[236:239], v[160:175]\n" \
  "s_waitcnt lgkmcnt(4)\n" \
  "v_mfma_f32_32x32x16_bf16 v[16:31], v[240:243], v[216:219], v[16:31]\n" \
  "s_waitcnt lgkmcnt(0)\n" \
  "s_barrier\n" \
  "ds_read_b64_tr_b16 v[232:233], v4 offset:24576\n" \
  "ds_read_b64_tr_b16 v[234:235], v4 offset:25600\n" \
  "v_mfma_f32_32x32x16_bf16 v[64:79], v[240:243], v[220:223], v[64:79]\n" \
  "ds_read_b128 v[208:211], v2 offset:16384\n" \
  "ds_read_b128 v[212:215], v2 offset:20480\n" \
  "v_mfma_f32_32x32x16_bf16 v[32:47], v[244:247], v[216:219], v[32:47]\n" \
  "ds_read_b64_tr_b16 v[236:237], v5 offset:24576\n" \
  "ds_read_b64_tr_b16 v[238:239], v5 offset:25600\n" \
  "v_mfma_f32_32x32x16_bf16 v[80:95], v[244:247], v[220:223], v[80:95]\n" \
  "ds_read_b64_tr_b16 v[240:241], v6 offset:0\n" \
  "ds_read_b64_tr_b16 v[242:243], v6 offset:768\n" \
  "v_mfma_f32_32x32x16_bf16 v[48:63], v[248:251], v[216:219], v[48:63]\n" \
  "ds_read_b64_tr_b16 v[244:245], v6 offset:32\n" \
  "ds_read_b64_tr_b16 v[246:247], v6 offset:800\n" \
  "v_mfma_f32_32x32x16_bf16 v[96:111], v[248:251], v[220:223], v[96:111]\n" \
  "ds_read_b64_tr_b16 v[248:249], v6 offset:64\n" \
  "ds_read_b64_tr_b16 v[250:251], v6 offset:832\n" \
  "s_waitcnt lgkmcnt(9)\n" \
  "v_mfma_f32_32x32x16_bf16 v[112:127], v[208:211], v[232:235], v[112:127]\n" \
  "ds_read_b128 v[216:219], v3 offset:16384\n" \
  "s_waitcnt lgkmcnt(9)\n" \
  "v_mfma_f32_32x32x16_bf16 v[144:159], v[212:215], v[232:235], v[144:159]\n" \
  "ds_read_b64_tr_b16 v[232:233], v4 offset:28672\n" \
  "ds_read_b64_tr_b16 v[234:235], v4 offset:29696\n" \
  "s_waitcnt lgkmcnt(9)\n" \
  "v_mfma_f32_32x32x16_bf16 v[128:143], v[208:211], v[236:239], v[128:143]\n" \
  "ds_read_b128 v[220:223], v3 offset:20480\n" \
  "v_mfma_f32_32x32x16_bf16 v[160:175], v[212:215], v[236:239], v[160:175]\n" \
  "ds_read_b64_tr_b16 v[236:237], v5 offset:28672\n" \
  "ds_read_b64_tr_b16 v[238:239], v5 offset:29696\n" \
  "s_waitcnt lgkmcnt(10)\n" \
  "v_mfma_f32_32x32x16_bf16 v[16:31], v[240:243], v[208:211], v[16:31]\n" \
  "v_mfma_f32_32x32x16_bf16 v[64:79], v[240:243], v[212:215], v[64:79]\n" \
  "ds_read_b64_tr_b16 v[240:241], v6 offset:3072\n" \
  "ds_read_b64_tr_b16 v[242:243], v6 offset:3840\n" \
  "s_waitcnt lgkmcnt(10)\n" \
  "v_mfma_f32_32x32x16_bf16 v[32:47], v[244:247], v[208:211], v[32:47]\n" \
  "v_mfma_f32_32x32x16_bf16 v[80:95], v[244:247], v[212:215], v[80:95]\n" \
  "ds_read_b64_tr_b16 v[244:245], v6 offset:3104\n" \
  "ds_read_b64_tr_b16 v[246:247], v6 offset:3872\n" \
  "s_waitcnt lgkmcnt(10)\n" \
  "v_mfma_f32_32x32x16_bf16 v[48:63], v[248:251], v[208:211], v[48:63]\n" \
  "v_mfma_f32_32x32x16_bf16 v[96:111], v[248:251], v[212:215], v[96:111]\n" \
  "ds_read_b64_tr_b16 v[248:249], v6 offset:3136\n" \
  "ds_read_b64_tr_b16 v[250:251], v6 offset:3904\n" \
  "s_waitcnt lgkmcnt(9)\n" \
  "v_mfma_f32_32x32x16_bf16 v[112:127], v[216:219], v[232:235], v[112:127]\n" \
  "s_waitcnt lgkmcnt(8)\n" \
  "v_mfma_f32_32x32x16_bf16 v[144:159], v[220:223], v[232:235], v[144:159]\n" \
  "s_waitcnt lgkmcnt(6)\n" \
  "v_mfma_f32_32x32x16_bf16 v[128:143], v[216:219], v[236:239], v[128:143]\n" \
  "v_mfma_f32_32x32x16_bf16 v[160:175], v[220:223], v[236:239], v[160:175]\n" \
  "s_waitcnt lgkmcnt(4)\n" \
  "v_mfma_f32_32x32x16_bf16 v[16:31], v[240:243], v[216:219], v[16:31]\n" \
  "s_waitcnt lgkmcnt(0)\n" \
  "s_barrier\n" \
  "ds_read_b64_tr_b16 v[232:233], v4 offset:0\n" \
  "ds_read_b64_tr_b16 v[234:235], v4 offset:1024\n" \
  "v_mfma_f32_32x32x16_bf16 v[64:79], v[240:243], v[220:223], v[64:79]\n" \
  "ds_read_b128 v[208:211], v0 offset:32768\n" \
  "ds_read_b128 v[212:215], v0 offset:36864\n" \
  "v_mfma_f32_32x32x16_bf16 v[32:47], v[244:247], v[216:219], v[32:47]\n" \
  "ds_read_b64_tr_b16 v[236:237], v5 offset:0\n" \
  "ds_read_b64_tr_b16 v[238:239], v5 offset:1024\n" \
  "v_mfma_f32_32x32x16_bf16 v[80:95], v[244:247], v[220:223], v[80:95]\n" \
  "ds_read_b64_tr_b16 v[240:241], v6 offset:12352\n" \
  "ds_read_b64_tr_b16 v[242:243], v6 offset:13120\n" \
  "v_mfma_f32_32x32x16_bf16 v[48:63], v[248:251], v[216:219], v[48:63]\n" \
  "ds_read_b64_tr_b16 v[244:245], v6 offset:12384\n" \
  "ds_read_b64_tr_b16 v[246:247], v6 offset:13152\n" \
  "v_mfma_f32_32x32x16_bf16 v[96:111], v[248:251], v[220:223], v[96:111]\n" \
  "ds_read_b64_tr_b16 v[248:249], v6 offset:12416\n" \
  "ds_read_b64_tr_b16 v[250:251], v6 offset:13184\n" \
  "s_waitcnt lgkmcnt(9)\n" \
  "v_mfma_f32_32x32x16_bf16 v[112:127], v[208:211], v[232:235], v[112:127]\n" \
  "ds_read_b128 v[216:219], v1 offset:32768\n" \
  "s_waitcnt lgkmcnt(9)\n" \
  "v_mfma_f32_32x32x16_bf16 v[144:159], v[212:215], v[232:235], v[144:159]\n" \
  "ds_read_b64_tr_b16 v[232:233], v4 offset:4096\n" \
  "ds_read_b64_tr_b16 v[234:235], v4 offset:5120\n" \
  "s_waitcnt lgkmcnt(9)\n" \
  "v_mfma_f32_32x32x16_bf16 v[128:143], v[208:211], v[236:239], v[128:143]\n" \
  "ds_read_b128 v[220:223], v1 offset:36864\n" \
  "v_mfma_f32_32x32x16_bf16 v[160:175], v[212:215], v[236:239], v[160:175]\n" \
  "ds_read_b64_tr_b16 v[236:237], v5 offset:4096\n" \
  "ds_read_b64_tr_b16 v[238:239], v5 offset:5120\n" \
  "s_waitcnt lgkmcnt(10)\n" \
  "v_mfma_f32_32x32x16_bf16 v[16:31], v[240:243], v[208:211], v[16:31]\n" \
  "v_mfma_f32_32x32x16_bf16 v[64:79], v[240:243], v[212:215], v[64:79]\n" \
  "ds_read_b64_tr_b16 v[240:241], v6 offset:15424\n" \
  "ds_read_b64_tr_b16 v[242:243], v6 offset:16192\n" \
  "s_waitcnt lgkmcnt(10)\n" \
  "v_mfma_f32_32x32x16_bf16 v[32:47], v[244:247], v[208:211], v[32:47]\n" \
  "v_mfma_f32_32x32x16_bf16 v[80:95], v[244:247], v[212:215], v[80:95]\n" \
  "ds_read_b64_tr_b16 v[244:245], v6 offset:15456\n" \
  "ds_read_b64_tr_b16 v[246:247], v6 offset:16224\n" \
  "s_waitcnt lgkmcnt(10)\n" \
  "v_mfma_f32_32x32x16_bf16 v[48:63], v[248:251], v[208:211], v[48:63]\n" \
  "v_mfma_f32_32x32x16_bf16 v[96:111], v[248:251], v[212:215], v[96:111]\n" \
  "ds_read_b64_tr_b16 v[248:249], v6 offset:15488\n" \
  "ds_read_b64_tr_b16 v[250:251], v6 offset:16256\n" \
  "s_waitcnt lgkmcnt(9)\n" \
  "v_mfma_f32_32x32x16_bf16 v[112:127], v[216:219], v[232:235], v[112:127]\n" \
  "s_waitcnt lgkmcnt(8)\n" \
  "v_mfma_f32_32x32x16_bf16 v[144:159], v[220:223], v[232:235], v[144:159]\n" \
  "s_waitcnt lgkmcnt(6)\n" \
  "v_mfma_f32_32x32x16_bf16 v[128:143], v[216:219], v[236:239], v[128:143]\n" \
  "v_mfma_f32_32x32x16_bf16 v[160:175], v[220:223], v[236:239], v[160:175]\n" \
  "s_waitcnt lgkmcnt(4)\n" \
  "v_mfma_f32_32x32x16_bf16 v[16:31], v[240:243], v[216:219], v[16:31]\n" \
  "s_waitcnt lgkmcnt(0)\n" \
  "s_barrier\n" \
  "ds_read_b64_tr_b16 v[232:233], v4 offset:8192\n" \
  "ds_read_b64_tr_b16 v[234:235], v4 offset:9216\n" \
  "v_mfma_f32_32x32x16_bf16 v[64:79], v[240:243], v[220:223], v[64:79]\n" \
  "ds_read_b128 v[208:211], v2 offset:32768\n" \
  "ds_read_b128 v[212:215], v2 offset:36864\n" \
  "v_mfma_f32_32x32x16_bf16 v[32:47], v[244:247], v[216:219], v[32:47]\n" \
  "ds_read_b64_tr_b16 v[236:237], v5 offset:8192\n" \
  "ds_read_b64_tr_b16 v[238:239], v5 offset:9216\n" \
  "v_mfma_f32_32x32x16_bf16 v[80:95], v[244:247], v[220:223], v[80:95]\n" \
  "ds_read_b64_tr_b16 v[240:241], v6 offset:24704\n" \
  "ds_read_b64_tr_b16 v[242:243], v6 offset:25472\n" \
  "v_mfma_f32_32x32x16_bf16 v[48:63], v[248:251], v[216:219], v[48:63]\n" \
  "ds_read_b64_tr_b16 v[244:245], v6 offset:24736\n" \
  "ds_read_b64_tr_b16 v[246:247], v6 offset:25504\n" \
  "v_mfma_f32_32x32x16_bf16 v[96:111], v[248:251], v[220:223], v[96:111]\n" \
  "ds_read_b64_tr_b16 v[248:249], v6 offset:24768\n" \
  "ds_read_b64_tr_b16 v[250:251], v6 offset:25536\n" \
  "s_waitcnt lgkmcnt(9)\n" \
  "v_mfma_f32_32x32x16_bf16 v[112:127], v[208:211], v[232:235], v[112:127]\n" \
  "ds_read_b128 v[216:219], v3 offset:32768\n" \
  "s_waitcnt lgkmcnt(9)\n" \
  "v_mfma_f32_32x32x16_bf16 v[144:159], v[212:215], v[232:235], v[144:159]\n" \
  "ds_read_b64_tr_b16 v[232:233], v4 offset:12288\n" \
  "ds_read_b64_tr_b16 v[234:235], v4 offset:13312\n" \
  "s_waitcnt lgkmcnt(9)\n" \
  "v_mfma_f32_32x32x16_bf16 v[128:143], v[208:211], v[236:239], v[128:143]\n" \
  "ds_read_b128 v[220:223], v3 offset:36864\n" \
  "v_mfma_f32_32x32x16_bf16 v[160:175], v[212:215], v[236:239], v[160:175]\n" \
  "ds_read_b64_tr_b16 v[236:237], v5 offset:12288\n" \
  "ds_read_b64_tr_b16 v[238:239], v5 offset:13312\n" \
  "s_waitcnt lgkmcnt(10)\n" \
  "v_mfma_f32_32x32x16_bf16 v[16:31], v[240:243], v[208:211], v[16:31]\n" \
  "v_mfma_f32_32x32x16_bf16 v[64:79], v[240:243], v[212:215], v[64:79]\n" \
  "ds_read_b64_tr_b16 v[240:241], v6 offset:27776\n" \
  "ds_read_b64_tr_b16 v[242:243], v6 offset:28544\n" \
  "s_waitcnt lgkmcnt(10)\n" \
  "v_mfma_f32_32x32x16_bf16 v[32:47], v[244:247], v[208:211], v[32:47]\n" \
  "v_mfma_f32_32x32x16_bf16 v[80:95], v[244:247], v[212:215], v[80:95]\n" \
  "ds_read_b64_tr_b16 v[244:245], v6 offset:27808\n" \
  "ds_read_b64_tr_b16 v[246:247], v6 offset:28576\n" \
  "s_waitcnt lgkmcnt(10)\n" \
  "v_mfma_f32_32x32x16_bf16 v[48:63], v[248:251], v[208:211], v[48:63]\n" \
  "v_mfma_f32_32x32x16_bf16 v[96:111], v[248:251], v[212:215], v[96:111]\n" \
  "ds_read_b64_tr_b16 v[248:249], v6 offset:27840\n" \
  "ds_read_b64_tr_b16 v[250:251], v6 offset:28608\n" \
  "s_waitcnt lgkmcnt(9)\n" \
  "v_mfma_f32_32x32x16_bf16 v[112:127], v[216:219], v[232:235], v[112:127]\n" \
  "s_waitcnt lgkmcnt(8)\n" \
  "v_mfma_f32_32x32x16_bf16 v[144:159], v[220:223], v[232:235], v[144:159]\n" \
  "s_waitcnt lgkmcnt(6)\n" \
  "v_mfma_f32_32x32x16_bf16 v[128:143], v[216:219], v[236:239], v[128:143]\n" \
  "v_mfma_f32_32x32x16_bf16 v[160:175], v[220:223], v[236:239], v[160:175]\n" \
  "s_waitcnt lgkmcnt(4)\n" \
  "v_mfma_f32_32x32x16_bf16 v[16:31], v[240:243], v[216:219], v[16:31]\n" \
  "s_waitcnt lgkmcnt(0)\n" \
  "s_barrier\n" \
  "ds_read_b64_tr_b16 v[232:233], v4 offset:16384\n" \
  "ds_read_b64_tr_b16 v[234:235], v4 offset:17408\n" \
  "v_mfma_f32_32x32x16_bf16 v[64:79], v[240:243], v[220:223], v[64:79]\n" \
  "ds_read_b128 v[208:211], v0 offset:49152\n" \
  "ds_read_b128 v[212:215], v0 offset:53248\n" \
  "v_mfma_f32_32x32x16_bf16 v[32:47], v[244:247], v[216:219], v[32:47]\n" \
  "ds_read_b64_tr_b16 v[236:237], v5 offset:16384\n" \
  "ds_read_b64_tr_b16 v[238:239], v5 offset:17408\n" \
  "v_mfma_f32_32x32x16_bf16 v[80:95], v[244:247], v[220:223], v[80:95]\n" \
  "ds_read_b64_tr_b16 v[240:241], v6 offset:37056\n" \
  "ds_read_b64_tr_b16 v[242:243], v6 offset:37824\n" \
  "v_mfma_f32_32x32x16_bf16 v[48:63], v[248:251], v[216:219], v[48:63]\n" \
  "ds_read_b64_tr_b16 v[244:245], v6 offset:37088\n" \
  "ds_read_b64_tr_b16 v[246:247], v6 offset:37856\n" \
  "v_mfma_f32_32x32x16_bf16 v[96:111], v[248:251], v[220:223], v[96:111]\n" \
  "ds_read_b64_tr_b16 v[248:249], v6 offset:37120\n" \
  "ds_read_b64_tr_b16 v[250:251], v6 offset:37888\n" \
  "s_waitcnt lgkmcnt(9)\n" \
  "v_mfma_f32_32x32x16_bf16 v[112:127], v[208:211], v[232:235], v[112:127]\n" \
  "ds_read_b128 v[216:219], v1 offset:49152\n" \
  "s_waitcnt lgkmcnt(9)\n" \
  "v_mfma_f32_32x32x16_bf16 v[144:159], v[212:215], v[232:235], v[144:159]\n" \
  "ds_read_b64_tr_b16 v[232:233], v4 offset:20480\n" \
  "ds_read_b64_tr_b16 v[234:235], v4 offset:21504\n" \
  "s_waitcnt lgkmcnt(9)\n" \
  "v_mfma_f32_32x32x16_bf16 v[128:143], v[208:211], v[236:239], v[128:143]\n" \
  "ds_read_b128 v[220:223], v1 offset:53248\n" \
  "v_mfma_f32_32x32x16_bf16 v[160:175], v[212:215], v[236:239], v[160:175]\n" \
  "ds_read_b64_tr_b16 v[236:237], v5 offset:20480\n" \
  "ds_read_b64_tr_b16 v[238:239], v5 offset:21504\n" \
  "s_waitcnt lgkmcnt(10)\n" \
  "v_mfma_f32_32x32x16_bf16 v[16:31], v[240:243], v[208:211], v[16:31]\n" \
  "v_mfma_f32_32x32x16_bf16 v[64:79], v[240:243], v[212:215], v[64:79]\n" \
  "ds_read_b64_tr_b16 v[240:241], v6 offset:40128\n" \
  "ds_read_b64_tr_b16 v[242:243], v6 offset:40896\n" \
  "s_waitcnt lgkmcnt(10)\n" \
  "v_mfma_f32_32x32x16_bf16 v[32:47], v[244:247], v[208:211], v[32:47]\n" \
  "v_mfma_f32_32x32x16_bf16 v[80:95], v[244:247], v[212:215], v[80:95]\n" \
  "ds_read_b64_tr_b16 v[244:245], v6 offset:40160\n" \
  "ds_read_b64_tr_b16 v[246:247], v6 offset:40928\n" \
  "s_waitcnt lgkmcnt(10)\n" \
  "v_mfma_f32_32x32x16_bf16 v[48:63], v[248:251], v[208:211], v[48:63]\n" \
  "v_mfma_f32_32x32x16_bf16 v[96:111], v[248:251], v[212:215], v[96:111]\n" \
  "ds_read_b64_tr_b16 v[248:249], v6 offset:40192\n" \
  "ds_read_b64_tr_b16 v[250:251], v6 offset:40960\n" \
  "s_waitcnt lgkmcnt(9)\n" \
  "v_mfma_f32_32x32x16_bf16 v[112:127], v[216:219], v[232:235], v[112:127]\n" \
  "s_waitcnt lgkmcnt(8)\n" \
  "v_mfma_f32_32x32x16_bf16 v[144:159], v[220:223], v[232:235], v[144:159]\n" \
  "s_waitcnt lgkmcnt(6)\n" \
  "v_mfma_f32_32x32x16_bf16 v[128:143], v[216:219], v[236:239], v[128:143]\n" \
  "v_mfma_f32_32x32x16_bf16 v[160:175], v[220:223], v[236:239], v[160:175]\n" \
  "s_waitcnt lgkmcnt(4)\n" \
  "v_mfma_f32_32x32x16_bf16 v[16:31], v[240:243], v[216:219], v[16:31]\n" \
  "s_waitcnt lgkmcnt(0)\n" \
  "s_barrier\n" \
  "ds_read_b64_tr_b16 v[232:233], v4 offset:24576\n" \
  "ds_read_b64_tr_b16 v[234:235], v4 offset:25600\n" \
  "v_mfma_f32_32x32x16_bf16 v[64:79], v[240:243], v[220:223], v[64:79]\n" \
  "ds_read_b128 v[208:211], v2 offset:49152\n" \
  "ds_read_b128 v[212:215], v2 offset:53248\n" \
  "v_mfma_f32_32x32x16_bf16 v[32:47], v[244:247], v[216:219], v[32:47]\n" \
  "ds_read_b64_tr_b16 v[236:237], v5 offset:24576\n" \
  "ds_read_b64_tr_b16 v[238:239], v5 offset:25600\n" \
  "v_mfma_f32_32x32x16_bf16 v[80:95], v[244:247], v[220:223], v[80:95]\n" \
  "ds_read_b64_tr_b16 v[240:241], v6 offset:49408\n" \
  "ds_read_b64_tr_b16 v[242:243], v6 offset:50176\n" \
  "v_mfma_f32_32x32x16_bf16 v[48:63], v[248:251], v[216:219], v[48:63]\n" \
  "ds_read_b64_tr_b16 v[244:245], v6 offset:49440\n" \
  "ds_read_b64_tr_b16 v[246:247], v6 offset:50208\n" \
  "v_mfma_f32_32x32x16_bf16 v[96:111], v[248:251], v[220:223], v[96:111]\n" \
  "ds_read_b64_tr_b16 v[248:249], v6 offset:49472\n" \
  "ds_read_b64_tr_b16 v[250:251], v6 offset:50240\n" \
  "s_waitcnt lgkmcnt(9)\n" \
  "v_mfma_f32_32x32x16_bf16 v[112:127], v[208:211], v[232:235], v[112:127]\n" \
  "ds_read_b128 v[216:219], v3 offset:49152\n" \
  "s_waitcnt lgkmcnt(9)\n" \
  "v_mfma_f32_32x32x16_bf16 v[144:159], v[212:215], v[232:235], v[144:159]\n" \
  "ds_read_b64_tr_b16 v[232:233], v4 offset:28672\n" \
  "ds_read_b64_tr_b16 v[234:235], v4 offset:29696\n" \
  "s_waitcnt lgkmcnt(9)\n" \
  "v_mfma_f32_32x32x16_bf16 v[128:143], v[208:211], v[236:239], v[128:143]\n" \
  "ds_read_b128 v[220:223], v3 offset:53248\n" \
  "v_mfma_f32_32x32x16_bf16 v[160:175], v[212:215], v[236:239], v[160:175]\n" \
  "ds_read_b64_tr_b16 v[236:237], v5 offset:28672\n" \
  "ds_read_b64_tr_b16 v[238:239], v5 offset:29696\n" \
  "s_waitcnt lgkmcnt(10)\n" \
  "v_mfma_f32_32x32x16_bf16 v[16:31], v[240:243], v[208:211], v[16:31]\n" \
  "v_mfma_f32_32x32x16_bf16 v[64:79], v[240:243], v[212:215], v[64:79]\n" \
  "ds_read_b64_tr_b16 v[240:241], v6 offset:52480\n" \
  "ds_read_b64_tr_b16 v[242:243], v6 offset:53248\n" \
  "s_waitcnt lgkmcnt(10)\n" \
  "v_mfma_f32_32x32x16_bf16 v[32:47], v[244:247], v[208:211], v[32:47]\n" \
  "v_mfma_f32_32x32x16_bf16 v[80:95], v[244:247], v[212:215], v[80:95]\n" \
  "ds_read_b64_tr_b16 v[244:245], v6 offset:52512\n" \
  "ds_read_b64_tr_b16 v[246:247], v6 offset:53280\n" \
  "s_waitcnt lgkmcnt(10)\n" \
  "v_mfma_f32_32x32x16_bf16 v[48:63], v[248:251], v[208:211], v[48:63]\n" \
  "v_mfma_f32_32x32x16_bf16 v[96:111], v[248:251], v[212:215], v[96:111]\n" \
  "ds_read_b64_tr_b16 v[248:249], v6 offset:52544\n" \
  "ds_read_b64_tr_b16 v[250:251], v6 offset:53312\n" \
  "s_waitcnt lgkmcnt(9)\n" \
  "v_mfma_f32_32x32x16_bf16 v[112:127], v[216:219], v[232:235], v[112:127]\n" \
  "s_waitcnt lgkmcnt(8)\n" \
  "v_mfma_f32_32x32x16_bf16 v[144:159], v[220:223], v[232:235], v[144:159]\n" \
  "s_waitcnt lgkmcnt(6)\n" \
  "v_mfma_f32_32x32x16_bf16 v[128:143], v[216:219], v[236:239], v[128:143]\n" \
  "v_mfma_f32_32x32x16_bf16 v[160:175], v[220:223], v[236:239], v[160:175]\n" \
  "s_waitcnt lgkmcnt(4)\n" \
  "v_mfma_f32_32x32x16_bf16 v[16:31], v[240:243], v[216:219], v[16:31]\n" \
  "s_waitcnt lgkmcnt(0)\n" \
  "s_barrier\n" \
  "ds_read_b64_tr_b16 v[232:233], v4 offset:0\n" \
  "ds_read_b64_tr_b16 v[234:235], v4 offset:1024\n" \
  "v_mfma_f32_32x32x16_bf16 v[64:79], v[240:243], v[220:223], v[64:79]\n" \
  "ds_read_b128 v[208:211], v0 offset:0\n" \
  "ds_read_b128 v[212:215], v0 offset:4096\n" \
  "v_mfma_f32_32x32x16_bf16 v[32:47], v[244:247], v[216:219], v[32:47]\n" \
  "ds_read_b64_tr_b16 v[236:237], v5 offset:0\n" \
  "ds_read_b64_tr_b16 v[238:239], v5 offset:1024\n" \
  "v_mfma_f32_32x32x16_bf16 v[80:95], v[244:247], v[220:223], v[80:95]\n" \
  "ds_read_b64_tr_b16 v[240:241], v6 offset:0\n" \
  "ds_read_b64_tr_b16 v[242:243], v6 offset:768\n" \
  "v_mfma_f32_32x32x16_bf16 v[48:63], v[248:251], v[216:219], v[48:63]\n" \
  "ds_read_b64_tr_b16 v[244:245], v6 offset:32\n" \
  "ds_read_b64_tr_b16 v[246:247], v6 offset:800\n" \
  "v_mfma_f32_32x32x16_bf16 v[96:111], v[248:251], v[220:223], v[96:111]\n" \
  "ds_read_b64_tr_b16 v[248:249], v6 offset:64\n" \
  "ds_read_b64_tr_b16 v[250:251], v6 offset:832\n" \
  "s_waitcnt lgkmcnt(9)\n" \
  "v_mfma_f32_32x32x16_bf16 v[112:127], v[208:211], v[232:235], v[112:127]\n" \
  "ds_read_b128 v[216:219], v1 offset:0\n" \
  "s_waitcnt lgkmcnt(9)\n" \
  "v_mfma_f32_32x32x16_bf16 v[144:159], v[212:215], v[232:235], v[144:159]\n" \
  "ds_read_b64_tr_b16 v[232:233], v4 offset:4096\n" \
  "ds_read_b64_tr_b16 v[234:235], v4 offset:5120\n" \
  "s_waitcnt lgkmcnt(9)\n" \
  "v_mfma_f32_32x32x16_bf16 v[128:143], v[208:211], v[236:239], v[128:143]\n" \
  "ds_read_b128 v[220:223], v1 offset:4096\n" \
  "v_mfma_f32_32x32x16_bf16 v[160:175], v[212:215], v[236:239], v[160:175]\n" \
  "ds_read_b64_tr_b16 v[236:237], v5 offset:4096\n" \
  "ds_read_b64_tr_b16 v[238:239], v5 offset:5120\n" \
  "s_waitcnt lgkmcnt(10)\n" \
  "v_mfma_f32_32x32x16_bf16 v[16:31], v[240:243], v[208:211], v[16:31]\n" \
  "v_mfma_f32_32x32x16_bf16 v[64:79], v[240:243], v[212:215], v[64:79]\n" \
  "ds_read_b64_tr_b16 v[240:241], v6 offset:3072\n" \
  "ds_read_b64_tr_b16 v[242:243], v6 offset:3840\n" \
  "s_waitcnt lgkmcnt(10)\n" \
  "v_mfma_f32_32x32x16_bf16 v[32:47], v[244:247], v[208:211], v[32:47]\n" \
  "v_mfma_f32_32x32x16_bf16 v[80:95], v[244:247], v[212:215], v[80:95]\n" \
  "ds_read_b64_tr_b16 v[244:245], v6 offset:3104\n" \
  "ds_read_b64_tr_b16 v[246:247], v6 offset:3872\n" \
  "s_waitcnt lgkmcnt(10)\n" \
  "v_mfma_f32_32x32x16_bf16 v[48:63], v[248:251], v[208:211], v[48:63]\n" \
  "v_mfma_f32_32x32x16_bf16 v[96:111], v[248:251], v[212:215], v[96:111]\n" \
  "ds_read_b64_tr_b16 v[248:249], v6 offset:3136\n" \
  "ds_read_b64_tr_b16 v[250:251], v6 offset:3904\n" \
  "s_waitcnt lgkmcnt(9)\n" \
  "v_mfma_f32_32x32x16_bf16 v[112:127], v[216:219], v[232:235], v[112:127]\n" \
  "s_waitcnt lgkmcnt(8)\n" \
  "v_mfma_f32_32x32x16_bf16 v[144:159], v[220:223], v[232:235], v[144:159]\n" \
  "s_waitcnt lgkmcnt(6)\n" \
  "v_mfma_f32_32x32x16_bf16 v[128:143], v[216:219], v[236:239], v[128:143]\n" \
  "v_mfma_f32_32x32x16_bf16 v[160:175], v[220:223], v[236:239], v[160:175]\n" \
  "s_waitcnt lgkmcnt(4)\n" \
  "v_mfma_f32_32x32x16_bf16 v[16:31], v[240:243], v[216:219], v[16:31]\n" \
  "s_waitcnt lgkmcnt(0)\n" \
  "s_barrier\n" \
  "ds_read_b64_tr_b16 v[232:233], v4 offset:8192\n" \
  "ds_read_b64_tr_b16 v[234:235], v4 offset:9216\n" \
  "v_mfma_f32_32x32x16_bf16 v[64:79], v[240:243], v[220:223], v[64:79]\n" \
  "ds_read_b128 v[208:211], v2 offset:0\n" \
  "ds_read_b128 v[212:215], v2 offset:4096\n" \
  "v_mfma_f32_32x32x16_bf16 v[32:47], v[244:247], v[216:219], v[32:47]\n" \
  "ds_read_b64_tr_b16 v[236:237], v5 offset:8192\n" \
  "ds_read_b64_tr_b16 v[238:239], v5 offset:9216\n" \
  "v_mfma_f32_32x32x16_bf16 v[80:95], v[244:247], v[220:223], v[80:95]\n" \
  "ds_read_b64_tr_b16 v[240:241], v6 offset:12352\n" \
  "ds_read_b64_tr_b16 v[242:243], v6 offset:13120\n" \
  "v_mfma_f32_32x32x16_bf16 v[48:63], v[248:251], v[216:219], v[48:63]\n" \
  "ds_read_b64_tr_b16 v[244:245], v6 offset:12384\n" \
  "ds_read_b64_tr_b16 v[246:247], v6 offset:13152\n" \
  "v_mfma_f32_32x32x16_bf16 v[96:111], v[248:251], v[220:223], v[96:111]\n" \
  "ds_read_b64_tr_b16 v[248:249], v6 offset:12416\n" \
  "ds_read_b64_tr_b16 v[250:251], v6 offset:13184\n" \
  "s_waitcnt lgkmcnt(9)\n" \
  "v_mfma_f32_32x32x16_bf16 v[112:127], v[208:211], v[232:235], v[112:127]\n" \
  "ds_read_b128 v[216:219], v3 offset:0\n" \
  "s_waitcnt lgkmcnt(9)\n" \
  "v_mfma_f32_32x32x16_bf16 v[144:159], v[212:215], v[232:235], v[144:159]\n" \
  "ds_read_b64_tr_b16 v[232:233], v4 offset:12288\n" \
  "ds_read_b64_tr_b16 v[234:235], v4 offset:13312\n" \
  "s_waitcnt lgkmcnt(9)\n" \
  "v_mfma_f32_32x32x16_bf16 v[128:143], v[208:211], v[236:239], v[128:143]\n" \
  "ds_read_b128 v[220:223], v3 offset:4096\n" \
  "v_mfma_f32_32x32x16_bf16 v[160:175], v[212:215], v[236:239], v[160:175]\n" \
  "ds_read_b64_tr_b16 v[236:237], v5 offset:12288\n" \
  "ds_read_b64_tr_b16 v[238:239], v5 offset:13312\n" \
  "s_waitcnt lgkmcnt(10)\n" \
  "v_mfma_f32_32x32x16_bf16 v[16:31], v[240:243], v[208:211], v[16:31]\n" \
  "v_mfma_f32_32x32x16_bf16 v[64:79], v[240:243], v[212:215], v[64:79]\n" \
  "ds_read_b64_tr_b16 v[240:241], v6 offset:15424\n" \
  "ds_read_b64_tr_b16 v[242:243], v6 offset:16192\n" \
  "s_waitcnt lgkmcnt(10)\n" \
  "v_mfma_f32_32x32x16_bf16 v[32:47], v[244:247], v[208:211], v[32:47]\n" \
  "v_mfma_f32_32x32x16_bf16 v[80:95], v[244:247], v[212:215], v[80:95]\n" \
  "ds_read_b64_tr_b16 v[244:245], v6 offset:15456\n" \
  "ds_read_b64_tr_b16 v[246:247], v6 offset:16224\n" \
  "s_waitcnt lgkmcnt(10)\n" \
  "v_mfma_f32_32x32x16_bf16 v[48:63], v[248:251], v[208:211], v[48:63]\n" \
  "v_mfma_f32_32x32x16_bf16 v[96:111], v[248:251], v[212:215], v[96:111]\n" \
  "ds_read_b64_tr_b16 v[248:249], v6 offset:15488\n" \
  "ds_read_b64_tr_b16 v[250:251], v6 offset:16256\n" \
  "s_waitcnt lgkmcnt(9)\n" \
  "v_mfma_f32_32x32x16_bf16 v[112:127], v[216:219], v[232:235], v[112:127]\n" \
  "s_waitcnt lgkmcnt(8)\n" \
  "v_mfma_f32_32x32x16_bf16 v[144:159], v[220:223], v[232:235], v[144:159]\n" \
  "s_waitcnt lgkmcnt(6)\n" \
  "v_mfma_f32_32x32x16_bf16 v[128:143], v[216:219], v[236:239], v[128:143]\n" \
  "v_mfma_f32_32x32x16_bf16 v[160:175], v[220:223], v[236:239], v[160:175]\n" \
  "s_waitcnt lgkmcnt(4)\n" \
  "v_mfma_f32_32x32x16_bf16 v[16:31], v[240:243], v[216:219], v[16:31]\n" \
  "s_waitcnt lgkmcnt(0)\n" \
  "s_barrier\n" \
  "ds_read_b64_tr_b16 v[232:233], v4 offset:16384\n" \
  "ds_read_b64_tr_b16 v[234:235], v4 offset:17408\n" \
  "v_mfma_f32_32x32x16_bf16 v[64:79], v[240:243], v[220:223], v[64:79]\n" \
  "ds_read_b128 v[176:179], v0 offset:16384\n" \
  "ds_read_b128 v[180:183], v0 offset:20480\n" \
  "v_mfma_f32_32x32x16_bf16 v[32:47], v[244:247], v[216:219], v[32:47]\n" \
  "ds_read_b64_tr_b16 v[236:237], v5 offset:16384\n" \
  "ds_read_b64_tr_b16 v[238:239], v5 offset:17408\n" \
  "v_mfma_f32_32x32x16_bf16 v[80:95], v[244:247], v[220:223], v[80:95]\n" \
  "ds_read_b64_tr_b16 v[252:253], v4 offset:20480\n" \
  "ds_read_b64_tr_b16 v[254:255], v4 offset:21504\n" \
  "v_mfma_f32_32x32x16_bf16 v[48:63], v[248:251], v[216:219], v[48:63]\n" \
  "ds_read_b128 v[184:187], v1 offset:16384\n" \
  "ds_read_b128 v[188:191], v1 offset:20480\n" \
  "v_mfma_f32_32x32x16_bf16 v[96:111], v[248:251], v[220:223], v[96:111]\n" \
  "ds_read_b64_tr_b16 v[240:241], v5 offset:20480\n" \
  "ds_read_b64_tr_b16 v[242:243], v5 offset:21504\n" \
  "s_waitcnt lgkmcnt(9)\n" \
  "v_mfma_f32_32x32x16_bf16 v[112:127], v[176:179], v[232:235], v[112:127]\n" \
  "ds_read_b64_tr_b16 v[244:245], v4 offset:24576\n" \
  "ds_read_b64_tr_b16 v[246:247], v4 offset:25600\n" \
  "s_waitcnt lgkmcnt(10)\n" \
  "v_mfma_f32_32x32x16_bf16 v[144:159], v[180:183], v[232:235], v[144:159]\n" \
  "ds_read_b128 v[192:195], v2 offset:16384\n" \
  "ds_read_b128 v[196:199], v2 offset:20480\n" \
  "s_waitcnt lgkmcnt(10)\n" \
  "v_mfma_f32_32x32x16_bf16 v[128:143], v[176:179], v[236:239], v[128:143]\n" \
  "ds_read_b64_tr_b16 v[248:249], v5 offset:24576\n" \
  "ds_read_b64_tr_b16 v[250:251], v5 offset:25600\n" \
  "v_mfma_f32_32x32x16_bf16 v[160:175], v[180:183], v[236:239], v[160:175]\n" \
  "s_waitcnt lgkmcnt(9)\n" \
  "v_mfma_f32_32x32x16_bf16 v[112:127], v[184:187], v[252:255], v[112:127]\n" \
  "ds_read_b64_tr_b16 v[232:233], v4 offset:28672\n" \
  "ds_read_b64_tr_b16 v[234:235], v4 offset:29696\n" \
  "s_waitcnt lgkmcnt(10)\n" \
  "v_mfma_f32_32x32x16_bf16 v[144:159], v[188:191], v[252:255], v[144:159]\n" \
  "ds_read_b128 v[200:203], v3 offset:16384\n" \
  "ds_read_b128 v[204:207], v3 offset:20480\n" \
  "s_waitcnt lgkmcnt(10)\n" \
  "v_mfma_f32_32x32x16_bf16 v[128:143], v[184:187], v[240:243], v[128:143]\n" \
  "ds_read_b64_tr_b16 v[236:237], v5 offset:28672\n" \
  "ds_read_b64_tr_b16 v[238:239], v5 offset:29696\n" \
  "v_mfma_f32_32x32x16_bf16 v[160:175], v[188:191], v[240:243], v[160:175]\n" \
  "s_waitcnt lgkmcnt(9)\n" \
  "v_mfma_f32_32x32x16_bf16 v[112:127], v[192:195], v[244:247], v[112:127]\n" \
  "s_barrier\n" \
  "ds_read_b64_tr_b16 v[252:253], v4 offset:0\n" \
  "ds_read_b64_tr_b16 v[254:255], v4 offset:1024\n" \
  "s_waitcnt lgkmcnt(10)\n" \
  "v_mfma_f32_32x32x16_bf16 v[144:159], v[196:199], v[244:247], v[144:159]\n" \
  "ds_read_b128 v[208:211], v0 offset:32768\n" \
  "ds_read_b128 v[212:215], v0 offset:36864\n" \
  "s_waitcnt lgkmcnt(10)\n" \
  "v_mfma_f32_32x32x16_bf16 v[128:143], v[192:195], v[248:251], v[128:143]\n" \
  "ds_read_b64_tr_b16 v[240:241], v5 offset:0\n" \
  "ds_read_b64_tr_b16 v[242:243], v5 offset:1024\n" \
  "v_mfma_f32_32x32x16_bf16 v[160:175], v[196:199], v[248:251], v[160:175]\n" \
  "s_waitcnt lgkmcnt(9)\n" \
  "v_mfma_f32_32x32x16_bf16 v[112:127], v[200:203], v[232:235], v[112:127]\n" \
  "ds_read_b64_tr_b16 v[244:245], v4 offset:4096\n" \
  "ds_read_b64_tr_b16 v[246:247], v4 offset:5120\n" \
  "s_waitcnt lgkmcnt(10)\n" \
  "v_mfma_f32_32x32x16_bf16 v[144:159], v[204:207], v[232:235], v[144:159]\n" \
  "ds_read_b128 v[216:219], v1 offset:32768\n" \
  "ds_read_b128 v[220:223], v1 offset:36864\n" \
  "s_waitcnt lgkmcnt(10)\n" \
  "v_mfma_f32_32x32x16_bf16 v[128:143], v[200:203], v[236:239], v[128:143]\n" \
  "ds_read_b64_tr_b16 v[248:249], v5 offset:4096\n" \
  "ds_read_b64_tr_b16 v[250:251], v5 offset:5120\n" \
  "v_mfma_f32_32x32x16_bf16 v[160:175], v[204:207], v[236:239], v[160:175]\n" \
  "s_waitcnt lgkmcnt(9)\n" \
  "v_mfma_f32_32x32x16_bf16 v[112:127], v[208:211], v[252:255], v[112:127]\n" \
  "ds_read_b64_tr_b16 v[232:233], v4 offset:32768\n" \
  "ds_read_b64_tr_b16 v[234:235], v4 offset:33792\n" \
  "s_waitcnt lgkmcnt(10)\n" \
  "v_mfma_f32_32x32x16_bf16 v[144:159], v[212:215], v[252:255], v[144:159]\n" \
  "ds_read_b128 v[224:227], v2 offset:32768\n" \
  "ds_read_b128 v[228:231], v2 offset:36864\n" \
  "s_waitcnt lgkmcnt(10)\n" \
  "v_mfma_f32_32x32x16_bf16 v[128:143], v[208:211], v[240:243], v[128:143]\n" \
  "ds_read_b64_tr_b16 v[236:237], v5 offset:32768\n" \
  "ds_read_b64_tr_b16 v[238:239], v5 offset:33792\n" \
  "v_mfma_f32_32x32x16_bf16 v[160:175], v[212:215], v[240:243], v[160:175]\n" \
  "s_waitcnt lgkmcnt(9)\n" \
  "v_mfma_f32_32x32x16_bf16 v[112:127], v[216:219], v[244:247], v[112:127]\n" \
  "ds_read_b64_tr_b16 v[252:253], v4 offset:36864\n" \
  "ds_read_b64_tr_b16 v[254:255], v4 offset:37888\n" \
  "s_waitcnt lgkmcnt(10)\n" \
  "v_mfma_f32_32x32x16_bf16 v[144:159], v[220:223], v[244:247], v[144:159]\n" \
  "ds_read_b128 v[208:211], v3 offset:32768\n" \
  "ds_read_b128 v[212:215], v3 offset:36864\n" \
  "s_waitcnt lgkmcnt(10)\n" \
  "v_mfma_f32_32x32x16_bf16 v[128:143], v[216:219], v[248:251], v[128:143]\n" \
  "ds_read_b64_tr_b16 v[240:241], v5 offset:36864\n" \
  "ds_read_b64_tr_b16 v[242:243], v5 offset:37888\n" \
  "v_mfma_f32_32x32x16_bf16 v[160:175], v[220:223], v[248:251], v[160:175]\n" \
  "s_waitcnt lgkmcnt(9)\n" \
  "v_mfma_f32_32x32x16_bf16 v[112:127], v[224:227], v[232:235], v[112:127]\n" \
  "s_barrier\n" \
  "ds_read_b64_tr_b16 v[244:245], v4 offset:8192\n" \
  "ds_read_b64_tr_b16 v[246:247], v4 offset:9216\n" \
  "s_waitcnt lgkmcnt(10)\n" \
  "v_mfma_f32_32x32x16_bf16 v[144:159], v[228:231], v[232:235], v[144:159]\n" \
  "ds_read_b128 v[216:219], v0 offset:49152\n" \
  "ds_read_b128 v[220:223], v0 offset:53248\n" \
  "s_waitcnt lgkmcnt(10)\n" \
  "v_mfma_f32_32x32x16_bf16 v[128:143], v[224:227], v[236:239], v[128:143]\n" \
  "ds_read_b64_tr_b16 v[248:249], v5 offset:8192\n" \
  "ds_read_b64_tr_b16 v[250:251], v5 offset:9216\n" \
  "v_mfma_f32_32x32x16_bf16 v[160:175], v[228:231], v[236:239], v[160:175]\n" \
  "s_waitcnt lgkmcnt(9)\n" \
  "v_mfma_f32_32x32x16_bf16 v[112:127], v[208:211], v[252:255], v[112:127]\n" \
  "ds_read_b64_tr_b16 v[232:233], v4 offset:12288\n" \
  "ds_read_b64_tr_b16 v[234:235], v4 offset:13312\n" \
  "s_waitcnt lgkmcnt(10)\n" \
  "v_mfma_f32_32x32x16_bf16 v[144:159], v[212:215], v[252:255], v[144:159]\n" \
  "ds_read_b128 v[224:227], v1 offset:49152\n" \
  "ds_read_b128 v[228:231], v1 offset:53248\n" \
  "s_waitcnt lgkmcnt(10)\n" \
  "v_mfma_f32_32x32x16_bf16 v[128:143], v[208:211], v[240:243], v[128:143]\n" \
  "ds_read_b64_tr_b16 v[236:237], v5 offset:12288\n" \
  "ds_read_b64_tr_b16 v[238:239], v5 offset:13312\n" \
  "v_mfma_f32_32x32x16_bf16 v[160:175], v[212:215], v[240:243], v[160:175]\n" \
  "s_waitcnt lgkmcnt(9)\n" \
  "v_mfma_f32_32x32x16_bf16 v[112:127], v[216:219], v[244:247], v[112:127]\n" \
  "ds_read_b64_tr_b16 v[252:253], v4 offset:40960\n" \
  "ds_read_b64_tr_b16 v[254:255], v4 offset:41984\n" \
  "s_waitcnt lgkmcnt(10)\n" \
  "v_mfma_f32_32x32x16_bf16 v[144:159], v[220:223], v[244:247], v[144:159]\n" \
  "ds_read_b128 v[208:211], v2 offset:49152\n" \
  "ds_read_b128 v[212:215], v2 offset:53248\n" \
  "s_waitcnt lgkmcnt(10)\n" \
  "v_mfma_f32_32x32x16_bf16 v[128:143], v[216:219], v[248:251], v[128:143]\n" \
  "ds_read_b64_tr_b16 v[240:241], v5 offset:40960\n" \
  "ds_read_b64_tr_b16 v[242:243], v5 offset:41984\n" \
  "v_mfma_f32_32x32x16_bf16 v[160:175], v[220:223], v[248:251], v[160:175]\n" \
  "s_waitcnt lgkmcnt(9)\n" \
  "v_mfma_f32_32x32x16_bf16 v[112:127], v[224:227], v[232:235], v[112:127]\n" \
  "ds_read_b64_tr_b16 v[244:245], v4 offset:45056\n" \
  "ds_read_b64_tr_b16 v[246:247], v4 offset:46080\n" \
  "s_waitcnt lgkmcnt(10)\n" \
  "v_mfma_f32_32x32x16_bf16 v[144:159], v[228:231], v[232:235], v[144:159]\n" \
  "ds_read_b128 v[216:219], v3 offset:49152\n" \
  "ds_read_b128 v[220:223], v3 offset:53248\n" \
  "s_waitcnt lgkmcnt(10)\n" \
  "v_mfma_f32_32x32x16_bf16 v[128:143], v[224:227], v[236:239], v[128:143]\n" \
  "ds_read_b64_tr_b16 v[248:249], v5 offset:45056\n" \
  "ds_read_b64_tr_b16 v[250:251], v5 offset:46080\n" \
  "v_mfma_f32_32x32x16_bf16 v[160:175], v[228:231], v[236:239], v[160:175]\n" \
  "s_waitcnt lgkmcnt(9)\n" \
  "v_mfma_f32_32x32x16_bf16 v[112:127], v[208:211], v[252:255], v[112:127]\n" \
  "s_barrier\n" \
  "ds_read_b64_tr_b16 v[232:233], v4 offset:49152\n" \
  "ds_read_b64_tr_b16 v[234:235], v4 offset:50176\n" \
  "s_waitcnt lgkmcnt(10)\n" \
  "v_mfma_f32_32x32x16_bf16 v[144:159], v[212:215], v[252:255], v[144:159]\n" \
  "ds_read_b128 v[224:227], v0 offset:0\n" \
  "ds_read_b128 v[228:231], v0 offset:4096\n" \
  "s_waitcnt lgkmcnt(10)\n" \
  "v_mfma_f32_32x32x16_bf16 v[128:143], v[208:211], v[240:243], v[128:143]\n" \
  "ds_read_b64_tr_b16 v[236:237], v5 offset:49152\n" \
  "ds_read_b64_tr_b16 v[238:239], v5 offset:50176\n" \
  "v_mfma_f32_32x32x16_bf16 v[160:175], v[212:215], v[240:243], v[160:175]\n" \
  "s_waitcnt lgkmcnt(9)\n" \
  "v_mfma_f32_32x32x16_bf16 v[112:127], v[216:219], v[244:247], v[112:127]\n" \
  "ds_read_b64_tr_b16 v[252:253], v4 offset:53248\n" \
  "ds_read_b64_tr_b16 v[254:255], v4 offset:54272\n" \
  "s_waitcnt lgkmcnt(10)\n" \
  "v_mfma_f32_32x32x16_bf16 v[144:159], v[220:223], v[244:247], v[144:159]\n" \
  "ds_read_b128 v[208:211], v1 offset:0\n" \
  "ds_read_b128 v[212:215], v1 offset:4096\n" \
  "s_waitcnt lgkmcnt(10)\n" \
  "v_mfma_f32_32x32x16_bf16 v[128:143], v[216:219], v[248:251], v[128:143]\n" \
  "ds_read_b64_tr_b16 v[240:241], v5 offset:53248\n" \
  "ds_read_b64_tr_b16 v[242:243], v5 offset:54272\n" \
  "v_mfma_f32_32x32x16_bf16 v[160:175], v[220:223], v[248:251], v[160:175]\n" \
  "s_waitcnt lgkmcnt(9)\n" \
  "v_mfma_f32_32x32x16_bf16 v[112:127], v[224:227], v[232:235], v[112:127]\n" \
  "ds_read_b64_tr_b16 v[244:245], v4 offset:16384\n" \
  "ds_read_b64_tr_b16 v[246:247], v4 offset:17408\n" \
  "s_waitcnt lgkmcnt(10)\n" \
  "v_mfma_f32_32x32x16_bf16 v[144:159], v[228:231], v[232:235], v[144:159]\n" \
  "ds_read_b128 v[216:219], v2 offset:0\n" \
  "ds_read_b128 v[220:223], v2 offset:4096\n" \
  "s_waitcnt lgkmcnt(10)\n" \
  "v_mfma_f32_32x32x16_bf16 v[128:143], v[224:227], v[236:239], v[128:143]\n" \
  "ds_read_b64_tr_b16 v[248:249], v5 offset:16384\n" \
  "ds_read_b64_tr_b16 v[250:251], v5 offset:17408\n" \
  "v_mfma_f32_32x32x16_bf16 v[160:175], v[228:231], v[236:239], v[160:175]\n" \
  "s_waitcnt lgkmcnt(9)\n" \
  "v_mfma_f32_32x32x16_bf16 v[112:127], v[208:211], v[252:255], v[112:127]\n" \
  "ds_read_b64_tr_b16 v[232:233], v4 offset:20480\n" \
  "ds_read_b64_tr_b16 v[234:235], v4 offset:21504\n" \
  "s_waitcnt lgkmcnt(10)\n" \
  "v_mfma_f32_32x32x16_bf16 v[144:159], v[212:215], v[252:255], v[144:159]\n" \
  "ds_read_b128 v[224:227], v3 offset:0\n" \
  "ds_read_b128 v[228:231], v3 offset:4096\n" \
  "s_waitcnt lgkmcnt(10)\n" \
  "v_mfma_f32_32x32x16_bf16 v[128:143], v[208:211], v[240:243], v[128:143]\n" \
  "ds_read_b64_tr_b16 v[236:237], v5 offset:20480\n" \
  "ds_read_b64_tr_b16 v[238:239], v5 offset:21504\n" \
  "v_mfma_f32_32x32x16_bf16 v[160:175], v[212:215], v[240:243], v[160:175]\n" \
  "s_waitcnt lgkmcnt(9)\n" \
  "v_mfma_f32_32x32x16_bf16 v[112:127], v[216:219], v[244:247], v[112:127]\n" \
  "s_barrier\n" \
  "ds_read_b64_tr_b16 v[252:253], v4 offset:24576\n" \
  "ds_read_b64_tr_b16 v[254:255], v4 offset:25600\n" \
  "s_waitcnt lgkmcnt(10)\n" \
  "v_mfma_f32_32x32x16_bf16 v[144:159], v[220:223], v[244:247], v[144:159]\n" \
  "ds_read_b128 v[208:211], v0 offset:16384\n" \
  "ds_read_b128 v[212:215], v0 offset:20480\n" \
  "s_waitcnt lgkmcnt(10)\n" \
  "v_mfma_f32_32x32x16_bf16 v[128:143], v[216:219], v[248:251], v[128:143]\n" \
  "ds_read_b64_tr_b16 v[240:241], v5 offset:24576\n" \
  "ds_read_b64_tr_b16 v[242:243], v5 offset:25600\n" \
  "v_mfma_f32_32x32x16_bf16 v[160:175], v[220:223], v[248:251], v[160:175]\n" \
  "s_waitcnt lgkmcnt(9)\n" \
  "v_mfma_f32_32x32x16_bf16 v[112:127], v[224:227], v[232:235], v[112:127]\n" \
  "ds_read_b64_tr_b16 v[244:245], v4 offset:28672\n" \
  "ds_read_b64_tr_b16 v[246:247], v4 offset:29696\n" \
  "s_waitcnt lgkmcnt(10)\n" \
  "v_mfma_f32_32x32x16_bf16 v[144:159], v[228:231], v[232:235], v[144:159]\n" \
  "ds_read_b128 v[216:219], v1 offset:16384\n" \
  "ds_read_b128 v[220:223], v1 offset:20480\n" \
  "s_waitcnt lgkmcnt(10)\n" \
  "v_mfma_f32_32x32x16_bf16 v[128:143], v[224:227], v[236:239], v[128:143]\n" \
  "ds_read_b64_tr_b16 v[248:249], v5 offset:28672\n" \
  "ds_read_b64_tr_b16 v[250:251], v5 offset:29696\n" \
  "v_mfma_f32_32x32x16_bf16 v[160:175], v[228:231], v[236:239], v[160:175]\n" \
  "s_waitcnt lgkmcnt(9)\n" \
  "v_mfma_f32_32x32x16_bf16 v[112:127], v[208:211], v[252:255], v[112:127]\n" \
  "ds_read_b64_tr_b16 v[232:233], v4 offset:0\n" \
  "ds_read_b64_tr_b16 v[234:235], v4 offset:1024\n" \
  "s_waitcnt lgkmcnt(10)\n" \
  "v_mfma_f32_32x32x16_bf16 v[144:159], v[212:215], v[252:255], v[144:159]\n" \
  "ds_read_b128 v[224:227], v2 offset:16384\n" \
  "ds_read_b128 v[228:231], v2 offset:20480\n" \
  "s_waitcnt lgkmcnt(10)\n" \
  "v_mfma_f32_32x32x16_bf16 v[128:143], v[208:211], v[240:243], v[128:143]\n" \
  "ds_read_b64_tr_b16 v[236:237], v5 offset:0\n" \
  "ds_read_b64_tr_b16 v[238:239], v5 offset:1024\n" \
  "v_mfma_f32_32x32x16_bf16 v[160:175], v[212:215], v[240:243], v[160:175]\n" \
  "s_waitcnt lgkmcnt(9)\n" \
  "v_mfma_f32_32x32x16_bf16 v[112:127], v[216:219], v[244:247], v[112:127]\n" \
  "ds_read_b64_tr_b16 v[252:253], v4 offset:4096\n" \
  "ds_read_b64_tr_b16 v[254:255], v4 offset:5120\n" \
  "s_waitcnt lgkmcnt(10)\n" \
  "v_mfma_f32_32x32x16_bf16 v[144:159], v[220:223], v[244:247], v[144:159]\n" \
  "ds_read_b128 v[208:211], v3 offset:16384\n" \
  "ds_read_b128 v[212:215], v3 offset:20480\n" \
  "s_waitcnt lgkmcnt(10)\n" \
  "v_mfma_f32_32x32x16_bf16 v[128:143], v[216:219], v[248:251], v[128:143]\n" \
  "ds_read_b64_tr_b16 v[240:241], v5 offset:4096\n" \
  "ds_read_b64_tr_b16 v[242:243], v5 offset:5120\n" \
  "v_mfma_f32_32x32x16_bf16 v[160:175], v[220:223], v[248:251], v[160:175]\n" \
  "s_waitcnt lgkmcnt(9)\n" \
  "v_mfma_f32_32x32x16_bf16 v[112:127], v[224:227], v[232:235], v[112:127]\n" \
  "v_mov_b32 v220, 0\n" \
  "v_mov_b32 v221, 0\n" \
  "v_mov_b32 v222, 0\n" \
  "v_mov_b32 v223, 0\n" \
  "s_waitcnt lgkmcnt(0)\n" \
  "s_barrier\n" \
  "ds_read_b64 v[216:217], v9 offset:0\n" \
  "ds_read_b64 v[218:219], v9 offset:256\n" \
  "v_mfma_f32_32x32x16_bf16 v[144:159], v[228:231], v[232:235], v[144:159]\n" \
  "s_mov_b32 exec_hi, 0\n" \
  "ds_read2_b32 v[220:221], v11 offset0:0 offset1:0\n" \
  "ds_read2_b32 v[222:223], v11 offset0:32 offset1:32\n" \
  "s_mov_b32 exec_hi, -1\n" \
  "v_mfma_f32_32x32x16_bf16 v[128:143], v[224:227], v[236:239], v[128:143]\n" \
  "ds_read_b64_tr_b16 v[244:245], v6 offset:24704\n" \
  "ds_read_b64_tr_b16 v[246:247], v6 offset:25472\n" \
  "v_mfma_f32_32x32x16_bf16 v[160:175], v[228:231], v[236:239], v[160:175]\n" \
  "ds_read_b64_tr_b16 v[248:249], v6 offset:24736\n" \
  "ds_read_b64_tr_b16 v[250:251], v6 offset:25504\n" \
  "v_mfma_f32_32x32x16_bf16 v[112:127], v[208:211], v[252:255], v[112:127]\n" \
  "ds_read_b64_tr_b16 v[232:233], v6 offset:24768\n" \
  "ds_read_b64_tr_b16 v[234:235], v6 offset:25536\n" \
  "v_mfma_f32_32x32x16_bf16 v[144:159], v[212:215], v[252:255], v[144:159]\n" \
  "ds_read_b32 v14, v11 offset:512\n" \
  "ds_read_b32 v15, v11 offset:640\n" \
  "v_mfma_f32_32x32x16_bf16 v[128:143], v[208:211], v[240:243], v[128:143]\n" \
  "ds_read_b128 v[224:227], v12 offset:0\n" \
  "ds_read_b128 v[228:231], v12 offset:32\n" \
  "v_mfma_f32_32x32x16_bf16 v[160:175], v[212:215], v[240:243], v[160:175]\n" \
  "s_waitcnt lgkmcnt(11)\n" \
  "v_mfma_f32_32x32x8_bf16 v[112:127], v[220:221], v[216:217], v[112:127]\n" \
  "v_mfma_f32_32x32x8_bf16 v[128:143], v[220:221], v[218:219], v[128:143]\n" \
  "s_waitcnt lgkmcnt(10)\n" \
  "v_mfma_f32_32x32x8_bf16 v[144:159], v[222:223], v[216:217], v[144:159]\n" \
  "v_mfma_f32_32x32x8_bf16 v[160:175], v[222:223], v[218:219], v[160:175]\n" \
  "v_subrev_u32 v9, 32768, v6\n" \
  "s_waitcnt lgkmcnt(8)\n" \
  "v_mfma_f32_32x32x16_bf16 v[16:31], v[244:247], v[176:179], v[16:31]\n" \
  "ds_read_b64_tr_b16 v[236:237], v6 offset:27776\n" \
  "ds_read_b64_tr_b16 v[238:239], v6 offset:28544\n" \
  "v_mfma_f32_32x32x16_bf16 v[64:79], v[244:247], v[180:183], v[64:79]\n" \
  "s_waitcnt lgkmcnt(8)\n" \
  "v_mfma_f32_32x32x16_bf16 v[32:47], v[248:251], v[176:179], v[32:47]\n" \
  "ds_read_b64_tr_b16 v[252:253], v6 offset:27808\n" \
  "ds_read_b64_tr_b16 v[254:255], v6 offset:28576\n" \
  "v_mfma_f32_32x32x16_bf16 v[80:95], v[248:251], v[180:183], v[80:95]\n" \
  "ds_read_b64_tr_b16 v[240:241], v6 offset:27840\n" \
  "ds_read_b64_tr_b16 v[242:243], v6 offset:28608\n" \
  "s_waitcnt lgkmcnt(7)\n" \
  "v_mul_f32 v112, v112, v224\n" \
  "v_mul_f32 v113, v113, v225\n" \
  "v_mul_f32 v114, v114, v226\n" \
  "v_mfma_f32_32x32x16_bf16 v[48:63], v[232:235], v[176:179], v[48:63]\n" \
  "v_mul_f32 v115, v115, v227\n" \
  "s_waitcnt lgkmcnt(6)\n" \
  "v_mul_f32 v116, v116, v228\n" \
  "v_mul_f32 v117, v117, v229\n" \
  "v_mul_f32 v118, v118, v230\n" \
  "v_mul_f32 v119, v119, v231\n" \
  "v_mfma_f32_32x32x16_bf16 v[96:111], v[232:235], v[180:183], v[96:111]\n" \
  "v_cvt_pk_bf16_f32 v112, v112, v113\n" \
  "v_cvt_pk_bf16_f32 v113, v114, v115\n" \
  "v_cvt_pk_bf16_f32 v114, v116, v117\n" \
  "v_cvt_pk_bf16_f32 v115, v118, v119\n" \
  "buffer_store_dwordx4 v[112:115], v7, %5, s92 offen offset:0 sc1\n" \
  "s_waitcnt lgkmcnt(4)\n" \
  "v_mfma_f32_32x32x16_bf16 v[16:31], v[236:239], v[184:187], v[16:31]\n" \
  "ds_read_b64_tr_b16 v[244:245], v6 offset:37056\n" \
  "ds_read_b64_tr_b16 v[246:247], v6 offset:37824\n" \
  "v_mfma_f32_32x32x16_bf16 v[64:79], v[236:239], v[188:191], v[64:79]\n" \
  "ds_read_b128 v[176:179], v12 offset:64\n" \
  "ds_read_b128 v[180:183], v12 offset:96\n" \
  "v_mul_f32 v128, v128, v224\n" \
  "v_mul_f32 v129, v129, v225\n" \
  "v_mul_f32 v130, v130, v226\n" \
  "s_waitcnt lgkmcnt(6)\n" \
  "v_mfma_f32_32x32x16_bf16 v[32:47], v[252:255], v[184:187], v[32:47]\n" \
  "ds_read_b64_tr_b16 v[248:249], v6 offset:37088\n" \
  "ds_read_b64_tr_b16 v[250:251], v6 offset:37856\n" \
  "v_mul_f32 v131, v131, v227\n" \
  "v_mul_f32 v132, v132, v228\n" \
  "v_mul_f32 v133, v133, v229\n" \
  "v_mfma_f32_32x32x16_bf16 v[80:95], v[252:255], v[188:191], v[80:95]\n" \
  "ds_read_b64_tr_b16 v[232:233], v6 offset:37120\n" \
  "ds_read_b64_tr_b16 v[234:235], v6 offset:37888\n" \
  "v_mul_f32 v134, v134, v230\n" \
  "v_mul_f32 v135, v135, v231\n" \
  "v_cvt_pk_bf16_f32 v128, v128, v129\n" \
  "s_waitcnt lgkmcnt(8)\n" \
  "v_mfma_f32_32x32x16_bf16 v[48:63], v[240:243], v[184:187], v[48:63]\n" \
  "v_cvt_pk_bf16_f32 v129, v130, v131\n" \
  "v_cvt_pk_bf16_f32 v130, v132, v133\n" \
  "v_cvt_pk_bf16_f32 v131, v134, v135\n" \
  "buffer_store_dwordx4 v[128:131], v8, %5, s92 offen offset:0 sc1\n" \
  "v_mfma_f32_32x32x16_bf16 v[96:111], v[240:243], v[188:191], v[96:111]\n" \
  "s_waitcnt lgkmcnt(5)\n" \
  "v_mul_f32 v120, v120, v176\n" \
  "v_mul_f32 v121, v121, v177\n" \
  "v_mul_f32 v122, v122, v178\n" \
  "v_mul_f32 v123, v123, v179\n" \
  "s_waitcnt lgkmcnt(4)\n" \
  "v_mul_f32 v124, v124, v180\n" \
  "v_mfma_f32_32x32x16_bf16 v[16:31], v[244:247], v[192:195], v[16:31]\n" \
  "ds_read_b64_tr_b16 v[236:237], v6 offset:40128\n" \
  "ds_read_b64_tr_b16 v[238:239], v6 offset:40896\n" \
  "v_mul_f32 v125, v125, v181\n" \
  "v_mul_f32 v126, v126, v182\n" \
  "v_mul_f32 v127, v127, v183\n" \
  "v_mfma_f32_32x32x16_bf16 v[64:79], v[244:247], v[196:199], v[64:79]\n" \
  "v_cvt_pk_bf16_f32 v120, v120, v121\n" \
  "v_cvt_pk_bf16_f32 v121, v122, v123\n" \
  "v_cvt_pk_bf16_f32 v122, v124, v125\n" \
  "v_cvt_pk_bf16_f32 v123, v126, v127\n" \
  "buffer_store_dwordx4 v[120:123], v7, %5, s92 offen offset:640 sc1\n" \
  "s_waitcnt lgkmcnt(4)\n" \
  "v_mfma_f32_32x32x16_bf16 v[32:47], v[248:251], v[192:195], v[32:47]\n" \
  "ds_read_b64_tr_b16 v[252:253], v6 offset:40160\n" \
  "ds_read_b64_tr_b16 v[254:255], v6 offset:40928\n" \
  "v_mfma_f32_32x32x16_bf16 v[80:95], v[248:251], v[196:199], v[80:95]\n" \
  "ds_read_b64_tr_b16 v[240:241], v6 offset:40192\n" \
  "ds_read_b64_tr_b16 v[242:243], v6 offset:40960\n" \
  "ds_read_b128 v[184:187], v12 offset:128\n" \
  "ds_read_b128 v[188:191], v12 offset:160\n" \
  "v_mul_f32 v136, v136, v176\n" \
  "s_waitcnt lgkmcnt(8)\n" \
  "v_mfma_f32_32x32x16_bf16 v[48:63], v[232:235], v[192:195], v[48:63]\n" \
  "v_mul_f32 v137, v137, v177\n" \
  "v_mul_f32 v138, v138, v178\n" \
  "v_mul_f32 v139, v139, v179\n" \
  "v_mul_f32 v140, v140, v180\n" \
  "v_mul_f32 v141, v141, v181\n" \
  "v_mfma_f32_32x32x16_bf16 v[96:111], v[232:235], v[196:199], v[96:111]\n" \
  "v_mul_f32 v142, v142, v182\n" \
  "v_mul_f32 v143, v143, v183\n" \
  "v_cvt_pk_bf16_f32 v136, v136, v137\n" \
  "v_cvt_pk_bf16_f32 v137, v138, v139\n" \
  "v_cvt_pk_bf16_f32 v138, v140, v141\n" \
  "s_waitcnt lgkmcnt(6)\n" \
  "v_mfma_f32_32x32x16_bf16 v[16:31], v[236:239], v[200:203], v[16:31]\n" \
  "ds_read_b64_tr_b16 v[244:245], v6 offset:49408\n" \
  "ds_read_b64_tr_b16 v[246:247], v6 offset:50176\n" \
  "v_cvt_pk_bf16_f32 v139, v142, v143\n" \
  "buffer_store_dwordx4 v[136:139], v8, %5, s92 offen offset:640 sc1\n" \
  "v_mfma_f32_32x32x16_bf16 v[64:79], v[236:239], v[204:207], v[64:79]\n" \
  "ds_read_b128 v[208:211], v0 offset:32768\n" \
  "ds_read_b128 v[212:215], v0 offset:36864\n" \
  "s_waitcnt lgkmcnt(5)\n" \
  "v_mul_f32 v144, v144, v184\n" \
  "v_mul_f32 v145, v145, v185\n" \
  "v_mul_f32 v146, v146, v186\n" \
  "v_mfma_f32_32x32x16_bf16 v[32:47], v[252:255], v[200:203], v[32:47]\n" \
  "ds_read_b64_tr_b16 v[248:249], v6 offset:49440\n" \
  "ds_read_b64_tr_b16 v[250:251], v6 offset:50208\n" \
  "v_mul_f32 v147, v147, v187\n" \
  "s_waitcnt lgkmcnt(6)\n" \
  "v_mul_f32 v148, v148, v188\n" \
  "v_mul_f32 v149, v149, v189\n" \
  "v_mfma_f32_32x32x16_bf16 v[80:95], v[252:255], v[204:207], v[80:95]\n" \
  "ds_read_b64_tr_b16 v[232:233], v6 offset:49472\n" \
  "ds_read_b64_tr_b16 v[234:235], v6 offset:50240\n" \
  "v_mul_f32 v150, v150, v190\n" \
  "v_mul_f32 v151, v151, v191\n" \
  "v_cvt_pk_bf16_f32 v144, v144, v145\n" \
  "v_mfma_f32_32x32x16_bf16 v[48:63], v[240:243], v[200:203], v[48:63]\n" \
  "v_cvt_pk_bf16_f32 v145, v146, v147\n" \
  "v_cvt_pk_bf16_f32 v146, v148, v149\n" \
  "v_cvt_pk_bf16_f32 v147, v150, v151\n" \
  "buffer_store_dwordx4 v[144:147], v7, %5, s93 offen offset:0 sc1\n" \
  "v_mfma_f32_32x32x16_bf16 v[96:111], v[240:243], v[204:207], v[96:111]\n" \
  "ds_read_b128 v[192:195], v12 offset:192\n" \
  "ds_read_b128 v[196:199], v12 offset:224\n" \
  "v_mul_f32 v160, v160, v184\n" \
  "v_mul_f32 v161, v161, v185\n" \
  "v_mul_f32 v162, v162, v186\n" \
  "s_waitcnt lgkmcnt(7)\n" \
  "v_mfma_f32_32x32x16_bf16 v[16:31], v[244:247], v[208:211], v[16:31]\n" \
  "ds_read_b64_tr_b16 v[236:237], v6 offset:52480\n" \
  "ds_read_b64_tr_b16 v[238:239], v6 offset:53248\n" \
  "v_mul_f32 v163, v163, v187\n" \
  "v_mul_f32 v164, v164, v188\n" \
  "v_mul_f32 v165, v165, v189\n" \
  "s_waitcnt lgkmcnt(8)\n" \
  "v_mfma_f32_32x32x16_bf16 v[64:79], v[244:247], v[212:215], v[64:79]\n" \
  "ds_read_b128 v[224:227], v1 offset:32768\n" \
  "ds_read_b128 v[228:231], v1 offset:36864\n" \
  "v_mul_f32 v166, v166, v190\n" \
  "v_mul_f32 v167, v167, v191\n" \
  "v_cvt_pk_bf16_f32 v160, v160, v161\n" \
  "s_waitcnt lgkmcnt(8)\n" \
  "v_mfma_f32_32x32x16_bf16 v[32:47], v[248:251], v[208:211], v[32:47]\n" \
  "ds_read_b64_tr_b16 v[252:253], v6 offset:52512\n" \
  "ds_read_b64_tr_b16 v[254:255], v6 offset:53280\n" \
  "v_cvt_pk_bf16_f32 v161, v162, v163\n" \
  "v_cvt_pk_bf16_f32 v162, v164, v165\n" \
  "v_cvt_pk_bf16_f32 v163, v166, v167\n" \
  "v_mfma_f32_32x32x16_bf16 v[80:95], v[248:251], v[212:215], v[80:95]\n" \
  "ds_read_b64_tr_b16 v[240:241], v6 offset:52544\n" \
  "ds_read_b64_tr_b16 v[242:243], v6 offset:53312\n" \
  "buffer_store_dwordx4 v[160:163], v8, %5, s93 offen offset:0 sc1\n" \
  "s_waitcnt lgkmcnt(9)\n" \
  "v_mul_f32 v152, v152, v192\n" \
  "v_mul_f32 v153, v153, v193\n" \
  "v_mfma_f32_32x32x16_bf16 v[48:63], v[232:235], v[208:211], v[48:63]\n" \
  "v_mul_f32 v154, v154, v194\n" \
  "v_mul_f32 v155, v155, v195\n" \
  "s_waitcnt lgkmcnt(8)\n" \
  "v_mul_f32 v156, v156, v196\n" \
  "v_mul_f32 v157, v157, v197\n" \
  "v_mul_f32 v158, v158, v198\n" \
  "v_mfma_f32_32x32x16_bf16 v[96:111], v[232:235], v[212:215], v[96:111]\n" \
  "v_mul_f32 v159, v159, v199\n" \
  "v_cvt_pk_bf16_f32 v152, v152, v153\n" \
  "v_cvt_pk_bf16_f32 v153, v154, v155\n" \
  "v_cvt_pk_bf16_f32 v154, v156, v157\n" \
  "v_cvt_pk_bf16_f32 v155, v158, v159\n" \
  "s_waitcnt lgkmcnt(5)\n" \
  "v_mfma_f32_32x32x16_bf16 v[16:31], v[236:239], v[224:227], v[16:31]\n" \
  "s_barrier\n" \
  "ds_read_b64_tr_b16 v[244:245], v6 offset:0\n" \
  "ds_read_b64_tr_b16 v[246:247], v6 offset:768\n" \
  "buffer_store_dwordx4 v[152:155], v7, %5, s93 offen offset:640 sc1\n" \
  "s_waitcnt lgkmcnt(6)\n" \
  "v_mfma_f32_32x32x16_bf16 v[64:79], v[236:239], v[228:231], v[64:79]\n" \
  "ds_read_b128 v[208:211], v2 offset:32768\n" \
  "ds_read_b128 v[212:215], v2 offset:36864\n" \
  "v_mul_f32 v168, v168, v192\n" \
  "v_mul_f32 v169, v169, v193\n" \
  "v_mul_f32 v170, v170, v194\n" \
  "s_waitcnt lgkmcnt(6)\n" \
  "v_mfma_f32_32x32x16_bf16 v[32:47], v[252:255], v[224:227], v[32:47]\n" \
  "ds_read_b64_tr_b16 v[248:249], v6 offset:32\n" \
  "ds_read_b64_tr_b16 v[250:251], v6 offset:800\n" \
  "v_mul_f32 v171, v171, v195\n" \
  "v_mul_f32 v172, v172, v196\n" \
  "v_mul_f32 v173, v173, v197\n" \
  "v_mfma_f32_32x32x16_bf16 v[80:95], v[252:255], v[228:231], v[80:95]\n" \
  "ds_read_b64_tr_b16 v[232:233], v6 offset:64\n" \
  "ds_read_b64_tr_b16 v[234:235], v6 offset:832\n" \
  "v_mul_f32 v174, v174, v198\n" \
  "v_mul_f32 v175, v175, v199\n" \
  "v_cvt_pk_bf16_f32 v168, v168, v169\n" \
  "s_waitcnt lgkmcnt(8)\n" \
  "v_mfma_f32_32x32x16_bf16 v[48:63], v[240:243], v[224:227], v[48:63]\n" \
  "v_cvt_pk_bf16_f32 v169, v170, v171\n" \
  "v_cvt_pk_bf16_f32 v170, v172, v173\n" \
  "v_cvt_pk_bf16_f32 v171, v174, v175\n" \
  "buffer_store_dwordx4 v[168:171], v8, %5, s93 offen offset:640 sc1\n" \
  "8:\n" \
  "v_mfma_f32_32x32x16_bf16 v[96:111], v[240:243], v[228:231], v[96:111]\n" \
  "s_waitcnt lgkmcnt(5)\n" \
  "v_mfma_f32_32x32x16_bf16 v[16:31], v[244:247], v[208:211], v[16:31]\n" \
  "ds_read_b64_tr_b16 v[236:237], v6 offset:3072\n" \
  "ds_read_b64_tr_b16 v[238:239], v6 offset:3840\n" \
  "s_waitcnt lgkmcnt(6)\n" \
  "v_mfma_f32_32x32x16_bf16 v[64:79], v[244:247], v[212:215], v[64:79]\n" \
  "ds_read_b128 v[224:227], v3 offset:32768\n" \
  "ds_read_b128 v[228:231], v3 offset:36864\n" \
  "s_waitcnt lgkmcnt(6)\n" \
  "v_mfma_f32_32x32x16_bf16 v[32:47], v[248:251], v[208:211], v[32:47]\n" \
  "ds_read_b64_tr_b16 v[252:253], v6 offset:3104\n" \
  "ds_read_b64_tr_b16 v[254:255], v6 offset:3872\n" \
  "v_mfma_f32_32x32x16_bf16 v[80:95], v[248:251], v[212:215], v[80:95]\n" \
  "ds_read_b64_tr_b16 v[240:241], v6 offset:3136\n" \
  "ds_read_b64_tr_b16 v[242:243], v6 offset:3904\n" \
  "s_waitcnt lgkmcnt(8)\n" \
  "v_mfma_f32_32x32x16_bf16 v[48:63], v[232:235], v[208:211], v[48:63]\n" \
  "v_mfma_f32_32x32x16_bf16 v[96:111], v[232:235], v[212:215], v[96:111]\n" \
  "s_waitcnt lgkmcnt(5)\n" \
  "v_mfma_f32_32x32x16_bf16 v[16:31], v[236:239], v[224:227], v[16:31]\n" \
  "ds_read_b64_tr_b16 v[244:245], v6 offset:12352\n" \
  "ds_read_b64_tr_b16 v[246:247], v6 offset:13120\n" \
  "s_waitcnt lgkmcnt(6)\n" \
  "v_mfma_f32_32x32x16_bf16 v[64:79], v[236:239], v[228:231], v[64:79]\n" \
  "ds_read_b128 v[208:211], v0 offset:49152\n" \
  "ds_read_b128 v[212:215], v0 offset:53248\n" \
  "s_waitcnt lgkmcnt(6)\n" \
  "v_mfma_f32_32x32x16_bf16 v[32:47], v[252:255], v[224:227], v[32:47]\n" \
  "ds_read_b64_tr_b16 v[248:249], v6 offset:12384\n" \
  "ds_read_b64_tr_b16 v[250:251], v6 offset:13152\n" \
  "v_mfma_f32_32x32x16_bf16 v[80:95], v[252:255], v[228:231], v[80:95]\n" \
  "ds_read_b64_tr_b16 v[232:233], v6 offset:12416\n" \
  "ds_read_b64_tr_b16 v[234:235], v6 offset:13184\n" \
  "s_waitcnt lgkmcnt(8)\n" \
  "v_mfma_f32_32x32x16_bf16 v[48:63], v[240:243], v[224:227], v[48:63]\n" \
  "v_mfma_f32_32x32x16_bf16 v[96:111], v[240:243], v[228:231], v[96:111]\n" \
  "s_waitcnt lgkmcnt(5)\n" \
  "v_mfma_f32_32x32x16_bf16 v[16:31], v[244:247], v[208:211], v[16:31]\n" \
  "ds_read_b64_tr_b16 v[236:237], v6 offset:15424\n" \
  "ds_read_b64_tr_b16 v[238:239], v6 offset:16192\n" \
  "s_waitcnt lgkmcnt(6)\n" \
  "v_mfma_f32_32x32x16_bf16 v[64:79], v[244:247], v[212:215], v[64:79]\n" \
  "ds_read_b128 v[224:227], v1 offset:49152\n" \
  "ds_read_b128 v[228:231], v1 offset:53248\n" \
  "s_waitcnt lgkmcnt(6)\n" \
  "v_mfma_f32_32x32x16_bf16 v[32:47], v[248:251], v[208:211], v[32:47]\n" \
  "ds_read_b64_tr_b16 v[252:253], v6 offset:15456\n" \
  "ds_read_b64_tr_b16 v[254:255], v6 offset:16224\n" \
  "v_mfma_f32_32x32x16_bf16 v[80:95], v[248:251], v[212:215], v[80:95]\n" \
  "ds_read_b64_tr_b16 v[240:241], v6 offset:15488\n" \
  "ds_read_b64_tr_b16 v[242:243], v6 offset:16256\n" \
  "s_waitcnt lgkmcnt(8)\n" \
  "v_mfma_f32_32x32x16_bf16 v[48:63], v[232:235], v[208:211], v[48:63]\n" \
  "v_mfma_f32_32x32x16_bf16 v[96:111], v[232:235], v[212:215], v[96:111]\n" \
  "s_waitcnt lgkmcnt(5)\n" \
  "v_mfma_f32_32x32x16_bf16 v[16:31], v[236:239], v[224:227], v[16:31]\n" \
  "s_waitcnt vmcnt(0)\n" \
  "s_waitcnt lgkmcnt(0)\n" \
  "s_barrier\n" \
  "ds_read_b64_tr_b16 v[244:245], v9 offset:0\n" \
  "ds_read_b64_tr_b16 v[246:247], v9 offset:768\n" \
  "v_mfma_f32_32x32x16_bf16 v[64:79], v[236:239], v[228:231], v[64:79]\n" \
  "ds_read_b128 v[208:211], v2 offset:49152\n" \
  "ds_read_b128 v[212:215], v2 offset:53248\n" \
  "v_mfma_f32_32x32x16_bf16 v[32:47], v[252:255], v[224:227], v[32:47]\n" \
  "ds_read_b64_tr_b16 v[248:249], v9 offset:32\n" \
  "ds_read_b64_tr_b16 v[250:251], v9 offset:800\n" \
  "v_mfma_f32_32x32x16_bf16 v[80:95], v[252:255], v[228:231], v[80:95]\n" \
  "ds_read_b64_tr_b16 v[232:233], v9 offset:64\n" \
  "ds_read_b64_tr_b16 v[234:235], v9 offset:832\n" \
  "v_mfma_f32_32x32x16_bf16 v[48:63], v[240:243], v[224:227], v[48:63]\n" \
  "v_mfma_f32_32x32x16_bf16 v[96:111], v[240:243], v[228:231], v[96:111]\n" \
  "s_waitcnt lgkmcnt(5)\n" \
  "v_mfma_f32_32x32x16_bf16 v[16:31], v[244:247], v[208:211], v[16:31]\n" \
  "ds_read_b64_tr_b16 v[236:237], v9 offset:3072\n" \
  "ds_read_b64_tr_b16 v[238:239], v9 offset:3840\n" \
  "s_waitcnt lgkmcnt(6)\n" \
  "v_mfma_f32_32x32x16_bf16 v[64:79], v[244:247], v[212:215], v[64:79]\n" \
  "ds_read_b128 v[224:227], v3 offset:49152\n" \
  "ds_read_b128 v[228:231], v3 offset:53248\n" \
  "s_waitcnt lgkmcnt(6)\n" \
  "v_mfma_f32_32x32x16_bf16 v[32:47], v[248:251], v[208:211], v[32:47]\n" \
  "ds_read_b64_tr_b16 v[252:253], v9 offset:3104\n" \
  "ds_read_b64_tr_b16 v[254:255], v9 offset:3872\n" \
  "v_mfma_f32_32x32x16_bf16 v[80:95], v[248:251], v[212:215], v[80:95]\n" \
  "ds_read_b64_tr_b16 v[240:241], v9 offset:3136\n" \
  "ds_read_b64_tr_b16 v[242:243], v9 offset:3904\n" \
  "s_waitcnt lgkmcnt(8)\n" \
  "v_mfma_f32_32x32x16_bf16 v[48:63], v[232:235], v[208:211], v[48:63]\n" \
  "v_mfma_f32_32x32x16_bf16 v[96:111], v[232:235], v[212:215], v[96:111]\n" \
  "s_waitcnt lgkmcnt(5)\n" \
  "v_mfma_f32_32x32x16_bf16 v[16:31], v[236:239], v[224:227], v[16:31]\n" \
  "ds_read_b64_tr_b16 v[244:245], v9 offset:12352\n" \
  "ds_read_b64_tr_b16 v[246:247], v9 offset:13120\n" \
  "s_waitcnt lgkmcnt(6)\n" \
  "v_mfma_f32_32x32x16_bf16 v[64:79], v[236:239], v[228:231], v[64:79]\n" \
  "ds_read_b128 v[208:211], v0 offset:0\n" \
  "ds_read_b128 v[212:215], v0 offset:4096\n" \
  "s_waitcnt lgkmcnt(6)\n" \
  "v_mfma_f32_32x32x16_bf16 v[32:47], v[252:255], v[224:227], v[32:47]\n" \
  "ds_read_b64_tr_b16 v[248:249], v9 offset:12384\n" \
  "ds_read_b64_tr_b16 v[250:251], v9 offset:13152\n" \
  "v_mfma_f32_32x32x16_bf16 v[80:95], v[252:255], v[228:231], v[80:95]\n" \
  "ds_read_b64_tr_b16 v[232:233], v9 offset:12416\n" \
  "ds_read_b64_tr_b16 v[234:235], v9 offset:13184\n" \
  "s_waitcnt lgkmcnt(8)\n" \
  "v_mfma_f32_32x32x16_bf16 v[48:63], v[240:243], v[224:227], v[48:63]\n" \
  "v_mfma_f32_32x32x16_bf16 v[96:111], v[240:243], v[228:231], v[96:111]\n" \
  "s_waitcnt lgkmcnt(5)\n" \
  "v_mfma_f32_32x32x16_bf16 v[16:31], v[244:247], v[208:211], v[16:31]\n" \
  "ds_read_b64_tr_b16 v[236:237], v9 offset:15424\n" \
  "ds_read_b64_tr_b16 v[238:239], v9 offset:16192\n" \
  "s_waitcnt lgkmcnt(6)\n" \
  "v_mfma_f32_32x32x16_bf16 v[64:79], v[244:247], v[212:215], v[64:79]\n" \
  "ds_read_b128 v[224:227], v1 offset:0\n" \
  "ds_read_b128 v[228:231], v1 offset:4096\n" \
  "s_waitcnt lgkmcnt(6)\n" \
  "v_mfma_f32_32x32x16_bf16 v[32:47], v[248:251], v[208:211], v[32:47]\n" \
  "ds_read_b64_tr_b16 v[252:253], v9 offset:15456\n" \
  "ds_read_b64_tr_b16 v[254:255], v9 offset:16224\n" \
  "v_mfma_f32_32x32x16_bf16 v[80:95], v[248:251], v[212:215], v[80:95]\n" \
  "ds_read_b64_tr_b16 v[240:241], v9 offset:15488\n" \
  "ds_read_b64_tr_b16 v[242:243], v9 offset:16256\n" \
  "s_waitcnt lgkmcnt(8)\n" \
  "v_mfma_f32_32x32x16_bf16 v[48:63], v[232:235], v[208:211], v[48:63]\n" \
  "v_mfma_f32_32x32x16_bf16 v[96:111], v[232:235], v[212:215], v[96:111]\n" \
  "s_waitcnt lgkmcnt(5)\n" \
  "v_mfma_f32_32x32x16_bf16 v[16:31], v[236:239], v[224:227], v[16:31]\n" \
  "s_barrier\n" \
  "ds_read_b64_tr_b16 v[244:245], v6 offset:24704\n" \
  "ds_read_b64_tr_b16 v[246:247], v6 offset:25472\n" \
  "s_waitcnt lgkmcnt(6)\n" \
  "v_mfma_f32_32x32x16_bf16 v[64:79], v[236:239], v[228:231], v[64:79]\n" \
  "ds_read_b128 v[208:211], v2 offset:0\n" \
  "ds_read_b128 v[212:215], v2 offset:4096\n" \
  "s_waitcnt lgkmcnt(6)\n" \
  "v_mfma_f32_32x32x16_bf16 v[32:47], v[252:255], v[224:227], v[32:47]\n" \
  "ds_read_b64_tr_b16 v[248:249], v6 offset:24736\n" \
  "ds_read_b64_tr_b16 v[250:251], v6 offset:25504\n" \
  "v_mfma_f32_32x32x16_bf16 v[80:95], v[252:255], v[228:231], v[80:95]\n" \
  "ds_read_b64_tr_b16 v[232:233], v6 offset:24768\n" \
  "ds_read_b64_tr_b16 v[234:235], v6 offset:25536\n" \
  "s_waitcnt lgkmcnt(8)\n" \
  "v_mfma_f32_32x32x16_bf16 v[48:63], v[240:243], v[224:227], v[48:63]\n" \
  "v_mfma_f32_32x32x16_bf16 v[96:111], v[240:243], v[228:231], v[96:111]\n" \
  "s_waitcnt lgkmcnt(5)\n" \
  "v_mfma_f32_32x32x16_bf16 v[16:31], v[244:247], v[208:211], v[16:31]\n" \
  "ds_read_b64_tr_b16 v[236:237], v6 offset:27776\n" \
  "ds_read_b64_tr_b16 v[238:239], v6 offset:28544\n" \
  "s_waitcnt lgkmcnt(6)\n" \
  "v_mfma_f32_32x32x16_bf16 v[64:79], v[244:247], v[212:215], v[64:79]\n" \
  "ds_read_b128 v[224:227], v3 offset:0\n" \
  "ds_read_b128 v[228:231], v3 offset:4096\n" \
  "s_waitcnt lgkmcnt(6)\n" \
  "v_mfma_f32_32x32x16_bf16 v[32:47], v[248:251], v[208:211], v[32:47]\n" \
  "ds_read_b64_tr_b16 v[252:253], v6 offset:27808\n" \
  "ds_read_b64_tr_b16 v[254:255], v6 offset:28576\n" \
  "v_mfma_f32_32x32x16_bf16 v[80:95], v[248:251], v[212:215], v[80:95]\n" \
  "ds_read_b64_tr_b16 v[240:241], v6 offset:27840\n" \
  "ds_read_b64_tr_b16 v[242:243], v6 offset:28608\n" \
  "s_waitcnt lgkmcnt(8)\n" \
  "v_mfma_f32_32x32x16_bf16 v[48:63], v[232:235], v[208:211], v[48:63]\n" \
  "v_mfma_f32_32x32x16_bf16 v[96:111], v[232:235], v[212:215], v[96:111]\n" \
  "ds_read_b64 v[216:217], v10 offset:0\n" \
  "ds_read_b64 v[218:219], v10 offset:128\n" \
  "ds_read_b64 v[4:5], v10 offset:256\n" \
  "v_mul_f32 v14, v14, v14\n" \
  "v_mul_f32 v15, v15, v15\n" \
  "s_waitcnt lgkmcnt(8)\n" \
  "v_mfma_f32_32x32x16_bf16 v[16:31], v[236:239], v[224:227], v[16:31]\n" \
  "s_waitcnt lgkmcnt(0)\n" \
  "s_barrier\n" \
  "ds_read_b64_tr_b16 v[244:245], v6 offset:37056\n" \
  "ds_read_b64_tr_b16 v[246:247], v6 offset:37824\n" \
  "v_mfma_f32_32x32x16_bf16 v[64:79], v[236:239], v[228:231], v[64:79]\n" \
  "ds_read_b128 v[208:211], v0 offset:16384\n" \
  "ds_read_b128 v[212:215], v0 offset:20480\n" \
  "v_mfma_f32_32x32x16_bf16 v[32:47], v[252:255], v[224:227], v[32:47]\n" \
  "ds_read_b64_tr_b16 v[248:249], v6 offset:37088\n" \
  "ds_read_b64_tr_b16 v[250:251], v6 offset:37856\n" \
  "v_mfma_f32_32x32x16_bf16 v[80:95], v[252:255], v[228:231], v[80:95]\n" \
  "ds_read_b64_tr_b16 v[232:233], v6 offset:37120\n" \
  "ds_read_b64_tr_b16 v[234:235], v6 offset:37888\n" \
  "v_mfma_f32_32x32x16_bf16 v[48:63], v[240:243], v[224:227], v[48:63]\n" \
  "v_mfma_f32_32x32x16_bf16 v[96:111], v[240:243], v[228:231], v[96:111]\n" \
  "v_mfma_f32_32x32x8_bf16 v[16:31], v[216:217], v[220:221], v[16:31]\n" \
  "v_mfma_f32_32x32x8_bf16 v[64:79], v[216:217], v[222:223], v[64:79]\n" \
  "v_mfma_f32_32x32x8_bf16 v[32:47], v[218:219], v[220:221], v[32:47]\n" \
  "v_mfma_f32_32x32x8_bf16 v[80:95], v[218:219], v[222:223], v[80:95]\n" \
  "v_mfma_f32_32x32x8_bf16 v[48:63], v[4:5], v[220:221], v[48:63]\n" \
  "v_mfma_f32_32x32x8_bf16 v[96:111], v[4:5], v[222:223], v[96:111]\n" \
  "s_waitcnt lgkmcnt(5)\n" \
  "v_mfma_f32_32x32x16_bf16 v[16:31], v[244:247], v[208:211], v[16:31]\n" \
  "ds_read_b64_tr_b16 v[236:237], v6 offset:40128\n" \
  "ds_read_b64_tr_b16 v[238:239], v6 offset:40896\n" \
  "s_waitcnt lgkmcnt(6)\n" \
  "v_mfma_f32_32x32x16_bf16 v[64:79], v[244:247], v[212:215], v[64:79]\n" \
  "ds_read_b128 v[224:227], v1 offset:16384\n" \
  "ds_read_b128 v[228:231], v1 offset:20480\n" \
  "s_waitcnt lgkmcnt(6)\n" \
  "v_mfma_f32_32x32x16_bf16 v[32:47], v[248:251], v[208:211], v[32:47]\n" \
  "ds_read_b64_tr_b16 v[252:253], v6 offset:40160\n" \
  "ds_read_b64_tr_b16 v[254:255], v6 offset:40928\n" \
  "v_mfma_f32_32x32x16_bf16 v[80:95], v[248:251], v[212:215], v[80:95]\n" \
  "ds_read_b64_tr_b16 v[240:241], v6 offset:40192\n" \
  "ds_read_b64_tr_b16 v[242:243], v6 offset:40960\n" \
  "s_waitcnt lgkmcnt(8)\n" \
  "v_mfma_f32_32x32x16_bf16 v[48:63], v[232:235], v[208:211], v[48:63]\n" \
  "v_mfma_f32_32x32x16_bf16 v[96:111], v[232:235], v[212:215], v[96:111]\n" \
  "s_waitcnt lgkmcnt(5)\n" \
  "v_mfma_f32_32x32x16_bf16 v[16:31], v[236:239], v[224:227], v[16:31]\n" \
  "ds_read_b64_tr_b16 v[244:245], v6 offset:49408\n" \
  "ds_read_b64_tr_b16 v[246:247], v6 offset:50176\n" \
  "s_waitcnt lgkmcnt(6)\n" \
  "v_mfma_f32_32x32x16_bf16 v[64:79], v[236:239], v[228:231], v[64:79]\n" \
  "ds_read_b128 v[208:211], v2 offset:16384\n" \
  "ds_read_b128 v[212:215], v2 offset:20480\n" \
  "s_waitcnt lgkmcnt(6)\n" \
  "v_mfma_f32_32x32x16_bf16 v[32:47], v[252:255], v[224:227], v[32:47]\n" \
  "ds_read_b64_tr_b16 v[248:249], v6 offset:49440\n" \
  "ds_read_b64_tr_b16 v[250:251], v6 offset:50208\n" \
  "v_mfma_f32_32x32x16_bf16 v[80:95], v[252:255], v[228:231], v[80:95]\n" \
  "ds_read_b64_tr_b16 v[232:233], v6 offset:49472\n" \
  "ds_read_b64_tr_b16 v[234:235], v6 offset:50240\n" \
  "s_waitcnt lgkmcnt(8)\n" \
  "v_mfma_f32_32x32x16_bf16 v[48:63], v[240:243], v[224:227], v[48:63]\n" \
  "v_mfma_f32_32x32x16_bf16 v[96:111], v[240:243], v[228:231], v[96:111]\n" \
  "s_waitcnt lgkmcnt(5)\n" \
  "v_mfma_f32_32x32x16_bf16 v[16:31], v[244:247], v[208:211], v[16:31]\n" \
  "ds_read_b64_tr_b16 v[236:237], v6 offset:52480\n" \
  "ds_read_b64_tr_b16 v[238:239], v6 offset:53248\n" \
  "s_waitcnt lgkmcnt(6)\n" \
  "v_mfma_f32_32x32x16_bf16 v[64:79], v[244:247], v[212:215], v[64:79]\n" \
  "ds_read_b128 v[224:227], v3 offset:16384\n" \
  "ds_read_b128 v[228:231], v3 offset:20480\n" \
  "s_waitcnt lgkmcnt(6)\n" \
  "v_mfma_f32_32x32x16_bf16 v[32:47], v[248:251], v[208:211], v[32:47]\n" \
  "ds_read_b64_tr_b16 v[252:253], v6 offset:52512\n" \
  "ds_read_b64_tr_b16 v[254:255], v6 offset:53280\n" \
  "v_mfma_f32_32x32x16_bf16 v[80:95], v[248:251], v[212:215], v[80:95]\n" \
  "ds_read_b64_tr_b16 v[240:241], v6 offset:52544\n" \
  "ds_read_b64_tr_b16 v[242:243], v6 offset:53312\n" \
  "s_waitcnt lgkmcnt(8)\n" \
  "v_mfma_f32_32x32x16_bf16 v[48:63], v[232:235], v[208:211], v[48:63]\n" \
  "v_mfma_f32_32x32x16_bf16 v[96:111], v[232:235], v[212:215], v[96:111]\n" \
  "s_waitcnt lgkmcnt(5)\n" \
  "v_mfma_f32_32x32x16_bf16 v[16:31], v[236:239], v[224:227], v[16:31]\n" \
  "s_waitcnt lgkmcnt(4)\n" \
  "v_mfma_f32_32x32x16_bf16 v[64:79], v[236:239], v[228:231], v[64:79]\n" \
  "s_waitcnt lgkmcnt(2)\n" \
  "v_mfma_f32_32x32x16_bf16 v[32:47], v[252:255], v[224:227], v[32:47]\n" \
  "v_mfma_f32_32x32x16_bf16 v[80:95], v[252:255], v[228:231], v[80:95]\n" \
  "s_waitcnt lgkmcnt(0)\n" \
  "v_mfma_f32_32x32x16_bf16 v[48:63], v[240:243], v[224:227], v[48:63]\n" \
  "v_mfma_f32_32x32x16_bf16 v[96:111], v[240:243], v[228:231], v[96:111]\n" \
  "s_nop 7\n" \
  "v_pk_mul_f32 v[16:17], v[16:17], v[14:15] op_sel:[0,0] op_sel_hi:[1,0]\n" \
  "v_pk_mul_f32 v[18:19], v[18:19], v[14:15] op_sel:[0,0] op_sel_hi:[1,0]\n" \
  "v_pk_mul_f32 v[20:21], v[20:21], v[14:15] op_sel:[0,0] op_sel_hi:[1,0]\n" \
  "v_pk_mul_f32 v[22:23], v[22:23], v[14:15] op_sel:[0,0] op_sel_hi:[1,0]\n" \
  "v_pk_mul_f32 v[16:17], v[16:17], v[24:25]\n" \
  "v_pk_mul_f32 v[18:19], v[18:19], v[26:27]\n" \
  "v_pk_mul_f32 v[20:21], v[20:21], v[28:29]\n" \
  "v_pk_mul_f32 v[22:23], v[22:23], v[30:31]\n" \
  "v_cvt_pk_bf16_f32 v164, v16, v17\n" \
  "v_cvt_pk_bf16_f32 v165, v18, v19\n" \
  "v_cvt_pk_bf16_f32 v166, v20, v21\n" \
  "v_cvt_pk_bf16_f32 v167, v22, v23\n" \
  "v_pk_mul_f32 v[64:65], v[64:65], v[14:15] op_sel:[0,1] op_sel_hi:[1,1]\n" \
  "v_pk_mul_f32 v[66:67], v[66:67], v[14:15] op_sel:[0,1] op_sel_hi:[1,1]\n" \
  "v_pk_mul_f32 v[68:69], v[68:69], v[14:15] op_sel:[0,1] op_sel_hi:[1,1]\n" \
  "v_pk_mul_f32 v[70:71], v[70:71], v[14:15] op_sel:[0,1] op_sel_hi:[1,1]\n" \
  "v_pk_mul_f32 v[64:65], v[64:65], v[72:73]\n" \
  "v_pk_mul_f32 v[66:67], v[66:67], v[74:75]\n" \
  "v_pk_mul_f32 v[68:69], v[68:69], v[76:77]\n" \
  "v_pk_mul_f32 v[70:71], v[70:71], v[78:79]\n" \
  "v_cvt_pk_bf16_f32 v176, v64, v65\n" \
  "v_cvt_pk_bf16_f32 v177, v66, v67\n" \
  "v_cvt_pk_bf16_f32 v178, v68, v69\n" \
  "v_cvt_pk_bf16_f32 v179, v70, v71\n" \
  "v_pk_mul_f32 v[32:33], v[32:33], v[14:15] op_sel:[0,0] op_sel_hi:[1,0]\n" \
  "v_pk_mul_f32 v[34:35], v[34:35], v[14:15] op_sel:[0,0] op_sel_hi:[1,0]\n" \
  "v_pk_mul_f32 v[36:37], v[36:37], v[14:15] op_sel:[0,0] op_sel_hi:[1,0]\n" \
  "v_pk_mul_f32 v[38:39], v[38:39], v[14:15] op_sel:[0,0] op_sel_hi:[1,0]\n" \
  "v_pk_mul_f32 v[32:33], v[32:33], v[40:41]\n" \
  "v_pk_mul_f32 v[34:35], v[34:35], v[42:43]\n" \
  "v_pk_mul_f32 v[36:37], v[36:37], v[44:45]\n" \
  "v_pk_mul_f32 v[38:39], v[38:39], v[46:47]\n" \
  "v_cvt_pk_bf16_f32 v168, v32, v33\n" \
  "v_cvt_pk_bf16_f32 v169, v34, v35\n" \
  "v_cvt_pk_bf16_f32 v170, v36, v37\n" \
  "v_cvt_pk_bf16_f32 v171, v38, v39\n" \
  "v_pk_mul_f32 v[80:81], v[80:81], v[14:15] op_sel:[0,1] op_sel_hi:[1,1]\n" \
  "v_pk_mul_f32 v[82:83], v[82:83], v[14:15] op_sel:[0,1] op_sel_hi:[1,1]\n" \
  "v_pk_mul_f32 v[84:85], v[84:85], v[14:15] op_sel:[0,1] op_sel_hi:[1,1]\n" \
  "v_pk_mul_f32 v[86:87], v[86:87], v[14:15] op_sel:[0,1] op_sel_hi:[1,1]\n" \
  "v_pk_mul_f32 v[80:81], v[80:81], v[88:89]\n" \
  "v_pk_mul_f32 v[82:83], v[82:83], v[90:91]\n" \
  "v_pk_mul_f32 v[84:85], v[84:85], v[92:93]\n" \
  "v_pk_mul_f32 v[86:87], v[86:87], v[94:95]\n" \
  "v_cvt_pk_bf16_f32 v180, v80, v81\n" \
  "v_cvt_pk_bf16_f32 v181, v82, v83\n" \
  "v_cvt_pk_bf16_f32 v182, v84, v85\n" \
  "v_cvt_pk_bf16_f32 v183, v86, v87\n" \
  "v_pk_mul_f32 v[48:49], v[48:49], v[14:15] op_sel:[0,0] op_sel_hi:[1,0]\n" \
  "v_pk_mul_f32 v[50:51], v[50:51], v[14:15] op_sel:[0,0] op_sel_hi:[1,0]\n" \
  "v_pk_mul_f32 v[52:53], v[52:53], v[14:15] op_sel:[0,0] op_sel_hi:[1,0]\n" \
  "v_pk_mul_f32 v[54:55], v[54:55], v[14:15] op_sel:[0,0] op_sel_hi:[1,0]\n" \
  "v_pk_mul_f32 v[48:49], v[48:49], v[56:57]\n" \
  "v_pk_mul_f32 v[50:51], v[50:51], v[58:59]\n" \
  "v_pk_mul_f32 v[52:53], v[52:53], v[60:61]\n" \
  "v_pk_mul_f32 v[54:55], v[54:55], v[62:63]\n" \
  "v_cvt_pk_bf16_f32 v172, v48, v49\n" \
  "v_cvt_pk_bf16_f32 v173, v50, v51\n" \
  "v_cvt_pk_bf16_f32 v174, v52, v53\n" \
  "v_cvt_pk_bf16_f32 v175, v54, v55\n" \
  "v_pk_mul_f32 v[96:97], v[96:97], v[14:15] op_sel:[0,1] op_sel_hi:[1,1]\n" \
  "v_pk_mul_f32 v[98:99], v[98:99], v[14:15] op_sel:[0,1] op_sel_hi:[1,1]\n" \
  "v_pk_mul_f32 v[100:101], v[100:101], v[14:15] op_sel:[0,1] op_sel_hi:[1,1]\n" \
  "v_pk_mul_f32 v[102:103], v[102:103], v[14:15] op_sel:[0,1] op_sel_hi:[1,1]\n" \
  "v_pk_mul_f32 v[96:97], v[96:97], v[104:105]\n" \
  "v_pk_mul_f32 v[98:99], v[98:99], v[106:107]\n" \
  "v_pk_mul_f32 v[100:101], v[100:101], v[108:109]\n" \
  "v_pk_mul_f32 v[102:103], v[102:103], v[110:111]\n" \
  "v_cvt_pk_bf16_f32 v184, v96, v97\n" \
  "v_cvt_pk_bf16_f32 v185, v98, v99\n" \
  "v_cvt_pk_bf16_f32 v186, v100, v101\n" \
  "v_cvt_pk_bf16_f32 v187, v102, v103\n"
#define P1_CLOB "v16", "v17", "v18", "v19", "v20", "v21", "v22", "v23", "v24", "v25", "v26", "v27", "v28", "v29", "v30", "v31", "v32", "v33", "v34", "v35", "v36", "v37", "v38", "v39", "v40", "v41", "v42", "v43", "v44", "v45", "v46", "v47", "v48", "v49", "v50", "v51", "v52", "v53", "v54", "v55", "v56", "v57", "v58", "v59", "v60", "v61", "v62", "v63", "v64", "v65", "v66", "v67", "v68", "v69", "v70", "v71", "v72", "v73", "v74", "v75", "v76", "v77", "v78", "v79", "v80", "v81", "v82", "v83", "v84", "v85", "v86", "v87", "v88", "v89", "v90", "v91", "v92", "v93", "v94", "v95", "v96", "v97", "v98", "v99", "v100", "v101", "v102", "v103", "v104", "v105", "v106", "v107", "v108", "v109", "v110", "v111", "v112", "v113", "v114", "v115", "v116", "v117", "v118", "v119", "v120", "v121", "v122", "v123", "v124", "v125", "v126", "v127", "v128", "v129", "v130", "v131", "v132", "v133", "v134", "v135", "v136", "v137", "v138", "v139", "v140", "v141", "v142", "v143", "v144", "v145", "v146", "v147", "v148", "v149", "v150", "v151", "v152", "v153", "v154", "v155", "v156", "v157", "v158", "v159", "v160", "v161", "v162", "v163", "v196", "v197", "v198", "v199", "v200", "v201", "v202", "v203", "v204", "v205", "v206", "v207", "v208", "v209", "v210", "v211", "v212", "v213", "v214", "v215", "v216", "v217", "v218", "v219", "v220", "v221", "v222", "v223", "v224", "v225", "v226", "v227", "v228", "v229", "v230", "v231", "v232", "v233", "v234", "v235", "v236", "v237", "v238", "v239", "v240", "v241", "v242", "v243", "v244", "v245", "v246", "v247", "v248", "v249", "v250", "v251", "v252", "v253", "v254", "v255", "s56", "s57", "vcc", "scc", "memory"
#define P1_FIRST "s_setprio 3\n" "s_add_u32 m0, %24, 0\n" "s_add_u32 %4, %21, 0\n" "buffer_load_dwordx4 %12, %18, %4 offen lds\n" "s_add_u32 m0, %24, 1024\n" "s_add_u32 %4, %21, 0\n" "buffer_load_dwordx4 %13, %18, %4 offen lds\n" "s_add_u32 m0, %24, 8192\n" "s_add_u32 %4, %21, 81920\n" "buffer_load_dwordx4 %12, %18, %4 offen lds\n" "s_add_u32 m0, %24, 9216\n" "s_add_u32 %4, %21, 81920\n" "buffer_load_dwordx4 %13, %18, %4 offen lds\n" "s_add_u32 m0, %24, 65536\n" "s_add_u32 %4, %22, 0\n" "buffer_load_dwordx4 %14, %19, %4 offen lds\n" "s_add_u32 m0, %24, 66560\n" "s_add_u32 %4, %22, 15360\n" "buffer_load_dwordx4 %14, %19, %4 offen lds\n" "s_add_u32 m0, %25, 123008\n" "s_add_u32 %4, %23, 0\n" "buffer_load_dwordx4 %15, %20, %4 offen lds\n" "s_add_u32 m0, %25, 124032\n" "s_add_u32 %4, %23, 0\n" "buffer_load_dwordx4 %16, %20, %4 offen lds\n" "s_add_u32 m0, %25, 125056\n" "s_add_u32 %4, %23, 0\n" "buffer_load_dwordx4 %17, %20, %4 offen lds\n"
#define P1_LOADER \
  { \
  SBAR(); \
  HOOK_SETUP1 \
  LT(0); \
  LQ(73728, 122880, 0); \
  LQ(74752, 138240, 0); \
  LG(135360, 98304, 0, 0); \
  LG(136384, 98304, 1, 0); \
  LG(137408, 98304, 2, 0); \
  LT(1); \
  } \
  HOOK_SETUP2 \
  HOOK_SETUP3 \
  vmwait<7>(); \
  bar_only(); \
  HOOK_AFTER_INIT \
  { \
  LX(16384, 128, 0, 0); \
  LX(17408, 128, 1, 0); \
  CSRQ(0, 0); \
  CSRG(1, 24704); \
  SBAR(); \
  LX(24576, 82048, 0, 0); \
  LX(25600, 82048, 1, 0); \
  CSMQ(0); \
  CSMG(1); \
  CSPIN \
  LQ(81920, 245760, 0); \
  LQ(82944, 261120, 0); \
  LG(147712, 196608, 0, 0); \
  LG(148736, 196608, 1, 0); \
  LG(149760, 196608, 2, 0); \
  } \
  LT(2); \
  vmwait<11>(); \
  bar_only(); \
  { \
  LQ(90112, 368640, 0); \
  LQ(91136, 384000, 0); \
  CSRQ(0, 8192); \
  CSRG(1, 37056); \
  SBAR(); \
  LG(98304, 294912, 0, 0); \
  LG(99328, 294912, 1, 0); \
  LG(100352, 294912, 2, 0); \
  CSMQ(0); \
  CSMG(1); \
  CSPIN \
  LX(32768, 256, 0, 0); \
  LX(33792, 256, 1, 0); \
  LX(40960, 82176, 0, 0); \
  LX(41984, 82176, 1, 0); \
  } \
  LT(3); \
  vmwait<11>(); \
  bar_only(); \
  { \
  LQ(65536, 491520, 0); \
  LQ(66560, 506880, 0); \
  CSRQ(0, 16384); \
  CSRG(1, 49408); \
  SBAR(); \
  LG(110656, 393216, 0, 0); \
  LG(111680, 393216, 1, 0); \
  LG(112704, 393216, 2, 0); \
  CSMQ(0); \
  CSMG(1); \
  CSPIN \
  LQ(73728, 614400, 0); \
  LQ(74752, 629760, 0); \
  LX(49152, 384, 0, 0); \
  LX(50176, 384, 1, 0); \
  } \
  vmwait<14>(); \
  bar_only(); \
  { \
  LG(123008, 491520, 0, 0); \
  LG(124032, 491520, 1, 0); \
  LG(125056, 491520, 2, 0); \
  CSRQ(0, 24576); \
  CSRG(1, 0); \
  SBAR(); \
  LX(57344, 82304, 0, 0); \
  LX(58368, 82304, 1, 0); \
  CSMQ(0); \
  CSMG(1); \
  CSPIN \
  LQ(81920, 737280, 0); \
  LQ(82944, 752640, 0); \
  LX(0, 512, 0, 0); \
  LX(1024, 512, 1, 0); \
  } \
  vmwait<13>(); \
  bar_only(); \
  { \
  LG(135360, 589824, 0, 0); \
  LG(136384, 589824, 1, 0); \
  LG(137408, 589824, 2, 0); \
  CSRQ(0, 0); \
  CSRG(1, 12352); \
  SBAR(); \
  LQ(90112, 860160, 0); \
  LQ(91136, 875520, 0); \
  CSMQ(0); \
  CSMG(1); \
  CSPIN \
  LG(147712, 688128, 0, 0); \
  LG(148736, 688128, 1, 0); \
  LG(149760, 688128, 2, 0); \
  } \
  vmwait<14>(); \
  bar_only(); \
  { \
  LX(8192, 82432, 0, 0); \
  LX(9216, 82432, 1, 0); \
  CSRQ(0, 8192); \
  CSRG(1, 24704); \
  SBAR(); \
  LQ(65536, 983040, 0); \
  LQ(66560, 998400, 0); \
  CSMQ(0); \
  CSMG(1); \
  CSPIN \
  LG(98304, 786432, 0, 0); \
  LG(99328, 786432, 1, 0); \
  LG(100352, 786432, 2, 0); \
  LX(16384, 640, 0, 1); \
  LX(17408, 640, 1, 1); \
  } \
  HOOK_PT_TOUCH(0, 5) \
  vmwait<14>(); \
  bar_only(); \
  { \
  LQ(73728, 1105920, 0); \
  LQ(74752, 1121280, 0); \
  CSRQ(0, 16384); \
  CSRG(1, 37056); \
  SBAR(); \
  LG(110656, 884736, 0, 0); \
  LG(111680, 884736, 1, 0); \
  LG(112704, 884736, 2, 0); \
  CSMQ(0); \
  CSMG(1); \
  CSPIN \
  LX(24576, 82560, 0, 1); \
  LX(25600, 82560, 1, 1); \
  LX(32768, 768, 0, 1); \
  LX(33792, 768, 1, 1); \
  } \
  HOOK_PT_TOUCH(1, 5) \
  vmwait<18>(); \
  bar_only(); \
  { \
  LQ(81920, 1228800, 1); \
  LQ(82944, 1244160, 1); \
  CSRQ(0, 24576); \
  CSRG(1, 49408); \
  SBAR(); \
  LX(40960, 82688, 0, 1); \
  LX(41984, 82688, 1, 1); \
  CSMQ(0); \
  CSMG(1); \
  CSPIN \
  LG(123008, 983040, 0, 1); \
  LG(124032, 983040, 1, 1); \
  LG(125056, 983040, 2, 1); \
  } \
  HOOK_PT_TOUCH(2, 5) \
  vmwait<18>(); \
  bar_only(); \
  { \
  LQ(90112, 1351680, 1); \
  LQ(91136, 1367040, 1); \
  CSRQ(0, 0); \
  CSRG(1, 0); \
  SBAR(); \
  LX(49152, 896, 0, 1); \
  LX(50176, 896, 1, 1); \
  CSMQ(0); \
  CSMG(1); \
  CSPIN \
  LX(57344, 82816, 0, 1); \
  LX(58368, 82816, 1, 1); \
  LG(135360, 1081344, 0, 1); \
  LG(136384, 1081344, 1, 1); \
  LG(137408, 1081344, 2, 1); \
  } \
  HOOK_PT_TOUCH(3, 5) \
  vmwait<20>(); \
  bar_only(); \
  { \
  LQ(65536, 1474560, 1); \
  LQ(66560, 1489920, 1); \
  CSRQ(0, 8192); \
  CSRG(1, 12352); \
  SBAR(); \
  LQ(98304, 1597440, 1); \
  LQ(99328, 1612800, 1); \
  CSMQ(0); \
  CSMG(1); \
  CSPIN \
  LG(147712, 1179648, 0, 1); \
  LG(148736, 1179648, 1, 1); \
  LG(149760, 1179648, 2, 1); \
  } \
  HOOK_STATS_LOAD \
  HOOK_PT_TOUCH(4, 5) \
  vmwait<15>(); \
  bar_only(); \
  { \
  LQ(73728, 1720320, 1); \
  LQ(74752, 1735680, 1); \
  SBAR(); \
  LQ(106496, 1843200, 1); \
  LQ(107520, 1858560, 1); \
  } \
  HOOK_CS_SEND \
  vmwait<10>(); \
  bar_only(); \
  { \
  LX(0, 1024, 0, 1); \
  LX(1024, 1024, 1, 1); \
  SBAR(); \
  LX(8192, 82944, 0, 1); \
  LX(9216, 82944, 1, 1); \
  LQ(114688, 1966080, 1); \
  LQ(115712, 1981440, 1); \
  LQ(81920, 2088960, 1); \
  LQ(82944, 2104320, 1); \
  } \
  vmwait<10>(); \
  bar_only(); \
  { \
  LX(16384, 1152, 0, 1); \
  LX(17408, 1152, 1, 1); \
  SBAR(); \
  LX(24576, 83072, 0, 1); \
  LX(25600, 83072, 1, 1); \
  LQ(90112, 2211840, 1); \
  LQ(91136, 2227200, 1); \
  LQ(65536, 2334720, 1); \
  LQ(66560, 2350080, 1); \
  } \
  HOOK_CS_RECV \
  vmwait<10>(); \
  bar_only(); \
  { \
  SBAR(); \
  } \
  HOOK_PT_LOAD \
  vmwait<29>(); \
  HOOK_STATS_USE \
  vmwait<7>(); \
  bar_only(); \
  { \
  SBAR(); \
  vmwait<5>(); \
  HOOK_CS_PUB \
  LG(98304, 1277952, 0, 1); \
  LG(99328, 1277952, 1, 1); \
  LG(100352, 1277952, 2, 1); \
  LG(110656, 1376256, 0, 1); \
  LG(111680, 1376256, 1, 1); \
  LG(112704, 1376256, 2, 1); \
  } \
  bar_only(); \
  { \
  LG(65536, 1474560, 0, 1); \
  LG(66560, 1474560, 1, 1); \
  LG(67584, 1474560, 2, 1); \
  LG(77888, 1572864, 0, 1); \
  LG(78912, 1572864, 1, 1); \
  LG(79936, 1572864, 2, 1); \
  SBAR(); \
  } \
  vmwait<6>(); \
  bar_only(); \
  { \
  SBAR(); \
  } \
  vmwait<0>(); \
  bar_only(); \
  HOOK_F1_SET \
  { \
  LG(123008, 1671168, 0, 1); \
  LG(124032, 1671168, 1, 1); \
  LG(125056, 1671168, 2, 1); \
  LG(135360, 1769472, 0, 1); \
  LG(136384, 1769472, 1, 1); \
  LG(137408, 1769472, 2, 1); \
  LG(147712, 1867776, 0, 1); \
  LG(148736, 1867776, 1, 1); \
  LG(149760, 1867776, 2, 1); \
  SBAR(); \
  } \
  HOOK_PARAMS \
  vmwait<6>(); \
  bar_only(); \
  { \
  SBAR(); \
  } \
  vmwait<0>(); \
  bar_only(); \
  HOOK_F1_LOAD

#define ATTN_ASM \
  "s_mov_b32 s60, 0xffffffff\n" \
  "s_mov_b32 s61, 0xff0fffff\n" \
  "s_mov_b32 s62, 0xffff0fff\n" \
  "s_mov_b32 s63, 0xfffff0f\n" \
  "v_mbcnt_lo_u32_b32 v1, -1, 0\n" \
  "v_mbcnt_hi_u32_b32 v1, -1, v1\n" \
  "v_lshlrev_b32 v0, 4, v1\n" \
  "v_and_b32 v14, 31, v1\n" \
  "v_lshrrev_b32 v15, 5, v1\n" \
  "s_and_b32 s56, s76, 8\n" \
  "v_lshl_add_u32 v9, v14, 4, s56\n" \
  "v_add_u32 v83, 1024, v9\n" \
  "s_mov_b32 exec_lo, 0\n" \
  "v_mov_b32 v83, 1856\n" \
  "s_mov_b32 exec_lo, -1\n" \
  "v_min_u32 v14, 21, v14\n" \
  "v_mul_u32_u24 v15, 384, v15\n" \
  "v_lshl_add_u32 v6, v14, 4, v15\n" \
  "s_add_u32 s56, s52, 1536\n" \
  "v_add_u32 v6, s56, v6\n" \
  "v_add_u32 v5, s52, v83\n" \
  "v_add_u32 v13, s52, v0\n" \
  "v_add_u32 v158, 40960, v13\n" \
  "v_add_u32 v159, 40960, v5\n" \
  "v_add_u32 v160, 40960, v6\n" \
  "s_cmp_lg_u32 s52, 0\n" \
  "s_cselect_b32 s57, 512, 0\n" \
  "s_lshl_b32 s56, s57, 1\n" \
  "s_add_u32 s58, s53, s56\n" \
  "s_sub_u32 s56, s53, s56\n" \
  "s_add_u32 s56, s56, 1024\n" \
  "v_add_u32 v81, s58, v0\n" \
  "v_add_u32 v80, s56, v0\n" \
  "s_add_u32 s58, s53, s57\n" \
  "s_sub_u32 s56, s53, s57\n" \
  "s_add_u32 s56, s56, 512\n" \
  "v_add_u32 v82, s58, v9\n" \
  "v_add_u32 v9, s56, v9\n" \
  "v_bfe_u32 v14, v1, 3, 2\n" \
  "v_add_u32 v15, 1, v14\n" \
  "v_and_b32 v15, 4, v15\n" \
  "v_mul_u32_u24 v14, 24576, v14\n" \
  "v_mul_u32_u24 v15, 36864, v15\n" \
  "v_sub_u32 v14, v14, v15\n" \
  "v_bfe_u32 v15, v1, 1, 2\n" \
  "v_mul_u32_u24 v15, 384, v15\n" \
  "v_and_b32 v8, 1, v1\n" \
  "v_lshl_add_u32 v15, v8, 4, v15\n" \
  "s_add_u32 s56, s42, 91968\n" \
  "v_add3_u32 v7, v14, v15, s56\n" \
  "v_add_u32 v8, -1, v8\n" \
  "v_and_b32 v136, 0x3f803f80, v8\n" \
  "v_and_b32 v137, 0x3f803f80, v8\n" \
  "v_and_b32 v138, 0x3f803f80, v8\n" \
  "v_and_b32 v139, 0x3f803f80, v8\n" \
  "v_mov_b32 v252, 0\n" \
  "v_mov_b32 v253, 0\n" \
  "v_mov_b32 v254, 0\n" \
  "v_mov_b32 v255, 0\n" \
  "v_mov_b32 v128, 0\n" \
  "v_mov_b32 v129, 0\n" \
  "v_mov_b32 v130, 0\n" \
  "v_mov_b32 v131, 0\n" \
  "v_and_b32 v10, 0x300, v0\n" \
  "v_add_u32 v10, v10, v0\n" \
  "v_mov_b32 v11, 0x7ffffff0\n" \
  "v_add_u32 v12, s51, v0\n" \
  "v_mov_b32 v161, 0xdddd\n" \
  "v_writelane_b32 v11, s50, 0\n" \
  "v_mfma_f32_32x32x16_bf16 v[16:31], v[128:131], v[128:131], 0\n" \
  "v_mfma_f32_32x32x16_bf16 v[32:47], v[128:131], v[128:131], 0\n" \
  "v_mov_b32 v163, s97\n" \
  "s_mov_b32 s57, s40\n" \
  "s_waitcnt vmcnt(0) lgkmcnt(0)\n" \
  "s_barrier\n" \
  "ds_read_b128 v[152:155], v0 offset:40960\n" \
  "ds_read_b128 v[244:247], v80\n" \
  "ds_read_b64 v[140:141], v83 offset:40960\n" \
  "s_mov_b32 exec_hi, 0\n" \
  "ds_read_b64 v[252:253], v9 offset:2048\n" \
  "ds_read_b64 v[254:255], v82 offset:2048\n" \
  "s_mov_b32 exec_hi, -1\n" \
  "ds_read_b128 v[248:251], v81\n" \
  "ds_read_b128 v[144:147], v158\n" \
  "ds_read_b96 v[2:4], v0 offset:13312\n" \
  "ds_read_b64 v[156:157], v159\n" \
  "s_mov_b32 exec_hi, 0\n" \
  "ds_write_b128 v7, v[136:139]\n" \
  "s_mov_b32 exec_hi, -1\n" \
  "ds_read_b128 v[148:151], v160\n" \
  "s_waitcnt lgkmcnt(9)\n" \
  "v_mfma_f32_32x32x16_bf16 v[48:63], v[152:155], v[244:247], 0\n" \
  "s_waitcnt lgkmcnt(7)\n" \
  "v_mfma_f32_32x32x8_bf16 v[48:63], v[140:141], v[252:253], v[48:63]\n" \
  "s_waitcnt lgkmcnt(5)\n" \
  "v_mfma_f32_32x32x16_bf16 v[64:79], v[152:155], v[248:251], 0\n" \
  "v_mfma_f32_32x32x8_bf16 v[64:79], v[140:141], v[254:255], v[64:79]\n" \
  "s_waitcnt lgkmcnt(3)\n" \
  "v_mfma_f32_32x32x16_bf16 v[96:111], v[144:147], v[244:247], 0\n" \
  "s_nop 6\n" \
  "v_max3_f32 v14, v48, v49, v50\n" \
  "v_max3_f32 v51, v51, v52, v53\n" \
  "v_max3_f32 v14, v14, v54, v55\n" \
  "v_max3_f32 v51, v51, v56, v57\n" \
  "v_max3_f32 v14, v14, v58, v59\n" \
  "v_max3_f32 v51, v51, v60, v61\n" \
  "v_max3_f32 v14, v14, v62, v63\n" \
  "s_mov_b32 exec_lo, 0\n" \
  "v_max_f32 v14, v14, v51\n" \
  "v_cvt_pk_bf16_f32 v252, -v14, 0\n" \
  "s_mov_b32 exec_lo, -1\n" \
  "s_waitcnt lgkmcnt(1)\n" \
  "v_mfma_f32_32x32x8_bf16 v[96:111], v[156:157], v[252:253], v[96:111]\n" \
  "v_max3_f32 v15, v64, v65, v66\n" \
  "v_max3_f32 v67, v67, v68, v69\n" \
  "v_max3_f32 v15, v15, v70, v71\n" \
  "v_max3_f32 v67, v67, v72, v73\n" \
  "v_max3_f32 v15, v15, v74, v75\n" \
  "v_max3_f32 v67, v67, v76, v77\n" \
  "v_max3_f32 v15, v15, v78, v79\n" \
  "s_mov_b32 exec_lo, 0\n" \
  "v_max_f32 v15, v15, v67\n" \
  "v_cvt_pk_bf16_f32 v254, -v15, 0\n" \
  "s_mov_b32 exec_lo, -1\n" \
  "v_mov_b32 v7, 1\n" \
  "s_setprio 3\n" \
  "v_mfma_f32_32x32x16_bf16 v[112:127], v[144:147], v[248:251], 0\n" \
  "ds_read_b128 v[144:147], v158 offset:3072\n" \
  "v_exp_f32 v128, v96\n" \
  "s_add_u32 m0, s42, 65536\n" \
  "s_nop 0\n" \
  "buffer_load_dwordx4 v2, %2, s57 offen sc1 lds\n" \
  "v_exp_f32 v129, v97\n" \
  "v_exp_f32 v130, v98\n" \
  "s_add_u32 m0, s42, 66560\n" \
  "s_mov_b64 exec, s[60:61]\n" \
  "buffer_load_dwordx4 v3, %2, s57 offen sc1 lds\n" \
  "s_mov_b64 exec, -1\n" \
  "v_exp_f32 v131, v99\n" \
  "ds_read_b128 v[152:155], v160 offset:768\n" \
  "v_exp_f32 v132, v100\n" \
  "s_add_u32 m0, s42, 67584\n" \
  "s_mov_b64 exec, s[62:63]\n" \
  "buffer_load_dwordx4 v4, %2, s57 offen sc1 lds\n" \
  "s_mov_b64 exec, -1\n" \
  "s_add_u32 s57, s57, 983040\n" \
  "s_cmp_ge_u32 s57, 9961472\n" \
  "s_cselect_b32 s56, 7864320, 0\n" \
  "s_sub_u32 s57, s57, s56\n" \
  "v_exp_f32 v133, v101\n" \
  "v_exp_f32 v134, v102\n" \
  "v_mfma_f32_32x32x8_bf16 v[112:127], v[156:157], v[254:255], v[112:127]\n" \
  "ds_read_b64 v[156:157], v159 offset:3072\n" \
  "s_add_u32 m0, s42, 90112\n" \
  "s_nop 0\n" \
  "buffer_load_dwordx4 v2, %2, s57 offen sc1 lds\n" \
  "v_exp_f32 v135, v103\n" \
  "v_exp_f32 v136, v104\n" \
  "s_add_u32 m0, s42, 91136\n" \
  "s_mov_b64 exec, s[60:61]\n" \
  "buffer_load_dwordx4 v3, %2, s57 offen sc1 lds\n" \
  "s_mov_b64 exec, -1\n" \
  "v_exp_f32 v137, v105\n" \
  "v_exp_f32 v138, v106\n" \
  "s_add_u32 m0, s42, 92160\n" \
  "s_mov_b64 exec, s[62:63]\n" \
  "buffer_load_dwordx4 v4, %2, s57 offen sc1 lds\n" \
  "s_mov_b64 exec, -1\n" \
  "s_add_u32 s57, s57, 983040\n" \
  "s_cmp_ge_u32 s57, 9961472\n" \
  "s_cselect_b32 s56, 7864320, 0\n" \
  "s_sub_u32 s57, s57, s56\n" \
  "v_exp_f32 v139, v107\n" \
  "s_waitcnt lgkmcnt(3)\n" \
  "v_smfmac_f32_32x32x32_bf16 v[16:31], v[148:151], v[128:135], v161\n" \
  "v_exp_f32 v140, v108\n" \
  "v_exp_f32 v141, v109\n" \
  "buffer_store_dwordx4 v[164:167], v12, %2, 0 offen offset:0 sc1\n" \
  "v_exp_f32 v142, v110\n" \
  "v_exp_f32 v143, v111\n" \
  "s_waitcnt lgkmcnt(0)\n" \
  "v_mfma_f32_32x32x16_bf16 v[96:111], v[144:147], v[244:247], 0\n" \
  "v_exp_f32 v128, v112\n" \
  "buffer_store_dwordx4 v[168:171], v12, %2, 0 offen offset:1024 sc1\n" \
  "v_exp_f32 v129, v113\n" \
  "v_exp_f32 v130, v114\n" \
  "v_exp_f32 v131, v115\n" \
  "v_smfmac_f32_32x32x32_bf16 v[16:31], v[152:155], v[136:143], v161\n" \
  "buffer_store_dwordx4 v[172:175], v12, %2, 0 offen offset:2048 sc1\n" \
  "v_exp_f32 v132, v116\n" \
  "v_exp_f32 v133, v117\n" \
  "v_exp_f32 v134, v118\n" \
  "v_mfma_f32_32x32x8_bf16 v[96:111], v[156:157], v[252:253], v[96:111]\n" \
  "buffer_store_dwordx4 v[176:179], v12, %2, s41 offen offset:0 sc1\n" \
  "v_exp_f32 v135, v119\n" \
  "v_exp_f32 v136, v120\n" \
  "v_exp_f32 v137, v121\n" \
  "buffer_store_dwordx4 v[180:183], v12, %2, s41 offen offset:1024 sc1\n" \
  "v_exp_f32 v138, v122\n" \
  "v_exp_f32 v139, v123\n" \
  "v_smfmac_f32_32x32x32_bf16 v[32:47], v[148:151], v[128:135], v161\n" \
  "ds_read_b128 v[148:151], v160 offset:3072\n" \
  "v_exp_f32 v140, v124\n" \
  "buffer_store_dwordx4 v[184:187], v12, %2, s41 offen offset:2048 sc1\n" \
  "v_exp_f32 v141, v125\n" \
  "v_exp_f32 v142, v126\n" \
  "v_exp_f32 v143, v127\n" \
  "s_setprio 2\n" \
  "v_mfma_f32_32x32x16_bf16 v[112:127], v[144:147], v[248:251], 0\n" \
  "ds_read_b128 v[144:147], v158 offset:6144\n" \
  "v_exp_f32 v128, v96\n" \
  "v_exp_f32 v129, v97\n" \
  "v_exp_f32 v130, v98\n" \
  "v_exp_f32 v131, v99\n" \
  "v_smfmac_f32_32x32x32_bf16 v[32:47], v[152:155], v[136:143], v161\n" \
  "ds_read_b128 v[152:155], v160 offset:3840\n" \
  "v_exp_f32 v132, v100\n" \
  "v_exp_f32 v133, v101\n" \
  "v_exp_f32 v134, v102\n" \
  "v_mfma_f32_32x32x8_bf16 v[112:127], v[156:157], v[254:255], v[112:127]\n" \
  "ds_read_b64 v[156:157], v159 offset:6144\n" \
  "v_exp_f32 v135, v103\n" \
  "v_exp_f32 v136, v104\n" \
  "v_exp_f32 v137, v105\n" \
  "v_exp_f32 v138, v106\n" \
  "v_exp_f32 v139, v107\n" \
  "s_waitcnt lgkmcnt(3)\n" \
  "v_smfmac_f32_32x32x32_bf16 v[16:31], v[148:151], v[128:135], v161\n" \
  "v_exp_f32 v140, v108\n" \
  "v_exp_f32 v141, v109\n" \
  "s_waitcnt vmcnt(9)\n" \
  "s_mov_b64 exec, 1\n" \
  "ds_add_u32 v0, v7 offset:42848\n" \
  "s_mov_b64 exec, -1\n" \
  "v_exp_f32 v142, v110\n" \
  "v_exp_f32 v143, v111\n" \
  "s_waitcnt lgkmcnt(0)\n" \
  "v_mfma_f32_32x32x16_bf16 v[96:111], v[144:147], v[244:247], 0\n" \
  "v_exp_f32 v128, v112\n" \
  "v_exp_f32 v129, v113\n" \
  "v_exp_f32 v130, v114\n" \
  "v_exp_f32 v131, v115\n" \
  "v_smfmac_f32_32x32x32_bf16 v[16:31], v[152:155], v[136:143], v161\n" \
  "v_exp_f32 v132, v116\n" \
  "v_exp_f32 v133, v117\n" \
  "v_exp_f32 v134, v118\n" \
  "v_mfma_f32_32x32x8_bf16 v[96:111], v[156:157], v[252:253], v[96:111]\n" \
  "v_exp_f32 v135, v119\n" \
  "v_exp_f32 v136, v120\n" \
  "v_exp_f32 v137, v121\n" \
  "v_exp_f32 v138, v122\n" \
  "v_exp_f32 v139, v123\n" \
  "v_smfmac_f32_32x32x32_bf16 v[32:47], v[148:151], v[128:135], v161\n" \
  "ds_read_b128 v[148:151], v160 offset:6144\n" \
  "v_exp_f32 v140, v124\n" \
  "v_exp_f32 v141, v125\n" \
  "v_exp_f32 v142, v126\n" \
  "v_exp_f32 v143, v127\n" \
  "s_setprio 1\n" \
  "v_mfma_f32_32x32x16_bf16 v[112:127], v[144:147], v[248:251], 0\n" \
  "ds_read_b128 v[144:147], v158 offset:9216\n" \
  "v_exp_f32 v128, v96\n" \
  "v_exp_f32 v129, v97\n" \
  "v_exp_f32 v130, v98\n" \
  "v_exp_f32 v131, v99\n" \
  "v_smfmac_f32_32x32x32_bf16 v[32:47], v[152:155], v[136:143], v161\n" \
  "ds_read_b128 v[152:155], v160 offset:6912\n" \
  "v_exp_f32 v132, v100\n" \
  "v_exp_f32 v133, v101\n" \
  "v_exp_f32 v134, v102\n" \
  "v_mfma_f32_32x32x8_bf16 v[112:127], v[156:157], v[254:255], v[112:127]\n" \
  "ds_read_b64 v[156:157], v159 offset:9216\n" \
  "v_exp_f32 v135, v103\n" \
  "v_exp_f32 v136, v104\n" \
  "v_exp_f32 v137, v105\n" \
  "v_exp_f32 v138, v106\n" \
  "v_exp_f32 v139, v107\n" \
  "s_waitcnt lgkmcnt(3)\n" \
  "v_smfmac_f32_32x32x32_bf16 v[16:31], v[148:151], v[128:135], v161\n" \
  "v_exp_f32 v140, v108\n" \
  "v_exp_f32 v141, v109\n" \
  "ds_read_b32 v162, v0 offset:42848\n" \
  "v_exp_f32 v142, v110\n" \
  "v_exp_f32 v143, v111\n" \
  "s_waitcnt lgkmcnt(0)\n" \
  "v_mfma_f32_32x32x16_bf16 v[96:111], v[144:147], v[244:247], 0\n" \
  "v_exp_f32 v128, v112\n" \
  "v_exp_f32 v129, v113\n" \
  "v_readfirstlane_b32 s56, v162\n" \
  "s_mov_b32 s58, 0x1000000\n" \
  "s_cmp_lt_u32 s56, 8\n" \
  "s_cbranch_scc1 81f\n" \
  "91:\n" \
  "v_exp_f32 v130, v114\n" \
  "v_exp_f32 v131, v115\n" \
  "v_smfmac_f32_32x32x32_bf16 v[16:31], v[152:155], v[136:143], v161\n" \
  "v_exp_f32 v132, v116\n" \
  "v_exp_f32 v133, v117\n" \
  "v_exp_f32 v134, v118\n" \
  "v_mfma_f32_32x32x8_bf16 v[96:111], v[156:157], v[252:253], v[96:111]\n" \
  "v_exp_f32 v135, v119\n" \
  "v_exp_f32 v136, v120\n" \
  "v_exp_f32 v137, v121\n" \
  "v_exp_f32 v138, v122\n" \
  "v_exp_f32 v139, v123\n" \
  "v_smfmac_f32_32x32x32_bf16 v[32:47], v[148:151], v[128:135], v161\n" \
  "ds_read_b128 v[148:151], v160 offset:9216\n" \
  "v_exp_f32 v140, v124\n" \
  "v_exp_f32 v141, v125\n" \
  "v_exp_f32 v142, v126\n" \
  "v_exp_f32 v143, v127\n" \
  "s_setprio 0\n" \
  "v_mfma_f32_32x32x16_bf16 v[112:127], v[144:147], v[248:251], 0\n" \
  "ds_read_b128 v[144:147], v158 offset:24576\n" \
  "v_exp_f32 v128, v96\n" \
  "v_exp_f32 v129, v97\n" \
  "v_exp_f32 v130, v98\n" \
  "v_exp_f32 v131, v99\n" \
  "v_smfmac_f32_32x32x32_bf16 v[32:47], v[152:155], v[136:143], v161\n" \
  "ds_read_b128 v[152:155], v160 offset:9984\n" \
  "v_exp_f32 v132, v100\n" \
  "v_exp_f32 v133, v101\n" \
  "v_exp_f32 v134, v102\n" \
  "v_mfma_f32_32x32x8_bf16 v[112:127], v[156:157], v[254:255], v[112:127]\n" \
  "ds_read_b64 v[156:157], v159 offset:24576\n" \
  "v_exp_f32 v135, v103\n" \
  "v_exp_f32 v136, v104\n" \
  "v_exp_f32 v137, v105\n" \
  "v_exp_f32 v138, v106\n" \
  "v_exp_f32 v139, v107\n" \
  "s_waitcnt lgkmcnt(3)\n" \
  "v_smfmac_f32_32x32x32_bf16 v[16:31], v[148:151], v[128:135], v161\n" \
  "v_exp_f32 v140, v108\n" \
  "v_exp_f32 v141, v109\n" \
  "s_waitcnt vmcnt(0)\n" \
  "s_mov_b64 exec, 1\n" \
  "ds_add_u32 v0, v7 offset:42848\n" \
  "s_mov_b64 exec, -1\n" \
  "v_exp_f32 v142, v110\n" \
  "v_exp_f32 v143, v111\n" \
  "s_waitcnt lgkmcnt(0)\n" \
  "v_mfma_f32_32x32x16_bf16 v[96:111], v[144:147], v[244:247], 0\n" \
  "v_exp_f32 v128, v112\n" \
  "v_exp_f32 v129, v113\n" \
  "v_exp_f32 v130, v114\n" \
  "v_exp_f32 v131, v115\n" \
  "v_smfmac_f32_32x32x32_bf16 v[16:31], v[152:155], v[136:143], v161\n" \
  "v_exp_f32 v132, v116\n" \
  "v_exp_f32 v133, v117\n" \
  "v_exp_f32 v134, v118\n" \
  "v_mfma_f32_32x32x8_bf16 v[96:111], v[156:157], v[252:253], v[96:111]\n" \
  "v_exp_f32 v135, v119\n" \
  "v_exp_f32 v136, v120\n" \
  "v_exp_f32 v137, v121\n" \
  "v_exp_f32 v138, v122\n" \
  "v_exp_f32 v139, v123\n" \
  "v_smfmac_f32_32x32x32_bf16 v[32:47], v[148:151], v[128:135], v161\n" \
  "ds_read_b128 v[148:151], v160 offset:24576\n" \
  "v_exp_f32 v140, v124\n" \
  "v_exp_f32 v141, v125\n" \
  "v_exp_f32 v142, v126\n" \
  "v_exp_f32 v143, v127\n" \
  "s_setprio 3\n" \
  "v_mfma_f32_32x32x16_bf16 v[112:127], v[144:147], v[248:251], 0\n" \
  "ds_read_b128 v[144:147], v158 offset:27648\n" \
  "v_exp_f32 v128, v96\n" \
  "v_exp_f32 v129, v97\n" \
  "v_exp_f32 v130, v98\n" \
  "v_exp_f32 v131, v99\n" \
  "v_smfmac_f32_32x32x32_bf16 v[32:47], v[152:155], v[136:143], v161\n" \
  "ds_read_b128 v[152:155], v160 offset:25344\n" \
  "v_exp_f32 v132, v100\n" \
  "v_exp_f32 v133, v101\n" \
  "s_add_u32 m0, s42, 114688\n" \
  "s_nop 0\n" \
  "buffer_load_dwordx4 v2, %2, s57 offen sc1 lds\n" \
  "v_exp_f32 v134, v102\n" \
  "v_mfma_f32_32x32x8_bf16 v[112:127], v[156:157], v[254:255], v[112:127]\n" \
  "ds_read_b64 v[156:157], v159 offset:27648\n" \
  "v_exp_f32 v135, v103\n" \
  "v_exp_f32 v136, v104\n" \
  "v_exp_f32 v137, v105\n" \
  "v_exp_f32 v138, v106\n" \
  "v_exp_f32 v139, v107\n" \
  "s_waitcnt lgkmcnt(3)\n" \
  "v_smfmac_f32_32x32x32_bf16 v[16:31], v[148:151], v[128:135], v161\n" \
  "v_exp_f32 v140, v108\n" \
  "s_add_u32 m0, s42, 115712\n" \
  "s_mov_b64 exec, s[60:61]\n" \
  "buffer_load_dwordx4 v3, %2, s57 offen sc1 lds\n" \
  "s_mov_b64 exec, -1\n" \
  "v_exp_f32 v141, v109\n" \
  "ds_read_b32 v162, v0 offset:42848\n" \
  "v_exp_f32 v142, v110\n" \
  "v_exp_f32 v143, v111\n" \
  "s_waitcnt lgkmcnt(0)\n" \
  "v_mfma_f32_32x32x16_bf16 v[96:111], v[144:147], v[244:247], 0\n" \
  "v_exp_f32 v128, v112\n" \
  "v_exp_f32 v129, v113\n" \
  "v_readfirstlane_b32 s56, v162\n" \
  "s_mov_b32 s58, 0x1000000\n" \
  "s_cmp_lt_u32 s56, 16\n" \
  "s_cbranch_scc1 82f\n" \
  "92:\n" \
  "buffer_store_dword v163, v11, %2, 0 offen sc1\n" \
  "v_exp_f32 v130, v114\n" \
  "v_exp_f32 v131, v115\n" \
  "v_smfmac_f32_32x32x32_bf16 v[16:31], v[152:155], v[136:143], v161\n" \
  "v_exp_f32 v132, v116\n" \
  "v_exp_f32 v133, v117\n" \
  "v_exp_f32 v134, v118\n" \
  "v_mfma_f32_32x32x8_bf16 v[96:111], v[156:157], v[252:253], v[96:111]\n" \
  "v_exp_f32 v135, v119\n" \
  "v_exp_f32 v136, v120\n" \
  "v_exp_f32 v137, v121\n" \
  "s_add_u32 m0, s42, 116736\n" \
  "s_mov_b64 exec, s[62:63]\n" \
  "buffer_load_dwordx4 v4, %2, s57 offen sc1 lds\n" \
  "s_mov_b64 exec, -1\n" \
  "s_add_u32 s57, s57, 983040\n" \
  "s_cmp_ge_u32 s57, 9961472\n" \
  "s_cselect_b32 s56, 7864320, 0\n" \
  "s_sub_u32 s57, s57, s56\n" \
  "v_exp_f32 v138, v122\n" \
  "v_exp_f32 v139, v123\n" \
  "v_smfmac_f32_32x32x32_bf16 v[32:47], v[148:151], v[128:135], v161\n" \
  "ds_read_b128 v[148:151], v160 offset:27648\n" \
  "v_exp_f32 v140, v124\n" \
  "v_exp_f32 v141, v125\n" \
  "v_exp_f32 v142, v126\n" \
  "v_exp_f32 v143, v127\n" \
  "s_setprio 2\n" \
  "v_mfma_f32_32x32x16_bf16 v[112:127], v[144:147], v[248:251], 0\n" \
  "ds_read_b128 v[144:147], v158 offset:30720\n" \
  "v_exp_f32 v128, v96\n" \
  "v_exp_f32 v129, v97\n" \
  "v_exp_f32 v130, v98\n" \
  "v_exp_f32 v131, v99\n" \
  "v_smfmac_f32_32x32x32_bf16 v[32:47], v[152:155], v[136:143], v161\n" \
  "ds_read_b128 v[152:155], v160 offset:28416\n" \
  "v_exp_f32 v132, v100\n" \
  "v_exp_f32 v133, v101\n" \
  "s_add_u32 m0, s42, 139264\n" \
  "s_nop 0\n" \
  "buffer_load_dwordx4 v2, %2, s57 offen sc1 lds\n" \
  "v_exp_f32 v134, v102\n" \
  "v_mfma_f32_32x32x8_bf16 v[112:127], v[156:157], v[254:255], v[112:127]\n" \
  "ds_read_b64 v[156:157], v159 offset:30720\n" \
  "v_exp_f32 v135, v103\n" \
  "v_exp_f32 v136, v104\n" \
  "v_exp_f32 v137, v105\n" \
  "v_exp_f32 v138, v106\n" \
  "v_exp_f32 v139, v107\n" \
  "s_waitcnt lgkmcnt(3)\n" \
  "v_smfmac_f32_32x32x32_bf16 v[16:31], v[148:151], v[128:135], v161\n" \
  "v_exp_f32 v140, v108\n" \
  "s_add_u32 m0, s42, 140288\n" \
  "s_mov_b64 exec, s[60:61]\n" \
  "buffer_load_dwordx4 v3, %2, s57 offen sc1 lds\n" \
  "s_mov_b64 exec, -1\n" \
  "v_exp_f32 v141, v109\n" \
  "v_exp_f32 v142, v110\n" \
  "v_exp_f32 v143, v111\n" \
  "s_waitcnt lgkmcnt(0)\n" \
  "v_mfma_f32_32x32x16_bf16 v[96:111], v[144:147], v[244:247], 0\n" \
  "v_exp_f32 v128, v112\n" \
  "v_exp_f32 v129, v113\n" \
  "v_exp_f32 v130, v114\n" \
  "v_exp_f32 v131, v115\n" \
  "v_smfmac_f32_32x32x32_bf16 v[16:31], v[152:155], v[136:143], v161\n" \
  "v_exp_f32 v132, v116\n" \
  "v_exp_f32 v133, v117\n" \
  "v_exp_f32 v134, v118\n" \
  "v_mfma_f32_32x32x8_bf16 v[96:111], v[156:157], v[252:253], v[96:111]\n" \
  "v_exp_f32 v135, v119\n" \
  "v_exp_f32 v136, v120\n" \
  "v_exp_f32 v137, v121\n" \
  "s_add_u32 m0, s42, 141312\n" \
  "s_mov_b64 exec, s[62:63]\n" \
  "buffer_load_dwordx4 v4, %2, s57 offen sc1 lds\n" \
  "s_mov_b64 exec, -1\n" \
  "s_add_u32 s57, s57, 983040\n" \
  "s_cmp_ge_u32 s57, 9961472\n" \
  "s_cselect_b32 s56, 7864320, 0\n" \
  "s_sub_u32 s57, s57, s56\n" \
  "v_exp_f32 v138, v122\n" \
  "v_exp_f32 v139, v123\n" \
  "v_smfmac_f32_32x32x32_bf16 v[32:47], v[148:151], v[128:135], v161\n" \
  "ds_read_b128 v[148:151], v160 offset:30720\n" \
  "v_exp_f32 v140, v124\n" \
  "v_exp_f32 v141, v125\n" \
  "v_exp_f32 v142, v126\n" \
  "v_exp_f32 v143, v127\n" \
  "s_setprio 1\n" \
  "v_mfma_f32_32x32x16_bf16 v[112:127], v[144:147], v[248:251], 0\n" \
  "ds_read_b128 v[144:147], v158 offset:33792\n" \
  "v_exp_f32 v128, v96\n" \
  "v_exp_f32 v129, v97\n" \
  "v_exp_f32 v130, v98\n" \
  "v_exp_f32 v131, v99\n" \
  "v_smfmac_f32_32x32x32_bf16 v[32:47], v[152:155], v[136:143], v161\n" \
  "ds_read_b128 v[152:155], v160 offset:31488\n" \
  "v_exp_f32 v132, v100\n" \
  "v_exp_f32 v133, v101\n" \
  "s_add_u32 m0, s42, 16384\n" \
  "s_nop 0\n" \
  "buffer_load_dwordx4 v2, %2, s57 offen sc1 lds\n" \
  "v_exp_f32 v134, v102\n" \
  "v_mfma_f32_32x32x8_bf16 v[112:127], v[156:157], v[254:255], v[112:127]\n" \
  "ds_read_b64 v[156:157], v159 offset:33792\n" \
  "v_exp_f32 v135, v103\n" \
  "v_exp_f32 v136, v104\n" \
  "v_exp_f32 v137, v105\n" \
  "v_exp_f32 v138, v106\n" \
  "v_exp_f32 v139, v107\n" \
  "s_waitcnt lgkmcnt(3)\n" \
  "v_smfmac_f32_32x32x32_bf16 v[16:31], v[148:151], v[128:135], v161\n" \
  "v_exp_f32 v140, v108\n" \
  "s_add_u32 m0, s42, 17408\n" \
  "s_mov_b64 exec, s[60:61]\n" \
  "buffer_load_dwordx4 v3, %2, s57 offen sc1 lds\n" \
  "s_mov_b64 exec, -1\n" \
  "v_exp_f32 v141, v109\n" \
  "v_exp_f32 v142, v110\n" \
  "v_exp_f32 v143, v111\n" \
  "s_waitcnt lgkmcnt(0)\n" \
  "v_mfma_f32_32x32x16_bf16 v[96:111], v[144:147], v[244:247], 0\n" \
  "v_exp_f32 v128, v112\n" \
  "v_exp_f32 v129, v113\n" \
  "v_exp_f32 v130, v114\n" \
  "v_exp_f32 v131, v115\n" \
  "v_smfmac_f32_32x32x32_bf16 v[16:31], v[152:155], v[136:143], v161\n" \
  "v_exp_f32 v132, v116\n" \
  "v_exp_f32 v133, v117\n" \
  "v_exp_f32 v134, v118\n" \
  "v_mfma_f32_32x32x8_bf16 v[96:111], v[156:157], v[252:253], v[96:111]\n" \
  "v_exp_f32 v135, v119\n" \
  "v_exp_f32 v136, v120\n" \
  "v_exp_f32 v137, v121\n" \
  "s_add_u32 m0, s42, 18432\n" \
  "s_mov_b64 exec, s[62:63]\n" \
  "buffer_load_dwordx4 v4, %2, s57 offen sc1 lds\n" \
  "s_mov_b64 exec, -1\n" \
  "s_add_u32 s57, s57, 983040\n" \
  "s_cmp_ge_u32 s57, 9961472\n" \
  "s_cselect_b32 s56, 7864320, 0\n" \
  "s_sub_u32 s57, s57, s56\n" \
  "v_exp_f32 v138, v122\n" \
  "v_exp_f32 v139, v123\n" \
  "v_smfmac_f32_32x32x32_bf16 v[32:47], v[148:151], v[128:135], v161\n" \
  "ds_read_b128 v[148:151], v160 offset:33792\n" \
  "v_exp_f32 v140, v124\n" \
  "v_exp_f32 v141, v125\n" \
  "v_exp_f32 v142, v126\n" \
  "v_exp_f32 v143, v127\n" \
  "s_setprio 0\n" \
  "v_mfma_f32_32x32x16_bf16 v[112:127], v[144:147], v[248:251], 0\n" \
  "ds_read_b128 v[144:147], v158 offset:49152\n" \
  "v_exp_f32 v128, v96\n" \
  "v_exp_f32 v129, v97\n" \
  "v_exp_f32 v130, v98\n" \
  "v_exp_f32 v131, v99\n" \
  "v_smfmac_f32_32x32x32_bf16 v[32:47], v[152:155], v[136:143], v161\n" \
  "ds_read_b128 v[152:155], v160 offset:34560\n" \
  "v_exp_f32 v132, v100\n" \
  "v_exp_f32 v133, v101\n" \
  "v_exp_f32 v134, v102\n" \
  "v_mfma_f32_32x32x8_bf16 v[112:127], v[156:157], v[254:255], v[112:127]\n" \
  "ds_read_b64 v[156:157], v159 offset:49152\n" \
  "v_exp_f32 v135, v103\n" \
  "v_mov_b32 v180, -1\n" \
  "v_mov_b32 v181, -1\n" \
  "v_mov_b32 v182, -1\n" \
  "v_mov_b32 v183, -1\n" \
  "buffer_store_dwordx4 v[180:183], v0, %2, s64 offen sc1\n" \
  "v_exp_f32 v136, v104\n" \
  "v_exp_f32 v137, v105\n" \
  "v_exp_f32 v138, v106\n" \
  "v_exp_f32 v139, v107\n" \
  "s_waitcnt lgkmcnt(3)\n" \
  "v_smfmac_f32_32x32x32_bf16 v[16:31], v[148:151], v[128:135], v161\n" \
  "v_exp_f32 v140, v108\n" \
  "v_exp_f32 v141, v109\n" \
  "v_exp_f32 v142, v110\n" \
  "v_exp_f32 v143, v111\n" \
  "s_waitcnt lgkmcnt(0)\n" \
  "v_mfma_f32_32x32x16_bf16 v[96:111], v[144:147], v[244:247], 0\n" \
  "v_exp_f32 v128, v112\n" \
  "v_exp_f32 v129, v113\n" \
  "v_exp_f32 v130, v114\n" \
  "v_exp_f32 v131, v115\n" \
  "v_smfmac_f32_32x32x32_bf16 v[16:31], v[152:155], v[136:143], v161\n" \
  "v_exp_f32 v132, v116\n" \
  "v_exp_f32 v133, v117\n" \
  "v_exp_f32 v134, v118\n" \
  "v_mfma_f32_32x32x8_bf16 v[96:111], v[156:157], v[252:253], v[96:111]\n" \
  "v_exp_f32 v135, v119\n" \
  "s_mov_b64 exec, 0xffff\n" \
  "buffer_store_dwordx4 v[180:183], v0, %2, s65 offen sc1\n" \
  "s_mov_b64 exec, -1\n" \
  "v_exp_f32 v136, v120\n" \
  "v_exp_f32 v137, v121\n" \
  "v_exp_f32 v138, v122\n" \
  "v_exp_f32 v139, v123\n" \
  "v_smfmac_f32_32x32x32_bf16 v[32:47], v[148:151], v[128:135], v161\n" \
  "ds_read_b128 v[148:151], v160 offset:49152\n" \
  "v_exp_f32 v140, v124\n" \
  "v_exp_f32 v141, v125\n" \
  "v_exp_f32 v142, v126\n" \
  "v_exp_f32 v143, v127\n" \
  "s_setprio 3\n" \
  "v_mfma_f32_32x32x16_bf16 v[112:127], v[144:147], v[248:251], 0\n" \
  "ds_read_b128 v[144:147], v158 offset:52224\n" \
  "v_exp_f32 v128, v96\n" \
  "v_exp_f32 v129, v97\n" \
  "s_movk_i32 s59, 8192\n" \
  "buffer_load_dwordx4 v[196:199], v0, %2, s59 offen sc1\n" \
  "v_exp_f32 v130, v98\n" \
  "v_exp_f32 v131, v99\n" \
  "v_smfmac_f32_32x32x32_bf16 v[32:47], v[152:155], v[136:143], v161\n" \
  "ds_read_b128 v[152:155], v160 offset:49920\n" \
  "v_exp_f32 v132, v100\n" \
  "v_exp_f32 v133, v101\n" \
  "v_exp_f32 v134, v102\n" \
  "v_mfma_f32_32x32x8_bf16 v[112:127], v[156:157], v[254:255], v[112:127]\n" \
  "ds_read_b64 v[156:157], v159 offset:52224\n" \
  "v_exp_f32 v135, v103\n" \
  "v_exp_f32 v136, v104\n" \
  "v_exp_f32 v137, v105\n" \
  "v_exp_f32 v138, v106\n" \
  "v_exp_f32 v139, v107\n" \
  "s_waitcnt lgkmcnt(3)\n" \
  "v_smfmac_f32_32x32x32_bf16 v[16:31], v[148:151], v[128:135], v161\n" \
  "v_exp_f32 v140, v108\n" \
  "v_exp_f32 v141, v109\n" \
  "s_waitcnt vmcnt(3)\n" \
  "s_mov_b64 exec, 1\n" \
  "ds_add_u32 v0, v7 offset:42848\n" \
  "s_mov_b64 exec, -1\n" \
  "v_exp_f32 v142, v110\n" \
  "v_exp_f32 v143, v111\n" \
  "s_waitcnt lgkmcnt(0)\n" \
  "v_mfma_f32_32x32x16_bf16 v[96:111], v[144:147], v[244:247], 0\n" \
  "v_exp_f32 v128, v112\n" \
  "v_exp_f32 v129, v113\n" \
  "v_exp_f32 v130, v114\n" \
  "v_exp_f32 v131, v115\n" \
  "v_smfmac_f32_32x32x32_bf16 v[16:31], v[152:155], v[136:143], v161\n" \
  "v_exp_f32 v132, v116\n" \
  "v_exp_f32 v133, v117\n" \
  "v_exp_f32 v134, v118\n" \
  "v_mfma_f32_32x32x8_bf16 v[96:111], v[156:157], v[252:253], v[96:111]\n" \
  "v_exp_f32 v135, v119\n" \
  "v_exp_f32 v136, v120\n" \
  "v_exp_f32 v137, v121\n" \
  "v_exp_f32 v138, v122\n" \
  "v_exp_f32 v139, v123\n" \
  "v_smfmac_f32_32x32x32_bf16 v[32:47], v[148:151], v[128:135], v161\n" \
  "ds_read_b128 v[148:151], v160 offset:52224\n" \
  "v_exp_f32 v140, v124\n" \
  "v_exp_f32 v141, v125\n" \
  "v_exp_f32 v142, v126\n" \
  "v_exp_f32 v143, v127\n" \
  "s_setprio 2\n" \
  "s_mov_b32 s58, 0x40000\n" \
  "s_waitcnt vmcnt(0)\n" \
  "1:\n" \
  "v_xor_b32 v196, v196, v163\n" \
  "v_xor_b32 v197, v197, v163\n" \
  "v_xor_b32 v198, v198, v163\n" \
  "v_xor_b32 v199, v199, v163\n" \
  "v_or3_b32 v196, v196, v197, v198\n" \
  "v_or_b32 v196, v196, v199\n" \
  "v_cmp_ne_u32 vcc, 0, v196\n" \
  "s_sub_u32 s58, s58, 1\n" \
  "s_cbranch_scc1 2f\n" \
  "s_cbranch_vccz 2f\n" \
  "s_sleep 10\n" \
  "buffer_load_dwordx4 v[196:199], v0, %2, s59 offen sc1\n" \
  "s_waitcnt vmcnt(0)\n" \
  "s_branch 1b\n" \
  "2:\n" \
  "v_mfma_f32_32x32x16_bf16 v[112:127], v[144:147], v[248:251], 0\n" \
  "ds_read_b128 v[144:147], v158 offset:55296\n" \
  "v_exp_f32 v128, v96\n" \
  "v_exp_f32 v129, v97\n" \
  "s_add_u32 s56, s44, 0\n" \
  "buffer_load_dwordx4 v[196:199], v0, %2, s56 offen sc1\n" \
  "v_exp_f32 v130, v98\n" \
  "v_exp_f32 v131, v99\n" \
  "v_smfmac_f32_32x32x32_bf16 v[32:47], v[152:155], v[136:143], v161\n" \
  "ds_read_b128 v[152:155], v160 offset:52992\n" \
  "v_exp_f32 v132, v100\n" \
  "v_exp_f32 v133, v101\n" \
  "v_exp_f32 v134, v102\n" \
  "v_mfma_f32_32x32x8_bf16 v[112:127], v[156:157], v[254:255], v[112:127]\n" \
  "ds_read_b64 v[156:157], v159 offset:55296\n" \
  "v_exp_f32 v135, v103\n" \
  "s_add_u32 s56, s45, 0\n" \
  "buffer_load_dwordx4 v[164:167], v10, %2, s56 offen offset:0 sc1\n" \
  "v_exp_f32 v136, v104\n" \
  "v_exp_f32 v137, v105\n" \
  "v_exp_f32 v138, v106\n" \
  "v_exp_f32 v139, v107\n" \
  "s_waitcnt lgkmcnt(3)\n" \
  "v_smfmac_f32_32x32x32_bf16 v[16:31], v[148:151], v[128:135], v161\n" \
  "v_exp_f32 v140, v108\n" \
  "v_exp_f32 v141, v109\n" \
  "s_add_u32 s56, s45, 0\n" \
  "buffer_load_dwordx4 v[168:171], v10, %2, s56 offen offset:256 sc1\n" \
  "v_exp_f32 v142, v110\n" \
  "v_exp_f32 v143, v111\n" \
  "s_waitcnt lgkmcnt(0)\n" \
  "v_mfma_f32_32x32x16_bf16 v[96:111], v[144:147], v[244:247], 0\n" \
  "v_exp_f32 v128, v112\n" \
  "v_exp_f32 v129, v113\n" \
  "v_exp_f32 v130, v114\n" \
  "v_exp_f32 v131, v115\n" \
  "v_smfmac_f32_32x32x32_bf16 v[16:31], v[152:155], v[136:143], v161\n" \
  "s_add_u32 s56, s46, 0\n" \
  "buffer_load_dwordx4 v[172:175], v10, %2, s56 offen offset:0 sc1\n" \
  "v_exp_f32 v132, v116\n" \
  "v_exp_f32 v133, v117\n" \
  "v_exp_f32 v134, v118\n" \
  "v_mfma_f32_32x32x8_bf16 v[96:111], v[156:157], v[252:253], v[96:111]\n" \
  "v_exp_f32 v135, v119\n" \
  "v_exp_f32 v136, v120\n" \
  "v_exp_f32 v137, v121\n" \
  "s_add_u32 s56, s46, 0\n" \
  "buffer_load_dwordx4 v[176:179], v10, %2, s56 offen offset:256 sc1\n" \
  "v_exp_f32 v138, v122\n" \
  "v_exp_f32 v139, v123\n" \
  "v_smfmac_f32_32x32x32_bf16 v[32:47], v[148:151], v[128:135], v161\n" \
  "ds_read_b128 v[148:151], v160 offset:55296\n" \
  "v_exp_f32 v140, v124\n" \
  "v_exp_f32 v141, v125\n" \
  "v_exp_f32 v142, v126\n" \
  "v_exp_f32 v143, v127\n" \
  "s_setprio 1\n" \
  "v_mfma_f32_32x32x16_bf16 v[112:127], v[144:147], v[248:251], 0\n" \
  "ds_read_b128 v[144:147], v158 offset:58368\n" \
  "v_exp_f32 v128, v96\n" \
  "v_exp_f32 v129, v97\n" \
  "s_add_u32 s56, s44, 1024\n" \
  "buffer_load_dwordx4 v[200:203], v0, %2, s56 offen sc1\n" \
  "v_exp_f32 v130, v98\n" \
  "v_exp_f32 v131, v99\n" \
  "v_smfmac_f32_32x32x32_bf16 v[32:47], v[152:155], v[136:143], v161\n" \
  "ds_read_b128 v[152:155], v160 offset:56064\n" \
  "v_exp_f32 v132, v100\n" \
  "v_exp_f32 v133, v101\n" \
  "v_exp_f32 v134, v102\n" \
  "v_mfma_f32_32x32x8_bf16 v[112:127], v[156:157], v[254:255], v[112:127]\n" \
  "ds_read_b64 v[156:157], v159 offset:58368\n" \
  "v_exp_f32 v135, v103\n" \
  "v_exp_f32 v136, v104\n" \
  "v_exp_f32 v137, v105\n" \
  "v_exp_f32 v138, v106\n" \
  "v_exp_f32 v139, v107\n" \
  "s_waitcnt lgkmcnt(3)\n" \
  "v_smfmac_f32_32x32x32_bf16 v[16:31], v[148:151], v[128:135], v161\n" \
  "s_add_u32 s56, s44, 2048\n" \
  "buffer_load_dwordx4 v[204:207], v0, %2, s56 offen sc1\n" \
  "v_exp_f32 v140, v108\n" \
  "v_exp_f32 v141, v109\n" \
  "ds_read_b32 v162, v0 offset:42848\n" \
  "v_exp_f32 v142, v110\n" \
  "v_exp_f32 v143, v111\n" \
  "s_waitcnt lgkmcnt(0)\n" \
  "v_mfma_f32_32x32x16_bf16 v[96:111], v[144:147], v[244:247], 0\n" \
  "v_exp_f32 v128, v112\n" \
  "v_exp_f32 v129, v113\n" \
  "v_readfirstlane_b32 s56, v162\n" \
  "s_mov_b32 s58, 0x1000000\n" \
  "s_cmp_lt_u32 s56, 24\n" \
  "s_cbranch_scc1 83f\n" \
  "93:\n" \
  "v_exp_f32 v130, v114\n" \
  "v_exp_f32 v131, v115\n" \
  "v_smfmac_f32_32x32x32_bf16 v[16:31], v[152:155], v[136:143], v161\n" \
  "v_exp_f32 v132, v116\n" \
  "v_exp_f32 v133, v117\n" \
  "s_add_u32 s56, s44, 3072\n" \
  "buffer_load_dwordx4 v[208:211], v0, %2, s56 offen sc1\n" \
  "v_exp_f32 v134, v118\n" \
  "v_mfma_f32_32x32x8_bf16 v[96:111], v[156:157], v[252:253], v[96:111]\n" \
  "v_exp_f32 v135, v119\n" \
  "v_exp_f32 v136, v120\n" \
  "v_exp_f32 v137, v121\n" \
  "v_exp_f32 v138, v122\n" \
  "v_exp_f32 v139, v123\n" \
  "v_smfmac_f32_32x32x32_bf16 v[32:47], v[148:151], v[128:135], v161\n" \
  "ds_read_b128 v[148:151], v160 offset:58368\n" \
  "v_exp_f32 v140, v124\n" \
  "v_exp_f32 v141, v125\n" \
  "v_exp_f32 v142, v126\n" \
  "v_exp_f32 v143, v127\n" \
  "s_setprio 0\n" \
  "v_mfma_f32_32x32x16_bf16 v[112:127], v[144:147], v[248:251], 0\n" \
  "v_add_u32 v158, 114688, v13\n" \
  "v_add_u32 v159, 114688, v5\n" \
  "ds_read_b128 v[144:147], v158 offset:0\n" \
  "v_exp_f32 v128, v96\n" \
  "v_exp_f32 v129, v97\n" \
  "s_waitcnt vmcnt(6)\n" \
  "v_mfma_f32_16x16x32_bf16 v[48:51], v[196:199], v[164:167], 0\n" \
  "v_exp_f32 v130, v98\n" \
  "v_exp_f32 v131, v99\n" \
  "v_smfmac_f32_32x32x32_bf16 v[32:47], v[152:155], v[136:143], v161\n" \
  "ds_read_b128 v[152:155], v160 offset:59136\n" \
  "v_add_u32 v160, 114688, v6\n" \
  "v_exp_f32 v132, v100\n" \
  "s_add_u32 m0, s42, 40960\n" \
  "s_nop 0\n" \
  "buffer_load_dwordx4 v2, %2, s57 offen sc1 lds\n" \
  "v_exp_f32 v133, v101\n" \
  "v_exp_f32 v134, v102\n" \
  "v_mfma_f32_32x32x8_bf16 v[112:127], v[156:157], v[254:255], v[112:127]\n" \
  "ds_read_b64 v[156:157], v159 offset:0\n" \
  "v_exp_f32 v135, v103\n" \
  "v_exp_f32 v136, v104\n" \
  "v_exp_f32 v137, v105\n" \
  "s_waitcnt vmcnt(6)\n" \
  "v_mfma_f32_16x16x32_bf16 v[52:55], v[196:199], v[168:171], 0\n" \
  "v_exp_f32 v138, v106\n" \
  "s_add_u32 s56, s45, 2048\n" \
  "buffer_load_dwordx4 v[180:183], v10, %2, s56 offen offset:0 sc1\n" \
  "v_exp_f32 v139, v107\n" \
  "s_waitcnt lgkmcnt(3)\n" \
  "v_smfmac_f32_32x32x32_bf16 v[16:31], v[148:151], v[128:135], v161\n" \
  "v_exp_f32 v140, v108\n" \
  "s_add_u32 m0, s42, 41984\n" \
  "s_mov_b64 exec, s[60:61]\n" \
  "buffer_load_dwordx4 v3, %2, s57 offen sc1 lds\n" \
  "s_mov_b64 exec, -1\n" \
  "v_exp_f32 v141, v109\n" \
  "s_waitcnt vmcnt(7)\n" \
  "v_mfma_f32_16x16x32_bf16 v[56:59], v[196:199], v[172:175], 0\n" \
  "v_exp_f32 v142, v110\n" \
  "v_exp_f32 v143, v111\n" \
  "s_waitcnt lgkmcnt(0)\n" \
  "v_mfma_f32_32x32x16_bf16 v[96:111], v[144:147], v[244:247], 0\n" \
  "v_exp_f32 v128, v112\n" \
  "v_exp_f32 v129, v113\n" \
  "s_waitcnt vmcnt(6)\n" \
  "v_mfma_f32_16x16x32_bf16 v[60:63], v[196:199], v[176:179], 0\n" \
  "v_exp_f32 v130, v114\n" \
  "s_add_u32 s56, s44, 4096\n" \
  "buffer_load_dwordx4 v[196:199], v0, %2, s56 offen sc1\n" \
  "v_exp_f32 v131, v115\n" \
  "v_smfmac_f32_32x32x32_bf16 v[16:31], v[152:155], v[136:143], v161\n" \
  "v_exp_f32 v132, v116\n" \
  "v_exp_f32 v133, v117\n" \
  "v_exp_f32 v134, v118\n" \
  "v_mfma_f32_32x32x8_bf16 v[96:111], v[156:157], v[252:253], v[96:111]\n" \
  "v_exp_f32 v135, v119\n" \
  "s_add_u32 m0, s42, 43008\n" \
  "s_mov_b64 exec, s[62:63]\n" \
  "buffer_load_dwordx4 v4, %2, s57 offen sc1 lds\n" \
  "s_mov_b64 exec, -1\n" \
  "s_add_u32 s57, s57, 983040\n" \
  "s_cmp_ge_u32 s57, 9961472\n" \
  "s_cselect_b32 s56, 7864320, 0\n" \
  "s_sub_u32 s57, s57, s56\n" \
  "v_exp_f32 v136, v120\n" \
  "v_exp_f32 v137, v121\n" \
  "s_waitcnt vmcnt(7)\n" \
  "v_mfma_f32_16x16x32_bf16 v[64:67], v[200:203], v[164:167], 0\n" \
  "v_exp_f32 v138, v122\n" \
  "v_exp_f32 v139, v123\n" \
  "v_smfmac_f32_32x32x32_bf16 v[32:47], v[148:151], v[128:135], v161\n" \
  "ds_read_b128 v[148:151], v160 offset:0\n" \
  "v_exp_f32 v140, v124\n" \
  "v_exp_f32 v141, v125\n" \
  "v_mfma_f32_16x16x32_bf16 v[68:71], v[200:203], v[168:171], 0\n" \
  "v_exp_f32 v142, v126\n" \
  "s_add_u32 s56, s45, 2048\n" \
  "buffer_load_dwordx4 v[184:187], v10, %2, s56 offen offset:256 sc1\n" \
  "v_exp_f32 v143, v127\n" \
  "s_setprio 3\n" \
  "v_mfma_f32_32x32x16_bf16 v[112:127], v[144:147], v[248:251], 0\n" \
  "ds_read_b128 v[144:147], v158 offset:3072\n" \
  "v_exp_f32 v128, v96\n" \
  "v_exp_f32 v129, v97\n" \
  "v_mfma_f32_16x16x32_bf16 v[72:75], v[200:203], v[172:175], 0\n" \
  "v_exp_f32 v130, v98\n" \
  "v_exp_f32 v131, v99\n" \
  "v_smfmac_f32_32x32x32_bf16 v[32:47], v[152:155], v[136:143], v161\n" \
  "ds_read_b128 v[152:155], v160 offset:768\n" \
  "v_exp_f32 v132, v100\n" \
  "s_add_u32 m0, s42, 65536\n" \
  "s_nop 0\n" \
  "buffer_load_dwordx4 v2, %2, s57 offen sc1 lds\n" \
  "v_exp_f32 v133, v101\n" \
  "v_exp_f32 v134, v102\n" \
  "v_mfma_f32_32x32x8_bf16 v[112:127], v[156:157], v[254:255], v[112:127]\n" \
  "ds_read_b64 v[156:157], v159 offset:3072\n" \
  "v_exp_f32 v135, v103\n" \
  "v_exp_f32 v136, v104\n" \
  "v_exp_f32 v137, v105\n" \
  "v_mfma_f32_16x16x32_bf16 v[76:79], v[200:203], v[176:179], 0\n" \
  "v_exp_f32 v138, v106\n" \
  "s_add_u32 s56, s44, 5120\n" \
  "buffer_load_dwordx4 v[200:203], v0, %2, s56 offen sc1\n" \
  "v_exp_f32 v139, v107\n" \
  "s_waitcnt lgkmcnt(3)\n" \
  "v_smfmac_f32_32x32x32_bf16 v[16:31], v[148:151], v[128:135], v161\n" \
  "v_exp_f32 v140, v108\n" \
  "s_add_u32 m0, s42, 66560\n" \
  "s_mov_b64 exec, s[60:61]\n" \
  "buffer_load_dwordx4 v3, %2, s57 offen sc1 lds\n" \
  "s_mov_b64 exec, -1\n" \
  "v_exp_f32 v141, v109\n" \
  "s_waitcnt vmcnt(10)\n" \
  "v_mfma_f32_16x16x32_bf16 v[80:83], v[204:207], v[164:167], 0\n" \
  "v_exp_f32 v142, v110\n" \
  "v_exp_f32 v143, v111\n" \
  "s_waitcnt lgkmcnt(0)\n" \
  "v_mfma_f32_32x32x16_bf16 v[96:111], v[144:147], v[244:247], 0\n" \
  "v_exp_f32 v128, v112\n" \
  "v_exp_f32 v129, v113\n" \
  "v_mfma_f32_16x16x32_bf16 v[84:87], v[204:207], v[168:171], 0\n" \
  "v_exp_f32 v130, v114\n" \
  "s_add_u32 s56, s46, 2048\n" \
  "buffer_load_dwordx4 v[188:191], v10, %2, s56 offen offset:0 sc1\n" \
  "v_exp_f32 v131, v115\n" \
  "v_smfmac_f32_32x32x32_bf16 v[16:31], v[152:155], v[136:143], v161\n" \
  "v_exp_f32 v132, v116\n" \
  "v_exp_f32 v133, v117\n" \
  "v_exp_f32 v134, v118\n" \
  "v_mfma_f32_32x32x8_bf16 v[96:111], v[156:157], v[252:253], v[96:111]\n" \
  "v_exp_f32 v135, v119\n" \
  "s_add_u32 m0, s42, 67584\n" \
  "s_mov_b64 exec, s[62:63]\n" \
  "buffer_load_dwordx4 v4, %2, s57 offen sc1 lds\n" \
  "s_mov_b64 exec, -1\n" \
  "s_add_u32 s57, s57, 983040\n" \
  "s_cmp_ge_u32 s57, 9961472\n" \
  "s_cselect_b32 s56, 7864320, 0\n" \
  "s_sub_u32 s57, s57, s56\n" \
  "v_exp_f32 v136, v120\n" \
  "v_exp_f32 v137, v121\n" \
  "v_mfma_f32_16x16x32_bf16 v[88:91], v[204:207], v[172:175], 0\n" \
  "v_exp_f32 v138, v122\n" \
  "v_exp_f32 v139, v123\n" \
  "v_smfmac_f32_32x32x32_bf16 v[32:47], v[148:151], v[128:135], v161\n" \
  "ds_read_b128 v[148:151], v160 offset:3072\n" \
  "v_exp_f32 v140, v124\n" \
  "v_exp_f32 v141, v125\n" \
  "v_mfma_f32_16x16x32_bf16 v[92:95], v[204:207], v[176:179], 0\n" \
  "v_exp_f32 v142, v126\n" \
  "s_add_u32 s56, s44, 6144\n" \
  "buffer_load_dwordx4 v[204:207], v0, %2, s56 offen sc1\n" \
  "v_exp_f32 v143, v127\n" \
  "s_setprio 2\n" \
  "v_mfma_f32_32x32x16_bf16 v[112:127], v[144:147], v[248:251], 0\n" \
  "ds_read_b128 v[144:147], v158 offset:6144\n" \
  "v_exp_f32 v128, v96\n" \
  "v_exp_f32 v129, v97\n" \
  "s_waitcnt vmcnt(12)\n" \
  "v_mfma_f32_16x16x32_bf16 v[212:215], v[208:211], v[164:167], 0\n" \
  "v_exp_f32 v130, v98\n" \
  "v_exp_f32 v131, v99\n" \
  "v_smfmac_f32_32x32x32_bf16 v[32:47], v[152:155], v[136:143], v161\n" \
  "ds_read_b128 v[152:155], v160 offset:3840\n" \
  "v_exp_f32 v132, v100\n" \
  "v_exp_f32 v133, v101\n" \
  "v_exp_f32 v134, v102\n" \
  "v_mfma_f32_32x32x8_bf16 v[112:127], v[156:157], v[254:255], v[112:127]\n" \
  "ds_read_b64 v[156:157], v159 offset:6144\n" \
  "v_exp_f32 v135, v103\n" \
  "v_exp_f32 v136, v104\n" \
  "v_exp_f32 v137, v105\n" \
  "v_mfma_f32_16x16x32_bf16 v[216:219], v[208:211], v[168:171], 0\n" \
  "v_exp_f32 v138, v106\n" \
  "s_add_u32 s56, s46, 2048\n" \
  "buffer_load_dwordx4 v[192:195], v10, %2, s56 offen offset:256 sc1\n" \
  "v_exp_f32 v139, v107\n" \
  "s_waitcnt lgkmcnt(3)\n" \
  "v_smfmac_f32_32x32x32_bf16 v[16:31], v[148:151], v[128:135], v161\n" \
  "v_exp_f32 v140, v108\n" \
  "v_exp_f32 v141, v109\n" \
  "v_mfma_f32_16x16x32_bf16 v[220:223], v[208:211], v[172:175], 0\n" \
  "v_exp_f32 v142, v110\n" \
  "v_exp_f32 v143, v111\n" \
  "s_waitcnt lgkmcnt(0)\n" \
  "v_mfma_f32_32x32x16_bf16 v[96:111], v[144:147], v[244:247], 0\n" \
  "v_exp_f32 v128, v112\n" \
  "v_exp_f32 v129, v113\n" \
  "v_mfma_f32_16x16x32_bf16 v[224:227], v[208:211], v[176:179], 0\n" \
  "v_exp_f32 v130, v114\n" \
  "s_add_u32 s56, s44, 7168\n" \
  "buffer_load_dwordx4 v[208:211], v0, %2, s56 offen sc1\n" \
  "v_exp_f32 v131, v115\n" \
  "v_smfmac_f32_32x32x32_bf16 v[16:31], v[152:155], v[136:143], v161\n" \
  "v_exp_f32 v132, v116\n" \
  "v_exp_f32 v133, v117\n" \
  "v_exp_f32 v134, v118\n" \
  "v_mfma_f32_32x32x8_bf16 v[96:111], v[156:157], v[252:253], v[96:111]\n" \
  "v_exp_f32 v135, v119\n" \
  "v_exp_f32 v136, v120\n" \
  "v_exp_f32 v137, v121\n" \
  "s_waitcnt vmcnt(10)\n" \
  "v_mfma_f32_16x16x32_bf16 v[228:231], v[196:199], v[164:167], 0\n" \
  "v_exp_f32 v138, v122\n" \
  "v_exp_f32 v139, v123\n" \
  "v_smfmac_f32_32x32x32_bf16 v[32:47], v[148:151], v[128:135], v161\n" \
  "ds_read_b128 v[148:151], v160 offset:6144\n" \
  "v_exp_f32 v140, v124\n" \
  "v_exp_f32 v141, v125\n" \
  "v_mfma_f32_16x16x32_bf16 v[232:235], v[196:199], v[168:171], 0\n" \
  "v_exp_f32 v142, v126\n" \
  "v_exp_f32 v143, v127\n" \
  "s_setprio 1\n" \
  "v_mfma_f32_32x32x16_bf16 v[112:127], v[144:147], v[248:251], 0\n" \
  "ds_read_b128 v[144:147], v158 offset:9216\n" \
  "v_exp_f32 v128, v96\n" \
  "v_exp_f32 v129, v97\n" \
  "v_mfma_f32_16x16x32_bf16 v[236:239], v[196:199], v[172:175], 0\n" \
  "v_exp_f32 v130, v98\n" \
  "v_exp_f32 v131, v99\n" \
  "v_smfmac_f32_32x32x32_bf16 v[32:47], v[152:155], v[136:143], v161\n" \
  "ds_read_b128 v[152:155], v160 offset:6912\n" \
  "v_exp_f32 v132, v100\n" \
  "v_exp_f32 v133, v101\n" \
  "s_add_u32 m0, s49, 0\n" \
  "s_add_u32 s56, s47, 0\n" \
  "buffer_load_dwordx4 v0, %2, s56 offen sc1 lds\n" \
  "v_exp_f32 v134, v102\n" \
  "v_mfma_f32_32x32x8_bf16 v[112:127], v[156:157], v[254:255], v[112:127]\n" \
  "ds_read_b64 v[156:157], v159 offset:9216\n" \
  "v_exp_f32 v135, v103\n" \
  "v_exp_f32 v136, v104\n" \
  "v_exp_f32 v137, v105\n" \
  "v_mfma_f32_16x16x32_bf16 v[240:243], v[196:199], v[176:179], 0\n" \
  "v_exp_f32 v138, v106\n" \
  "s_add_u32 s56, s44, 8192\n" \
  "buffer_load_dwordx4 v[196:199], v0, %2, s56 offen sc1\n" \
  "v_exp_f32 v139, v107\n" \
  "s_waitcnt lgkmcnt(3)\n" \
  "v_smfmac_f32_32x32x32_bf16 v[16:31], v[148:151], v[128:135], v161\n" \
  "v_exp_f32 v140, v108\n" \
  "v_exp_f32 v141, v109\n" \
  "s_waitcnt vmcnt(8)\n" \
  "v_mfma_f32_16x16x32_bf16 v[48:51], v[200:203], v[180:183], v[48:51]\n" \
  "v_exp_f32 v142, v110\n" \
  "v_exp_f32 v143, v111\n" \
  "s_waitcnt lgkmcnt(0)\n" \
  "v_mfma_f32_32x32x16_bf16 v[96:111], v[144:147], v[244:247], 0\n" \
  "v_exp_f32 v128, v112\n" \
  "v_exp_f32 v129, v113\n" \
  "v_mfma_f32_16x16x32_bf16 v[52:55], v[200:203], v[184:187], v[52:55]\n" \
  "v_exp_f32 v130, v114\n" \
  "s_add_u32 s56, s45, 4096\n" \
  "buffer_load_dwordx4 v[164:167], v10, %2, s56 offen offset:0 sc1\n" \
  "v_exp_f32 v131, v115\n" \
  "v_smfmac_f32_32x32x32_bf16 v[16:31], v[152:155], v[136:143], v161\n" \
  "v_exp_f32 v132, v116\n" \
  "v_exp_f32 v133, v117\n" \
  "v_exp_f32 v134, v118\n" \
  "v_mfma_f32_32x32x8_bf16 v[96:111], v[156:157], v[252:253], v[96:111]\n" \
  "v_exp_f32 v135, v119\n" \
  "v_exp_f32 v136, v120\n" \
  "v_exp_f32 v137, v121\n" \
  "s_waitcnt vmcnt(7)\n" \
  "v_mfma_f32_16x16x32_bf16 v[56:59], v[200:203], v[188:191], v[56:59]\n" \
  "v_exp_f32 v138, v122\n" \
  "v_exp_f32 v139, v123\n" \
  "v_smfmac_f32_32x32x32_bf16 v[32:47], v[148:151], v[128:135], v161\n" \
  "ds_read_b128 v[148:151], v160 offset:9216\n" \
  "v_exp_f32 v140, v124\n" \
  "v_exp_f32 v141, v125\n" \
  "s_waitcnt vmcnt(4)\n" \
  "v_mfma_f32_16x16x32_bf16 v[60:63], v[200:203], v[192:195], v[60:63]\n" \
  "v_exp_f32 v142, v126\n" \
  "s_add_u32 s56, s44, 9216\n" \
  "buffer_load_dwordx4 v[200:203], v0, %2, s56 offen sc1\n" \
  "v_exp_f32 v143, v127\n" \
  "s_setprio 0\n" \
  "v_mfma_f32_32x32x16_bf16 v[112:127], v[144:147], v[248:251], 0\n" \
  "ds_read_b128 v[144:147], v158 offset:24576\n" \
  "v_exp_f32 v128, v96\n" \
  "v_exp_f32 v129, v97\n" \
  "v_mfma_f32_16x16x32_bf16 v[64:67], v[204:207], v[180:183], v[64:67]\n" \
  "v_exp_f32 v130, v98\n" \
  "v_exp_f32 v131, v99\n" \
  "v_smfmac_f32_32x32x32_bf16 v[32:47], v[152:155], v[136:143], v161\n" \
  "ds_read_b128 v[152:155], v160 offset:9984\n" \
  "v_exp_f32 v132, v100\n" \
  "v_exp_f32 v133, v101\n" \
  "s_add_u32 m0, s49, 1024\n" \
  "s_add_u32 s56, s47, 1024\n" \
  "buffer_load_dwordx4 v0, %2, s56 offen sc1 lds\n" \
  "v_exp_f32 v134, v102\n" \
  "v_mfma_f32_32x32x8_bf16 v[112:127], v[156:157], v[254:255], v[112:127]\n" \
  "ds_read_b64 v[156:157], v159 offset:24576\n" \
  "v_exp_f32 v135, v103\n" \
  "v_exp_f32 v136, v104\n" \
  "v_exp_f32 v137, v105\n" \
  "v_mfma_f32_16x16x32_bf16 v[68:71], v[204:207], v[184:187], v[68:71]\n" \
  "v_exp_f32 v138, v106\n" \
  "s_add_u32 s56, s45, 4096\n" \
  "buffer_load_dwordx4 v[168:171], v10, %2, s56 offen offset:256 sc1\n" \
  "v_exp_f32 v139, v107\n" \
  "s_waitcnt lgkmcnt(3)\n" \
  "v_smfmac_f32_32x32x32_bf16 v[16:31], v[148:151], v[128:135], v161\n" \
  "v_exp_f32 v140, v108\n" \
  "v_exp_f32 v141, v109\n" \
  "v_mfma_f32_16x16x32_bf16 v[72:75], v[204:207], v[188:191], v[72:75]\n" \
  "v_exp_f32 v142, v110\n" \
  "v_exp_f32 v143, v111\n" \
  "s_waitcnt lgkmcnt(0)\n" \
  "v_mfma_f32_32x32x16_bf16 v[96:111], v[144:147], v[244:247], 0\n" \
  "v_exp_f32 v128, v112\n" \
  "v_exp_f32 v129, v113\n" \
  "v_mfma_f32_16x16x32_bf16 v[76:79], v[204:207], v[192:195], v[76:79]\n" \
  "v_exp_f32 v130, v114\n" \
  "s_add_u32 s56, s44, 10240\n" \
  "buffer_load_dwordx4 v[204:207], v0, %2, s56 offen sc1\n" \
  "v_exp_f32 v131, v115\n" \
  "v_smfmac_f32_32x32x32_bf16 v[16:31], v[152:155], v[136:143], v161\n" \
  "v_exp_f32 v132, v116\n" \
  "v_exp_f32 v133, v117\n" \
  "v_exp_f32 v134, v118\n" \
  "v_mfma_f32_32x32x8_bf16 v[96:111], v[156:157], v[252:253], v[96:111]\n" \
  "v_exp_f32 v135, v119\n" \
  "v_exp_f32 v136, v120\n" \
  "v_exp_f32 v137, v121\n" \
  "s_waitcnt vmcnt(7)\n" \
  "v_mfma_f32_16x16x32_bf16 v[80:83], v[208:211], v[180:183], v[80:83]\n" \
  "v_exp_f32 v138, v122\n" \
  "v_exp_f32 v139, v123\n" \
  "v_smfmac_f32_32x32x32_bf16 v[32:47], v[148:151], v[128:135], v161\n" \
  "ds_read_b128 v[148:151], v160 offset:24576\n" \
  "v_exp_f32 v140, v124\n" \
  "v_exp_f32 v141, v125\n" \
  "v_mfma_f32_16x16x32_bf16 v[84:87], v[208:211], v[184:187], v[84:87]\n" \
  "v_exp_f32 v142, v126\n" \
  "s_add_u32 s56, s46, 4096\n" \
  "buffer_load_dwordx4 v[172:175], v10, %2, s56 offen offset:0 sc1\n" \
  "v_exp_f32 v143, v127\n" \
  "s_setprio 3\n" \
  "v_mfma_f32_32x32x16_bf16 v[112:127], v[144:147], v[248:251], 0\n" \
  "ds_read_b128 v[144:147], v158 offset:27648\n" \
  "v_exp_f32 v128, v96\n" \
  "v_exp_f32 v129, v97\n" \
  "v_mfma_f32_16x16x32_bf16 v[88:91], v[208:211], v[188:191], v[88:91]\n" \
  "v_exp_f32 v130, v98\n" \
  "v_exp_f32 v131, v99\n" \
  "v_smfmac_f32_32x32x32_bf16 v[32:47], v[152:155], v[136:143], v161\n" \
  "ds_read_b128 v[152:155], v160 offset:25344\n" \
  "v_exp_f32 v132, v100\n" \
  "v_exp_f32 v133, v101\n" \
  "s_add_u32 s56, s48, 0\n" \
  "buffer_load_dwordx2 v[8:9], v0, %2, s56 offen sc1\n" \
  "v_exp_f32 v134, v102\n" \
  "v_mfma_f32_32x32x8_bf16 v[112:127], v[156:157], v[254:255], v[112:127]\n" \
  "ds_read_b64 v[156:157], v159 offset:27648\n" \
  "v_exp_f32 v135, v103\n" \
  "v_exp_f32 v136, v104\n" \
  "v_exp_f32 v137, v105\n" \
  "v_mfma_f32_16x16x32_bf16 v[92:95], v[208:211], v[192:195], v[92:95]\n" \
  "v_exp_f32 v138, v106\n" \
  "s_add_u32 s56, s44, 11264\n" \
  "buffer_load_dwordx4 v[208:211], v0, %2, s56 offen sc1\n" \
  "v_exp_f32 v139, v107\n" \
  "s_waitcnt lgkmcnt(3)\n" \
  "v_smfmac_f32_32x32x32_bf16 v[16:31], v[148:151], v[128:135], v161\n" \
  "v_exp_f32 v140, v108\n" \
  "v_exp_f32 v141, v109\n" \
  "s_waitcnt vmcnt(8)\n" \
  "v_mfma_f32_16x16x32_bf16 v[212:215], v[196:199], v[180:183], v[212:215]\n" \
  "v_exp_f32 v142, v110\n" \
  "v_exp_f32 v143, v111\n" \
  "s_waitcnt lgkmcnt(0)\n" \
  "v_mfma_f32_32x32x16_bf16 v[96:111], v[144:147], v[244:247], 0\n" \
  "v_exp_f32 v128, v112\n" \
  "v_exp_f32 v129, v113\n" \
  "v_mfma_f32_16x16x32_bf16 v[216:219], v[196:199], v[184:187], v[216:219]\n" \
  "v_exp_f32 v130, v114\n" \
  "s_add_u32 s56, s46, 4096\n" \
  "buffer_load_dwordx4 v[176:179], v10, %2, s56 offen offset:256 sc1\n" \
  "v_exp_f32 v131, v115\n" \
  "v_smfmac_f32_32x32x32_bf16 v[16:31], v[152:155], v[136:143], v161\n" \
  "v_exp_f32 v132, v116\n" \
  "v_exp_f32 v133, v117\n" \
  "v_exp_f32 v134, v118\n" \
  "v_mfma_f32_32x32x8_bf16 v[96:111], v[156:157], v[252:253], v[96:111]\n" \
  "v_exp_f32 v135, v119\n" \
  "v_exp_f32 v136, v120\n" \
  "v_exp_f32 v137, v121\n" \
  "v_mfma_f32_16x16x32_bf16 v[220:223], v[196:199], v[188:191], v[220:223]\n" \
  "v_exp_f32 v138, v122\n" \
  "v_exp_f32 v139, v123\n" \
  "v_smfmac_f32_32x32x32_bf16 v[32:47], v[148:151], v[128:135], v161\n" \
  "ds_read_b128 v[148:151], v160 offset:27648\n" \
  "v_exp_f32 v140, v124\n" \
  "v_exp_f32 v141, v125\n" \
  "v_mfma_f32_16x16x32_bf16 v[224:227], v[196:199], v[192:195], v[224:227]\n" \
  "v_exp_f32 v142, v126\n" \
  "s_add_u32 s56, s44, 12288\n" \
  "buffer_load_dwordx4 v[196:199], v0, %2, s56 offen sc1\n" \
  "v_exp_f32 v143, v127\n" \
  "s_setprio 2\n" \
  "v_mfma_f32_32x32x16_bf16 v[112:127], v[144:147], v[248:251], 0\n" \
  "ds_read_b128 v[144:147], v158 offset:30720\n" \
  "v_exp_f32 v128, v96\n" \
  "v_exp_f32 v129, v97\n" \
  "s_waitcnt vmcnt(8)\n" \
  "v_mfma_f32_16x16x32_bf16 v[228:231], v[200:203], v[180:183], v[228:231]\n" \
  "v_exp_f32 v130, v98\n" \
  "v_exp_f32 v131, v99\n" \
  "v_smfmac_f32_32x32x32_bf16 v[32:47], v[152:155], v[136:143], v161\n" \
  "ds_read_b128 v[152:155], v160 offset:28416\n" \
  "v_exp_f32 v132, v100\n" \
  "v_exp_f32 v133, v101\n" \
  "v_exp_f32 v134, v102\n" \
  "v_mfma_f32_32x32x8_bf16 v[112:127], v[156:157], v[254:255], v[112:127]\n" \
  "ds_read_b64 v[156:157], v159 offset:30720\n" \
  "v_exp_f32 v135, v103\n" \
  "v_exp_f32 v136, v104\n" \
  "v_exp_f32 v137, v105\n" \
  "v_mfma_f32_16x16x32_bf16 v[232:235], v[200:203], v[184:187], v[232:235]\n" \
  "v_exp_f32 v138, v106\n" \
  "v_exp_f32 v139, v107\n" \
  "s_waitcnt lgkmcnt(3)\n" \
  "v_smfmac_f32_32x32x32_bf16 v[16:31], v[148:151], v[128:135], v161\n" \
  "v_exp_f32 v140, v108\n" \
  "v_exp_f32 v141, v109\n" \
  "v_mfma_f32_16x16x32_bf16 v[236:239], v[200:203], v[188:191], v[236:239]\n" \
  "v_exp_f32 v142, v110\n" \
  "v_exp_f32 v143, v111\n" \
  "s_waitcnt lgkmcnt(0)\n" \
  "v_mfma_f32_32x32x16_bf16 v[96:111], v[144:147], v[244:247], 0\n" \
  "v_exp_f32 v128, v112\n" \
  "v_exp_f32 v129, v113\n" \
  "v_mfma_f32_16x16x32_bf16 v[240:243], v[200:203], v[192:195], v[240:243]\n" \
  "v_exp_f32 v130, v114\n" \
  "s_add_u32 s56, s44, 13312\n" \
  "buffer_load_dwordx4 v[200:203], v0, %2, s56 offen sc1\n" \
  "v_exp_f32 v131, v115\n" \
  "v_smfmac_f32_32x32x32_bf16 v[16:31], v[152:155], v[136:143], v161\n" \
  "v_exp_f32 v132, v116\n" \
  "v_exp_f32 v133, v117\n" \
  "v_exp_f32 v134, v118\n" \
  "v_mfma_f32_32x32x8_bf16 v[96:111], v[156:157], v[252:253], v[96:111]\n" \
  "v_exp_f32 v135, v119\n" \
  "v_exp_f32 v136, v120\n" \
  "v_exp_f32 v137, v121\n" \
  "s_waitcnt vmcnt(6)\n" \
  "v_mfma_f32_16x16x32_bf16 v[48:51], v[204:207], v[164:167], v[48:51]\n" \
  "v_exp_f32 v138, v122\n" \
  "v_exp_f32 v139, v123\n" \
  "v_smfmac_f32_32x32x32_bf16 v[32:47], v[148:151], v[128:135], v161\n" \
  "ds_read_b128 v[148:151], v160 offset:30720\n" \
  "v_exp_f32 v140, v124\n" \
  "v_exp_f32 v141, v125\n" \
  "v_mfma_f32_16x16x32_bf16 v[52:55], v[204:207], v[168:171], v[52:55]\n" \
  "v_exp_f32 v142, v126\n" \
  "s_add_u32 s56, s45, 6144\n" \
  "buffer_load_dwordx4 v[180:183], v10, %2, s56 offen offset:0 sc1\n" \
  "v_exp_f32 v143, v127\n" \
  "s_setprio 1\n" \
  "v_mfma_f32_32x32x16_bf16 v[112:127], v[144:147], v[248:251], 0\n" \
  "ds_read_b128 v[144:147], v158 offset:33792\n" \
  "v_exp_f32 v128, v96\n" \
  "v_exp_f32 v129, v97\n" \
  "s_waitcnt vmcnt(6)\n" \
  "v_mfma_f32_16x16x32_bf16 v[56:59], v[204:207], v[172:175], v[56:59]\n" \
  "v_exp_f32 v130, v98\n" \
  "v_exp_f32 v131, v99\n" \
  "v_smfmac_f32_32x32x32_bf16 v[32:47], v[152:155], v[136:143], v161\n" \
  "ds_read_b128 v[152:155], v160 offset:31488\n" \
  "v_exp_f32 v132, v100\n" \
  "v_exp_f32 v133, v101\n" \
  "v_exp_f32 v134, v102\n" \
  "v_mfma_f32_32x32x8_bf16 v[112:127], v[156:157], v[254:255], v[112:127]\n" \
  "ds_read_b64 v[156:157], v159 offset:33792\n" \
  "v_exp_f32 v135, v103\n" \
  "v_exp_f32 v136, v104\n" \
  "v_exp_f32 v137, v105\n" \
  "s_waitcnt vmcnt(3)\n" \
  "v_mfma_f32_16x16x32_bf16 v[60:63], v[204:207], v[176:179], v[60:63]\n" \
  "v_exp_f32 v138, v106\n" \
  "s_add_u32 s56, s44, 14336\n" \
  "buffer_load_dwordx4 v[204:207], v0, %2, s56 offen sc1\n" \
  "v_exp_f32 v139, v107\n" \
  "s_waitcnt lgkmcnt(3)\n" \
  "v_smfmac_f32_32x32x32_bf16 v[16:31], v[148:151], v[128:135], v161\n" \
  "v_exp_f32 v140, v108\n" \
  "v_exp_f32 v141, v109\n" \
  "v_mfma_f32_16x16x32_bf16 v[64:67], v[208:211], v[164:167], v[64:67]\n" \
  "v_exp_f32 v142, v110\n" \
  "v_exp_f32 v143, v111\n" \
  "s_waitcnt lgkmcnt(0)\n" \
  "v_mfma_f32_32x32x16_bf16 v[96:111], v[144:147], v[244:247], 0\n" \
  "v_exp_f32 v128, v112\n" \
  "v_exp_f32 v129, v113\n" \
  "v_mfma_f32_16x16x32_bf16 v[68:71], v[208:211], v[168:171], v[68:71]\n" \
  "v_exp_f32 v130, v114\n" \
  "s_add_u32 s56, s45, 6144\n" \
  "buffer_load_dwordx4 v[184:187], v10, %2, s56 offen offset:256 sc1\n" \
  "v_exp_f32 v131, v115\n" \
  "v_smfmac_f32_32x32x32_bf16 v[16:31], v[152:155], v[136:143], v161\n" \
  "v_exp_f32 v132, v116\n" \
  "v_exp_f32 v133, v117\n" \
  "v_exp_f32 v134, v118\n" \
  "v_mfma_f32_32x32x8_bf16 v[96:111], v[156:157], v[252:253], v[96:111]\n" \
  "v_exp_f32 v135, v119\n" \
  "v_exp_f32 v136, v120\n" \
  "v_exp_f32 v137, v121\n" \
  "v_mfma_f32_16x16x32_bf16 v[72:75], v[208:211], v[172:175], v[72:75]\n" \
  "v_exp_f32 v138, v122\n" \
  "v_exp_f32 v139, v123\n" \
  "v_smfmac_f32_32x32x32_bf16 v[32:47], v[148:151], v[128:135], v161\n" \
  "ds_read_b128 v[148:151], v160 offset:33792\n" \
  "v_exp_f32 v140, v124\n" \
  "v_exp_f32 v141, v125\n" \
  "v_mfma_f32_16x16x32_bf16 v[76:79], v[208:211], v[176:179], v[76:79]\n" \
  "v_exp_f32 v142, v126\n" \
  "s_add_u32 s56, s44, 15360\n" \
  "buffer_load_dwordx4 v[208:211], v0, %2, s56 offen sc1\n" \
  "v_exp_f32 v143, v127\n" \
  "s_setprio 0\n" \
  "v_mfma_f32_32x32x16_bf16 v[112:127], v[144:147], v[248:251], 0\n" \
  "v_add_u32 v158, 16384, v13\n" \
  "v_add_u32 v159, 16384, v5\n" \
  "ds_read_b128 v[144:147], v158 offset:0\n" \
  "v_exp_f32 v128, v96\n" \
  "v_exp_f32 v129, v97\n" \
  "s_waitcnt vmcnt(5)\n" \
  "v_mfma_f32_16x16x32_bf16 v[80:83], v[196:199], v[164:167], v[80:83]\n" \
  "v_exp_f32 v130, v98\n" \
  "v_exp_f32 v131, v99\n" \
  "v_smfmac_f32_32x32x32_bf16 v[32:47], v[152:155], v[136:143], v161\n" \
  "ds_read_b128 v[152:155], v160 offset:34560\n" \
  "v_add_u32 v160, 16384, v6\n" \
  "v_exp_f32 v132, v100\n" \
  "v_exp_f32 v133, v101\n" \
  "v_exp_f32 v134, v102\n" \
  "v_mfma_f32_32x32x8_bf16 v[112:127], v[156:157], v[254:255], v[112:127]\n" \
  "ds_read_b64 v[156:157], v159 offset:0\n" \
  "v_exp_f32 v135, v103\n" \
  "v_exp_f32 v136, v104\n" \
  "v_exp_f32 v137, v105\n" \
  "v_mfma_f32_16x16x32_bf16 v[84:87], v[196:199], v[168:171], v[84:87]\n" \
  "v_exp_f32 v138, v106\n" \
  "s_add_u32 s56, s46, 6144\n" \
  "buffer_load_dwordx4 v[188:191], v10, %2, s56 offen offset:0 sc1\n" \
  "v_exp_f32 v139, v107\n" \
  "s_waitcnt lgkmcnt(3)\n" \
  "v_smfmac_f32_32x32x32_bf16 v[16:31], v[148:151], v[128:135], v161\n" \
  "v_exp_f32 v140, v108\n" \
  "v_exp_f32 v141, v109\n" \
  "v_mfma_f32_16x16x32_bf16 v[88:91], v[196:199], v[172:175], v[88:91]\n" \
  "v_exp_f32 v142, v110\n" \
  "v_exp_f32 v143, v111\n" \
  "s_waitcnt lgkmcnt(0)\n" \
  "v_mfma_f32_32x32x16_bf16 v[96:111], v[144:147], v[244:247], 0\n" \
  "v_exp_f32 v128, v112\n" \
  "v_exp_f32 v129, v113\n" \
  "v_mfma_f32_16x16x32_bf16 v[92:95], v[196:199], v[176:179], v[92:95]\n" \
  "v_exp_f32 v130, v114\n" \
  "s_add_u32 s56, s44, 16384\n" \
  "buffer_load_dwordx4 v[196:199], v0, %2, s56 offen sc1\n" \
  "v_exp_f32 v131, v115\n" \
  "v_smfmac_f32_32x32x32_bf16 v[16:31], v[152:155], v[136:143], v161\n" \
  "v_exp_f32 v132, v116\n" \
  "v_exp_f32 v133, v117\n" \
  "v_exp_f32 v134, v118\n" \
  "v_mfma_f32_32x32x8_bf16 v[96:111], v[156:157], v[252:253], v[96:111]\n" \
  "v_exp_f32 v135, v119\n" \
  "v_exp_f32 v136, v120\n" \
  "v_exp_f32 v137, v121\n" \
  "s_waitcnt vmcnt(6)\n" \
  "v_mfma_f32_16x16x32_bf16 v[212:215], v[200:203], v[164:167], v[212:215]\n" \
  "v_exp_f32 v138, v122\n" \
  "v_exp_f32 v139, v123\n" \
  "v_smfmac_f32_32x32x32_bf16 v[32:47], v[148:151], v[128:135], v161\n" \
  "ds_read_b128 v[148:151], v160 offset:0\n" \
  "v_exp_f32 v140, v124\n" \
  "v_exp_f32 v141, v125\n" \
  "v_mfma_f32_16x16x32_bf16 v[216:219], v[200:203], v[168:171], v[216:219]\n" \
  "v_exp_f32 v142, v126\n" \
  "s_add_u32 s56, s46, 6144\n" \
  "buffer_load_dwordx4 v[192:195], v10, %2, s56 offen offset:256 sc1\n" \
  "v_exp_f32 v143, v127\n" \
  "s_setprio 3\n" \
  "v_mfma_f32_32x32x16_bf16 v[112:127], v[144:147], v[248:251], 0\n" \
  "ds_read_b128 v[144:147], v158 offset:3072\n" \
  "v_exp_f32 v128, v96\n" \
  "v_exp_f32 v129, v97\n" \
  "v_mfma_f32_16x16x32_bf16 v[220:223], v[200:203], v[172:175], v[220:223]\n" \
  "v_exp_f32 v130, v98\n" \
  "v_exp_f32 v131, v99\n" \
  "v_smfmac_f32_32x32x32_bf16 v[32:47], v[152:155], v[136:143], v161\n" \
  "ds_read_b128 v[152:155], v160 offset:768\n" \
  "v_exp_f32 v132, v100\n" \
  "v_exp_f32 v133, v101\n" \
  "v_exp_f32 v134, v102\n" \
  "v_mfma_f32_32x32x8_bf16 v[112:127], v[156:157], v[254:255], v[112:127]\n" \
  "ds_read_b64 v[156:157], v159 offset:3072\n" \
  "v_exp_f32 v135, v103\n" \
  "v_exp_f32 v136, v104\n" \
  "v_exp_f32 v137, v105\n" \
  "v_mfma_f32_16x16x32_bf16 v[224:227], v[200:203], v[176:179], v[224:227]\n" \
  "v_exp_f32 v138, v106\n" \
  "s_add_u32 s56, s44, 17408\n" \
  "buffer_load_dwordx4 v[200:203], v0, %2, s56 offen sc1\n" \
  "v_exp_f32 v139, v107\n" \
  "s_waitcnt lgkmcnt(3)\n" \
  "v_smfmac_f32_32x32x32_bf16 v[16:31], v[148:151], v[128:135], v161\n" \
  "v_exp_f32 v140, v108\n" \
  "v_exp_f32 v141, v109\n" \
  "s_waitcnt vmcnt(6)\n" \
  "v_mfma_f32_16x16x32_bf16 v[228:231], v[204:207], v[164:167], v[228:231]\n" \
  "s_mov_b64 exec, 1\n" \
  "ds_add_u32 v0, v7 offset:42848\n" \
  "s_mov_b64 exec, -1\n" \
  "v_exp_f32 v142, v110\n" \
  "v_exp_f32 v143, v111\n" \
  "s_waitcnt lgkmcnt(0)\n" \
  "v_mfma_f32_32x32x16_bf16 v[96:111], v[144:147], v[244:247], 0\n" \
  "v_exp_f32 v128, v112\n" \
  "v_exp_f32 v129, v113\n" \
  "v_mfma_f32_16x16x32_bf16 v[232:235], v[204:207], v[168:171], v[232:235]\n" \
  "v_exp_f32 v130, v114\n" \
  "v_exp_f32 v131, v115\n" \
  "v_smfmac_f32_32x32x32_bf16 v[16:31], v[152:155], v[136:143], v161\n" \
  "v_exp_f32 v132, v116\n" \
  "v_exp_f32 v133, v117\n" \
  "v_exp_f32 v134, v118\n" \
  "v_mfma_f32_32x32x8_bf16 v[96:111], v[156:157], v[252:253], v[96:111]\n" \
  "v_exp_f32 v135, v119\n" \
  "v_exp_f32 v136, v120\n" \
  "v_exp_f32 v137, v121\n" \
  "v_mfma_f32_16x16x32_bf16 v[236:239], v[204:207], v[172:175], v[236:239]\n" \
  "v_exp_f32 v138, v122\n" \
  "v_exp_f32 v139, v123\n" \
  "v_smfmac_f32_32x32x32_bf16 v[32:47], v[148:151], v[128:135], v161\n" \
  "ds_read_b128 v[148:151], v160 offset:3072\n" \
  "v_exp_f32 v140, v124\n" \
  "v_exp_f32 v141, v125\n" \
  "v_mfma_f32_16x16x32_bf16 v[240:243], v[204:207], v[176:179], v[240:243]\n" \
  "v_exp_f32 v142, v126\n" \
  "s_add_u32 s56, s44, 18432\n" \
  "buffer_load_dwordx4 v[204:207], v0, %2, s56 offen sc1\n" \
  "v_exp_f32 v143, v127\n" \
  "s_setprio 2\n" \
  "v_mfma_f32_32x32x16_bf16 v[112:127], v[144:147], v[248:251], 0\n" \
  "ds_read_b128 v[144:147], v158 offset:6144\n" \
  "v_exp_f32 v128, v96\n" \
  "v_exp_f32 v129, v97\n" \
  "s_waitcnt vmcnt(5)\n" \
  "v_mfma_f32_16x16x32_bf16 v[48:51], v[208:211], v[180:183], v[48:51]\n" \
  "v_exp_f32 v130, v98\n" \
  "v_exp_f32 v131, v99\n" \
  "v_smfmac_f32_32x32x32_bf16 v[32:47], v[152:155], v[136:143], v161\n" \
  "ds_read_b128 v[152:155], v160 offset:3840\n" \
  "v_exp_f32 v132, v100\n" \
  "v_exp_f32 v133, v101\n" \
  "v_exp_f32 v134, v102\n" \
  "v_mfma_f32_32x32x8_bf16 v[112:127], v[156:157], v[254:255], v[112:127]\n" \
  "ds_read_b64 v[156:157], v159 offset:6144\n" \
  "v_exp_f32 v135, v103\n" \
  "v_exp_f32 v136, v104\n" \
  "v_exp_f32 v137, v105\n" \
  "v_mfma_f32_16x16x32_bf16 v[52:55], v[208:211], v[184:187], v[52:55]\n" \
  "v_exp_f32 v138, v106\n" \
  "s_add_u32 s56, s45, 8192\n" \
  "buffer_load_dwordx4 v[164:167], v10, %2, s56 offen offset:0 sc1\n" \
  "v_exp_f32 v139, v107\n" \
  "s_waitcnt lgkmcnt(3)\n" \
  "v_smfmac_f32_32x32x32_bf16 v[16:31], v[148:151], v[128:135], v161\n" \
  "v_exp_f32 v140, v108\n" \
  "v_exp_f32 v141, v109\n" \
  "s_waitcnt vmcnt(5)\n" \
  "v_mfma_f32_16x16x32_bf16 v[56:59], v[208:211], v[188:191], v[56:59]\n" \
  "v_exp_f32 v142, v110\n" \
  "v_exp_f32 v143, v111\n" \
  "s_waitcnt lgkmcnt(0)\n" \
  "v_mfma_f32_32x32x16_bf16 v[96:111], v[144:147], v[244:247], 0\n" \
  "v_exp_f32 v128, v112\n" \
  "v_exp_f32 v129, v113\n" \
  "s_waitcnt vmcnt(3)\n" \
  "v_mfma_f32_16x16x32_bf16 v[60:63], v[208:211], v[192:195], v[60:63]\n" \
  "v_exp_f32 v130, v114\n" \
  "s_add_u32 s56, s44, 19456\n" \
  "buffer_load_dwordx4 v[208:211], v0, %2, s56 offen sc1\n" \
  "v_exp_f32 v131, v115\n" \
  "v_smfmac_f32_32x32x32_bf16 v[16:31], v[152:155], v[136:143], v161\n" \
  "v_exp_f32 v132, v116\n" \
  "v_exp_f32 v133, v117\n" \
  "v_exp_f32 v134, v118\n" \
  "v_mfma_f32_32x32x8_bf16 v[96:111], v[156:157], v[252:253], v[96:111]\n" \
  "v_exp_f32 v135, v119\n" \
  "v_exp_f32 v136, v120\n" \
  "v_exp_f32 v137, v121\n" \
  "v_mfma_f32_16x16x32_bf16 v[64:67], v[196:199], v[180:183], v[64:67]\n" \
  "v_exp_f32 v138, v122\n" \
  "v_exp_f32 v139, v123\n" \
  "v_smfmac_f32_32x32x32_bf16 v[32:47], v[148:151], v[128:135], v161\n" \
  "ds_read_b128 v[148:151], v160 offset:6144\n" \
  "v_exp_f32 v140, v124\n" \
  "v_exp_f32 v141, v125\n" \
  "v_mfma_f32_16x16x32_bf16 v[68:71], v[196:199], v[184:187], v[68:71]\n" \
  "v_exp_f32 v142, v126\n" \
  "s_add_u32 s56, s45, 8192\n" \
  "buffer_load_dwordx4 v[168:171], v10, %2, s56 offen offset:256 sc1\n" \
  "v_exp_f32 v143, v127\n" \
  "s_setprio 1\n" \
  "v_mfma_f32_32x32x16_bf16 v[112:127], v[144:147], v[248:251], 0\n" \
  "ds_read_b128 v[144:147], v158 offset:9216\n" \
  "v_exp_f32 v128, v96\n" \
  "v_exp_f32 v129, v97\n" \
  "v_mfma_f32_16x16x32_bf16 v[72:75], v[196:199], v[188:191], v[72:75]\n" \
  "v_exp_f32 v130, v98\n" \
  "v_exp_f32 v131, v99\n" \
  "v_smfmac_f32_32x32x32_bf16 v[32:47], v[152:155], v[136:143], v161\n" \
  "ds_read_b128 v[152:155], v160 offset:6912\n" \
  "v_exp_f32 v132, v100\n" \
  "v_exp_f32 v133, v101\n" \
  "v_exp_f32 v134, v102\n" \
  "v_mfma_f32_32x32x8_bf16 v[112:127], v[156:157], v[254:255], v[112:127]\n" \
  "ds_read_b64 v[156:157], v159 offset:9216\n" \
  "v_exp_f32 v135, v103\n" \
  "v_exp_f32 v136, v104\n" \
  "v_exp_f32 v137, v105\n" \
  "v_mfma_f32_16x16x32_bf16 v[76:79], v[196:199], v[192:195], v[76:79]\n" \
  "v_exp_f32 v138, v106\n" \
  "s_add_u32 s56, s44, 20480\n" \
  "buffer_load_dwordx4 v[196:199], v0, %2, s56 offen sc1\n" \
  "v_exp_f32 v139, v107\n" \
  "s_waitcnt lgkmcnt(3)\n" \
  "v_smfmac_f32_32x32x32_bf16 v[16:31], v[148:151], v[128:135], v161\n" \
  "v_exp_f32 v140, v108\n" \
  "v_exp_f32 v141, v109\n" \
  "s_waitcnt vmcnt(5)\n" \
  "v_mfma_f32_16x16x32_bf16 v[80:83], v[200:203], v[180:183], v[80:83]\n" \
  "ds_read_b32 v162, v0 offset:42848\n" \
  "v_exp_f32 v142, v110\n" \
  "v_exp_f32 v143, v111\n" \
  "s_waitcnt lgkmcnt(0)\n" \
  "v_mfma_f32_32x32x16_bf16 v[96:111], v[144:147], v[244:247], 0\n" \
  "v_exp_f32 v128, v112\n" \
  "v_exp_f32 v129, v113\n" \
  "v_mfma_f32_16x16x32_bf16 v[84:87], v[200:203], v[184:187], v[84:87]\n" \
  "v_readfirstlane_b32 s56, v162\n" \
  "s_mov_b32 s58, 0x1000000\n" \
  "s_cmp_lt_u32 s56, 32\n" \
  "s_cbranch_scc1 84f\n" \
  "94:\n" \
  "v_exp_f32 v130, v114\n" \
  "s_add_u32 s56, s46, 8192\n" \
  "buffer_load_dwordx4 v[172:175], v10, %2, s56 offen offset:0 sc1\n" \
  "v_exp_f32 v131, v115\n" \
  "v_smfmac_f32_32x32x32_bf16 v[16:31], v[152:155], v[136:143], v161\n" \
  "v_exp_f32 v132, v116\n" \
  "v_exp_f32 v133, v117\n" \
  "v_exp_f32 v134, v118\n" \
  "v_mfma_f32_32x32x8_bf16 v[96:111], v[156:157], v[252:253], v[96:111]\n" \
  "v_exp_f32 v135, v119\n" \
  "v_exp_f32 v136, v120\n" \
  "v_exp_f32 v137, v121\n" \
  "v_mfma_f32_16x16x32_bf16 v[88:91], v[200:203], v[188:191], v[88:91]\n" \
  "v_exp_f32 v138, v122\n" \
  "v_exp_f32 v139, v123\n" \
  "v_smfmac_f32_32x32x32_bf16 v[32:47], v[148:151], v[128:135], v161\n" \
  "ds_read_b128 v[148:151], v160 offset:9216\n" \
  "v_exp_f32 v140, v124\n" \
  "v_exp_f32 v141, v125\n" \
  "v_mfma_f32_16x16x32_bf16 v[92:95], v[200:203], v[192:195], v[92:95]\n" \
  "v_exp_f32 v142, v126\n" \
  "s_add_u32 s56, s44, 21504\n" \
  "buffer_load_dwordx4 v[200:203], v0, %2, s56 offen sc1\n" \
  "v_exp_f32 v143, v127\n" \
  "s_setprio 0\n" \
  "v_mfma_f32_32x32x16_bf16 v[112:127], v[144:147], v[248:251], 0\n" \
  "ds_read_b128 v[144:147], v158 offset:24576\n" \
  "v_exp_f32 v128, v96\n" \
  "v_exp_f32 v129, v97\n" \
  "s_waitcnt vmcnt(6)\n" \
  "v_mfma_f32_16x16x32_bf16 v[212:215], v[204:207], v[180:183], v[212:215]\n" \
  "v_exp_f32 v130, v98\n" \
  "v_exp_f32 v131, v99\n" \
  "v_smfmac_f32_32x32x32_bf16 v[32:47], v[152:155], v[136:143], v161\n" \
  "ds_read_b128 v[152:155], v160 offset:9984\n" \
  "v_exp_f32 v132, v100\n" \
  "v_exp_f32 v133, v101\n" \
  "s_add_u32 m0, s42, 90112\n" \
  "s_add_u32 s56, s47, 2048\n" \
  "buffer_load_dwordx4 v0, %2, s56 offen sc1 lds\n" \
  "v_exp_f32 v134, v102\n" \
  "v_mfma_f32_32x32x8_bf16 v[112:127], v[156:157], v[254:255], v[112:127]\n" \
  "ds_read_b64 v[156:157], v159 offset:24576\n" \
  "v_exp_f32 v135, v103\n" \
  "v_exp_f32 v136, v104\n" \
  "v_exp_f32 v137, v105\n" \
  "v_mfma_f32_16x16x32_bf16 v[216:219], v[204:207], v[184:187], v[216:219]\n" \
  "v_exp_f32 v138, v106\n" \
  "s_add_u32 s56, s46, 8192\n" \
  "buffer_load_dwordx4 v[176:179], v10, %2, s56 offen offset:256 sc1\n" \
  "v_exp_f32 v139, v107\n" \
  "s_waitcnt lgkmcnt(3)\n" \
  "v_smfmac_f32_32x32x32_bf16 v[16:31], v[148:151], v[128:135], v161\n" \
  "v_exp_f32 v140, v108\n" \
  "v_exp_f32 v141, v109\n" \
  "v_mfma_f32_16x16x32_bf16 v[220:223], v[204:207], v[188:191], v[220:223]\n" \
  "v_exp_f32 v142, v110\n" \
  "v_exp_f32 v143, v111\n" \
  "s_waitcnt lgkmcnt(0)\n" \
  "v_mfma_f32_32x32x16_bf16 v[96:111], v[144:147], v[244:247], 0\n" \
  "v_exp_f32 v128, v112\n" \
  "v_exp_f32 v129, v113\n" \
  "v_mfma_f32_16x16x32_bf16 v[224:227], v[204:207], v[192:195], v[224:227]\n" \
  "v_exp_f32 v130, v114\n" \
  "s_add_u32 s56, s44, 22528\n" \
  "buffer_load_dwordx4 v[204:207], v0, %2, s56 offen sc1\n" \
  "v_exp_f32 v131, v115\n" \
  "v_smfmac_f32_32x32x32_bf16 v[16:31], v[152:155], v[136:143], v161\n" \
  "v_exp_f32 v132, v116\n" \
  "v_exp_f32 v133, v117\n" \
  "s_add_u32 m0, s42, 91136\n" \
  "s_add_u32 s56, s47, 3072\n" \
  "buffer_load_dwordx4 v0, %2, s56 offen sc1 lds\n" \
  "v_exp_f32 v134, v118\n" \
  "v_mfma_f32_32x32x8_bf16 v[96:111], v[156:157], v[252:253], v[96:111]\n" \
  "v_exp_f32 v135, v119\n" \
  "v_exp_f32 v136, v120\n" \
  "v_exp_f32 v137, v121\n" \
  "s_waitcnt vmcnt(8)\n" \
  "v_mfma_f32_16x16x32_bf16 v[228:231], v[208:211], v[180:183], v[228:231]\n" \
  "v_exp_f32 v138, v122\n" \
  "v_exp_f32 v139, v123\n" \
  "v_smfmac_f32_32x32x32_bf16 v[32:47], v[148:151], v[128:135], v161\n" \
  "ds_read_b128 v[148:151], v160 offset:24576\n" \
  "v_exp_f32 v140, v124\n" \
  "v_exp_f32 v141, v125\n" \
  "v_mfma_f32_16x16x32_bf16 v[232:235], v[208:211], v[184:187], v[232:235]\n" \
  "v_exp_f32 v142, v126\n" \
  "v_exp_f32 v143, v127\n" \
  "s_setprio 3\n" \
  "v_mfma_f32_32x32x16_bf16 v[112:127], v[144:147], v[248:251], 0\n" \
  "ds_read_b128 v[144:147], v158 offset:27648\n" \
  "v_exp_f32 v128, v96\n" \
  "v_exp_f32 v129, v97\n" \
  "v_mfma_f32_16x16x32_bf16 v[236:239], v[208:211], v[188:191], v[236:239]\n" \
  "v_exp_f32 v130, v98\n" \
  "v_exp_f32 v131, v99\n" \
  "v_smfmac_f32_32x32x32_bf16 v[32:47], v[152:155], v[136:143], v161\n" \
  "ds_read_b128 v[152:155], v160 offset:25344\n" \
  "v_exp_f32 v132, v100\n" \
  "v_exp_f32 v133, v101\n" \
  "s_add_u32 m0, s42, 92160\n" \
  "s_add_u32 s56, s47, 4096\n" \
  "buffer_load_dwordx4 v0, %2, s56 offen sc1 lds\n" \
  "v_exp_f32 v134, v102\n" \
  "v_mfma_f32_32x32x8_bf16 v[112:127], v[156:157], v[254:255], v[112:127]\n" \
  "ds_read_b64 v[156:157], v159 offset:27648\n" \
  "v_exp_f32 v135, v103\n" \
  "v_exp_f32 v136, v104\n" \
  "v_exp_f32 v137, v105\n" \
  "v_mfma_f32_16x16x32_bf16 v[240:243], v[208:211], v[192:195], v[240:243]\n" \
  "v_exp_f32 v138, v106\n" \
  "s_add_u32 s56, s44, 23552\n" \
  "buffer_load_dwordx4 v[208:211], v0, %2, s56 offen sc1\n" \
  "v_exp_f32 v139, v107\n" \
  "s_waitcnt lgkmcnt(3)\n" \
  "v_smfmac_f32_32x32x32_bf16 v[16:31], v[148:151], v[128:135], v161\n" \
  "v_exp_f32 v140, v108\n" \
  "v_exp_f32 v141, v109\n" \
  "s_waitcnt vmcnt(8)\n" \
  "v_mfma_f32_16x16x32_bf16 v[48:51], v[196:199], v[164:167], v[48:51]\n" \
  "v_exp_f32 v142, v110\n" \
  "v_exp_f32 v143, v111\n" \
  "s_waitcnt lgkmcnt(0)\n" \
  "v_mfma_f32_32x32x16_bf16 v[96:111], v[144:147], v[244:247], 0\n" \
  "v_exp_f32 v128, v112\n" \
  "v_exp_f32 v129, v113\n" \
  "v_mfma_f32_16x16x32_bf16 v[52:55], v[196:199], v[168:171], v[52:55]\n" \
  "v_exp_f32 v130, v114\n" \
  "s_add_u32 s56, s45, 10240\n" \
  "buffer_load_dwordx4 v[180:183], v10, %2, s56 offen offset:0 sc1\n" \
  "v_exp_f32 v131, v115\n" \
  "v_smfmac_f32_32x32x32_bf16 v[16:31], v[152:155], v[136:143], v161\n" \
  "v_exp_f32 v132, v116\n" \
  "v_exp_f32 v133, v117\n" \
  "s_add_u32 m0, s42, 114688\n" \
  "s_add_u32 s56, s47, 5120\n" \
  "buffer_load_dwordx4 v0, %2, s56 offen sc1 lds\n" \
  "v_exp_f32 v134, v118\n" \
  "v_mfma_f32_32x32x8_bf16 v[96:111], v[156:157], v[252:253], v[96:111]\n" \
  "v_exp_f32 v135, v119\n" \
  "v_exp_f32 v136, v120\n" \
  "v_exp_f32 v137, v121\n" \
  "s_waitcnt vmcnt(9)\n" \
  "v_mfma_f32_16x16x32_bf16 v[56:59], v[196:199], v[172:175], v[56:59]\n" \
  "v_exp_f32 v138, v122\n" \
  "v_exp_f32 v139, v123\n" \
  "v_smfmac_f32_32x32x32_bf16 v[32:47], v[148:151], v[128:135], v161\n" \
  "ds_read_b128 v[148:151], v160 offset:27648\n" \
  "v_exp_f32 v140, v124\n" \
  "v_exp_f32 v141, v125\n" \
  "s_waitcnt vmcnt(6)\n" \
  "v_mfma_f32_16x16x32_bf16 v[60:63], v[196:199], v[176:179], v[60:63]\n" \
  "v_exp_f32 v142, v126\n" \
  "s_add_u32 s56, s44, 24576\n" \
  "buffer_load_dwordx4 v[196:199], v0, %2, s56 offen sc1\n" \
  "v_exp_f32 v143, v127\n" \
  "s_setprio 2\n" \
  "v_mfma_f32_32x32x16_bf16 v[112:127], v[144:147], v[248:251], 0\n" \
  "ds_read_b128 v[144:147], v158 offset:30720\n" \
  "v_exp_f32 v128, v96\n" \
  "v_exp_f32 v129, v97\n" \
  "v_mfma_f32_16x16x32_bf16 v[64:67], v[200:203], v[164:167], v[64:67]\n" \
  "v_exp_f32 v130, v98\n" \
  "v_exp_f32 v131, v99\n" \
  "v_smfmac_f32_32x32x32_bf16 v[32:47], v[152:155], v[136:143], v161\n" \
  "ds_read_b128 v[152:155], v160 offset:28416\n" \
  "v_exp_f32 v132, v100\n" \
  "v_exp_f32 v133, v101\n" \
  "s_add_u32 m0, s42, 115712\n" \
  "s_add_u32 s56, s47, 6144\n" \
  "buffer_load_dwordx4 v0, %2, s56 offen sc1 lds\n" \
  "v_exp_f32 v134, v102\n" \
  "v_mfma_f32_32x32x8_bf16 v[112:127], v[156:157], v[254:255], v[112:127]\n" \
  "ds_read_b64 v[156:157], v159 offset:30720\n" \
  "v_exp_f32 v135, v103\n" \
  "v_exp_f32 v136, v104\n" \
  "v_exp_f32 v137, v105\n" \
  "v_mfma_f32_16x16x32_bf16 v[68:71], v[200:203], v[168:171], v[68:71]\n" \
  "v_exp_f32 v138, v106\n" \
  "s_add_u32 s56, s45, 10240\n" \
  "buffer_load_dwordx4 v[184:187], v10, %2, s56 offen offset:256 sc1\n" \
  "v_exp_f32 v139, v107\n" \
  "s_waitcnt lgkmcnt(3)\n" \
  "v_smfmac_f32_32x32x32_bf16 v[16:31], v[148:151], v[128:135], v161\n" \
  "v_exp_f32 v140, v108\n" \
  "v_exp_f32 v141, v109\n" \
  "v_mfma_f32_16x16x32_bf16 v[72:75], v[200:203], v[172:175], v[72:75]\n" \
  "v_exp_f32 v142, v110\n" \
  "v_exp_f32 v143, v111\n" \
  "s_waitcnt lgkmcnt(0)\n" \
  "v_mfma_f32_32x32x16_bf16 v[96:111], v[144:147], v[244:247], 0\n" \
  "v_exp_f32 v128, v112\n" \
  "v_exp_f32 v129, v113\n" \
  "v_mfma_f32_16x16x32_bf16 v[76:79], v[200:203], v[176:179], v[76:79]\n" \
  "v_exp_f32 v130, v114\n" \
  "s_add_u32 s56, s44, 25600\n" \
  "buffer_load_dwordx4 v[200:203], v0, %2, s56 offen sc1\n" \
  "v_exp_f32 v131, v115\n" \
  "v_smfmac_f32_32x32x32_bf16 v[16:31], v[152:155], v[136:143], v161\n" \
  "v_exp_f32 v132, v116\n" \
  "v_exp_f32 v133, v117\n" \
  "s_add_u32 m0, s42, 116736\n" \
  "s_add_u32 s56, s47, 7168\n" \
  "buffer_load_dwordx4 v0, %2, s56 offen sc1 lds\n" \
  "v_exp_f32 v134, v118\n" \
  "v_mfma_f32_32x32x8_bf16 v[96:111], v[156:157], v[252:253], v[96:111]\n" \
  "v_exp_f32 v135, v119\n" \
  "v_exp_f32 v136, v120\n" \
  "v_exp_f32 v137, v121\n" \
  "s_waitcnt vmcnt(10)\n" \
  "v_mfma_f32_16x16x32_bf16 v[80:83], v[204:207], v[164:167], v[80:83]\n" \
  "v_exp_f32 v138, v122\n" \
  "v_exp_f32 v139, v123\n" \
  "v_smfmac_f32_32x32x32_bf16 v[32:47], v[148:151], v[128:135], v161\n" \
  "ds_read_b128 v[148:151], v160 offset:30720\n" \
  "v_exp_f32 v140, v124\n" \
  "v_exp_f32 v141, v125\n" \
  "v_mfma_f32_16x16x32_bf16 v[84:87], v[204:207], v[168:171], v[84:87]\n" \
  "v_exp_f32 v142, v126\n" \
  "s_add_u32 s56, s46, 10240\n" \
  "buffer_load_dwordx4 v[188:191], v10, %2, s56 offen offset:0 sc1\n" \
  "v_exp_f32 v143, v127\n" \
  "s_setprio 1\n" \
  "v_mfma_f32_32x32x16_bf16 v[112:127], v[144:147], v[248:251], 0\n" \
  "ds_read_b128 v[144:147], v158 offset:33792\n" \
  "v_exp_f32 v128, v96\n" \
  "v_exp_f32 v129, v97\n" \
  "v_mfma_f32_16x16x32_bf16 v[88:91], v[204:207], v[172:175], v[88:91]\n" \
  "v_exp_f32 v130, v98\n" \
  "v_exp_f32 v131, v99\n" \
  "v_smfmac_f32_32x32x32_bf16 v[32:47], v[152:155], v[136:143], v161\n" \
  "ds_read_b128 v[152:155], v160 offset:31488\n" \
  "v_exp_f32 v132, v100\n" \
  "v_exp_f32 v133, v101\n" \
  "v_exp_f32 v134, v102\n" \
  "v_mfma_f32_32x32x8_bf16 v[112:127], v[156:157], v[254:255], v[112:127]\n" \
  "ds_read_b64 v[156:157], v159 offset:33792\n" \
  "v_exp_f32 v135, v103\n" \
  "v_exp_f32 v136, v104\n" \
  "v_exp_f32 v137, v105\n" \
  "v_mfma_f32_16x16x32_bf16 v[92:95], v[204:207], v[176:179], v[92:95]\n" \
  "v_exp_f32 v138, v106\n" \
  "s_add_u32 s56, s44, 26624\n" \
  "buffer_load_dwordx4 v[204:207], v0, %2, s56 offen sc1\n" \
  "v_exp_f32 v139, v107\n" \
  "s_waitcnt lgkmcnt(3)\n" \
  "v_smfmac_f32_32x32x32_bf16 v[16:31], v[148:151], v[128:135], v161\n" \
  "v_exp_f32 v140, v108\n" \
  "v_exp_f32 v141, v109\n" \
  "s_waitcnt vmcnt(9)\n" \
  "v_mfma_f32_16x16x32_bf16 v[212:215], v[208:211], v[164:167], v[212:215]\n" \
  "v_exp_f32 v142, v110\n" \
  "v_exp_f32 v143, v111\n" \
  "s_waitcnt lgkmcnt(0)\n" \
  "v_mfma_f32_32x32x16_bf16 v[96:111], v[144:147], v[244:247], 0\n" \
  "v_exp_f32 v128, v112\n" \
  "v_exp_f32 v129, v113\n" \
  "v_mfma_f32_16x16x32_bf16 v[216:219], v[208:211], v[168:171], v[216:219]\n" \
  "v_exp_f32 v130, v114\n" \
  "s_add_u32 s56, s46, 10240\n" \
  "buffer_load_dwordx4 v[192:195], v10, %2, s56 offen offset:256 sc1\n" \
  "v_exp_f32 v131, v115\n" \
  "v_smfmac_f32_32x32x32_bf16 v[16:31], v[152:155], v[136:143], v161\n" \
  "v_exp_f32 v132, v116\n" \
  "v_exp_f32 v133, v117\n" \
  "v_exp_f32 v134, v118\n" \
  "v_mfma_f32_32x32x8_bf16 v[96:111], v[156:157], v[252:253], v[96:111]\n" \
  "v_exp_f32 v135, v119\n" \
  "v_exp_f32 v136, v120\n" \
  "v_exp_f32 v137, v121\n" \
  "v_mfma_f32_16x16x32_bf16 v[220:223], v[208:211], v[172:175], v[220:223]\n" \
  "v_exp_f32 v138, v122\n" \
  "v_exp_f32 v139, v123\n" \
  "v_smfmac_f32_32x32x32_bf16 v[32:47], v[148:151], v[128:135], v161\n" \
  "ds_read_b128 v[148:151], v160 offset:33792\n" \
  "v_exp_f32 v140, v124\n" \
  "v_exp_f32 v141, v125\n" \
  "v_mfma_f32_16x16x32_bf16 v[224:227], v[208:211], v[176:179], v[224:227]\n" \
  "v_exp_f32 v142, v126\n" \
  "s_add_u32 s56, s44, 27648\n" \
  "buffer_load_dwordx4 v[208:211], v0, %2, s56 offen sc1\n" \
  "v_exp_f32 v143, v127\n" \
  "s_setprio 0\n" \
  "v_mfma_f32_32x32x16_bf16 v[112:127], v[144:147], v[248:251], 0\n" \
  "ds_read_b128 v[144:147], v158 offset:49152\n" \
  "v_exp_f32 v128, v96\n" \
  "v_exp_f32 v129, v97\n" \
  "s_waitcnt vmcnt(8)\n" \
  "v_mfma_f32_16x16x32_bf16 v[228:231], v[196:199], v[164:167], v[228:231]\n" \
  "v_exp_f32 v130, v98\n" \
  "v_exp_f32 v131, v99\n" \
  "v_smfmac_f32_32x32x32_bf16 v[32:47], v[152:155], v[136:143], v161\n" \
  "ds_read_b128 v[152:155], v160 offset:34560\n" \
  "v_exp_f32 v132, v100\n" \
  "v_exp_f32 v133, v101\n" \
  "v_exp_f32 v134, v102\n" \
  "v_mfma_f32_32x32x8_bf16 v[112:127], v[156:157], v[254:255], v[112:127]\n" \
  "ds_read_b64 v[156:157], v159 offset:49152\n" \
  "v_exp_f32 v135, v103\n" \
  "s_add_u32 s56, s42, 139264\n" \
  "v_add_u32 v2, s56, v0\n" \
  "v_add_u32 v3, s73, v0\n" \
  "v_and_b32 v4, 15, v1\n" \
  "v_lshrrev_b32 v5, 4, v1\n" \
  "v_mul_u32_u24 v4, 1280, v4\n" \
  "v_lshl_add_u32 v6, v5, 4, v4\n" \
  "v_lshl_add_u32 v4, v5, 3, v4\n" \
  "v_lshrrev_b32 v12, 1, v0\n" \
  "v_lshrrev_b32 v11, 1, v10\n" \
  "v_and_b32 v15, 3, v1\n" \
  "v_lshlrev_b32 v15, 2, v15\n" \
  "v_exp_f32 v136, v104\n" \
  "v_exp_f32 v137, v105\n" \
  "v_mfma_f32_16x16x32_bf16 v[232:235], v[196:199], v[168:171], v[232:235]\n" \
  "v_exp_f32 v138, v106\n" \
  "v_exp_f32 v139, v107\n" \
  "s_waitcnt lgkmcnt(3)\n" \
  "v_smfmac_f32_32x32x32_bf16 v[16:31], v[148:151], v[128:135], v161\n" \
  "v_exp_f32 v140, v108\n" \
  "v_exp_f32 v141, v109\n" \
  "v_mfma_f32_16x16x32_bf16 v[236:239], v[196:199], v[172:175], v[236:239]\n" \
  "v_exp_f32 v142, v110\n" \
  "v_exp_f32 v143, v111\n" \
  "s_waitcnt lgkmcnt(0)\n" \
  "v_mfma_f32_32x32x16_bf16 v[96:111], v[144:147], v[244:247], 0\n" \
  "v_exp_f32 v128, v112\n" \
  "v_exp_f32 v129, v113\n" \
  "v_mfma_f32_16x16x32_bf16 v[240:243], v[196:199], v[176:179], v[240:243]\n" \
  "v_exp_f32 v130, v114\n" \
  "s_add_u32 s56, s44, 28672\n" \
  "buffer_load_dwordx4 v[196:199], v0, %2, s56 offen sc1\n" \
  "v_exp_f32 v131, v115\n" \
  "v_smfmac_f32_32x32x32_bf16 v[16:31], v[152:155], v[136:143], v161\n" \
  "v_exp_f32 v132, v116\n" \
  "v_exp_f32 v133, v117\n" \
  "v_exp_f32 v134, v118\n" \
  "v_mfma_f32_32x32x8_bf16 v[96:111], v[156:157], v[252:253], v[96:111]\n" \
  "v_exp_f32 v135, v119\n" \
  "v_exp_f32 v136, v120\n" \
  "v_exp_f32 v137, v121\n" \
  "s_waitcnt vmcnt(6)\n" \
  "v_mfma_f32_16x16x32_bf16 v[48:51], v[200:203], v[180:183], v[48:51]\n" \
  "v_exp_f32 v138, v122\n" \
  "v_exp_f32 v139, v123\n" \
  "v_smfmac_f32_32x32x32_bf16 v[32:47], v[148:151], v[128:135], v161\n" \
  "ds_read_b128 v[148:151], v160 offset:49152\n" \
  "v_exp_f32 v140, v124\n" \
  "v_exp_f32 v141, v125\n" \
  "v_mfma_f32_16x16x32_bf16 v[52:55], v[200:203], v[184:187], v[52:55]\n" \
  "v_exp_f32 v142, v126\n" \
  "v_exp_f32 v143, v127\n" \
  "s_setprio 3\n" \
  "v_mfma_f32_32x32x16_bf16 v[112:127], v[144:147], v[248:251], 0\n" \
  "ds_read_b128 v[144:147], v158 offset:52224\n" \
  "v_exp_f32 v128, v96\n" \
  "v_exp_f32 v129, v97\n" \
  "s_waitcnt vmcnt(4)\n" \
  "v_mfma_f32_16x16x32_bf16 v[56:59], v[200:203], v[188:191], v[56:59]\n" \
  "v_exp_f32 v130, v98\n" \
  "v_exp_f32 v131, v99\n" \
  "v_smfmac_f32_32x32x32_bf16 v[32:47], v[152:155], v[136:143], v161\n" \
  "ds_read_b128 v[152:155], v160 offset:49920\n" \
  "v_exp_f32 v132, v100\n" \
  "v_exp_f32 v133, v101\n" \
  "s_add_u32 s56, s47, 8192\n" \
  "buffer_load_dwordx4 v[164:167], v0, %2, s56 offen sc1\n" \
  "v_exp_f32 v134, v102\n" \
  "v_mfma_f32_32x32x8_bf16 v[112:127], v[156:157], v[254:255], v[112:127]\n" \
  "ds_read_b64 v[156:157], v159 offset:52224\n" \
  "v_exp_f32 v135, v103\n" \
  "v_exp_f32 v136, v104\n" \
  "v_exp_f32 v137, v105\n" \
  "s_waitcnt vmcnt(3)\n" \
  "v_mfma_f32_16x16x32_bf16 v[60:63], v[200:203], v[192:195], v[60:63]\n" \
  "v_exp_f32 v138, v106\n" \
  "s_add_u32 s56, s44, 29696\n" \
  "buffer_load_dwordx4 v[200:203], v0, %2, s56 offen sc1\n" \
  "v_exp_f32 v139, v107\n" \
  "s_waitcnt lgkmcnt(3)\n" \
  "v_smfmac_f32_32x32x32_bf16 v[16:31], v[148:151], v[128:135], v161\n" \
  "v_exp_f32 v140, v108\n" \
  "s_add_u32 s56, s47, 9216\n" \
  "buffer_load_dwordx4 v[168:171], v0, %2, s56 offen sc1\n" \
  "v_exp_f32 v141, v109\n" \
  "v_mfma_f32_16x16x32_bf16 v[64:67], v[204:207], v[180:183], v[64:67]\n" \
  "v_exp_f32 v142, v110\n" \
  "v_exp_f32 v143, v111\n" \
  "s_waitcnt lgkmcnt(0)\n" \
  "v_mfma_f32_32x32x16_bf16 v[96:111], v[144:147], v[244:247], 0\n" \
  "v_exp_f32 v128, v112\n" \
  "v_exp_f32 v129, v113\n" \
  "v_mfma_f32_16x16x32_bf16 v[68:71], v[204:207], v[184:187], v[68:71]\n" \
  "v_exp_f32 v130, v114\n" \
  "v_exp_f32 v131, v115\n" \
  "v_smfmac_f32_32x32x32_bf16 v[16:31], v[152:155], v[136:143], v161\n" \
  "v_exp_f32 v132, v116\n" \
  "v_exp_f32 v133, v117\n" \
  "s_add_u32 s56, s48, 1024\n" \
  "buffer_load_dwordx2 v[172:173], v0, %2, s56 offen sc1\n" \
  "v_exp_f32 v134, v118\n" \
  "v_mfma_f32_32x32x8_bf16 v[96:111], v[156:157], v[252:253], v[96:111]\n" \
  "v_exp_f32 v135, v119\n" \
  "v_exp_f32 v136, v120\n" \
  "v_exp_f32 v137, v121\n" \
  "v_mfma_f32_16x16x32_bf16 v[72:75], v[204:207], v[188:191], v[72:75]\n" \
  "v_exp_f32 v138, v122\n" \
  "v_exp_f32 v139, v123\n" \
  "v_smfmac_f32_32x32x32_bf16 v[32:47], v[148:151], v[128:135], v161\n" \
  "ds_read_b128 v[148:151], v160 offset:52224\n" \
  "v_exp_f32 v140, v124\n" \
  "v_exp_f32 v141, v125\n" \
  "v_mfma_f32_16x16x32_bf16 v[76:79], v[204:207], v[192:195], v[76:79]\n" \
  "v_exp_f32 v142, v126\n" \
  "v_exp_f32 v143, v127\n" \
  "s_setprio 2\n" \
  "v_mfma_f32_32x32x16_bf16 v[112:127], v[144:147], v[248:251], 0\n" \
  "ds_read_b128 v[144:147], v158 offset:55296\n" \
  "v_exp_f32 v128, v96\n" \
  "v_exp_f32 v129, v97\n" \
  "s_waitcnt vmcnt(5)\n" \
  "v_mfma_f32_16x16x32_bf16 v[80:83], v[208:211], v[180:183], v[80:83]\n" \
  "v_exp_f32 v130, v98\n" \
  "v_exp_f32 v131, v99\n" \
  "v_smfmac_f32_32x32x32_bf16 v[32:47], v[152:155], v[136:143], v161\n" \
  "ds_read_b128 v[152:155], v160 offset:52992\n" \
  "v_exp_f32 v132, v100\n" \
  "v_exp_f32 v133, v101\n" \
  "s_add_u32 s56, s48, 2048\n" \
  "buffer_load_dwordx2 v[174:175], v0, %2, s56 offen sc1\n" \
  "v_exp_f32 v134, v102\n" \
  "v_mfma_f32_32x32x8_bf16 v[112:127], v[156:157], v[254:255], v[112:127]\n" \
  "ds_read_b64 v[156:157], v159 offset:55296\n" \
  "v_exp_f32 v135, v103\n" \
  "v_exp_f32 v136, v104\n" \
  "v_exp_f32 v137, v105\n" \
  "v_mfma_f32_16x16x32_bf16 v[84:87], v[208:211], v[184:187], v[84:87]\n" \
  "v_exp_f32 v138, v106\n" \
  "v_exp_f32 v139, v107\n" \
  "s_waitcnt lgkmcnt(3)\n" \
  "v_smfmac_f32_32x32x32_bf16 v[16:31], v[148:151], v[128:135], v161\n" \
  "v_exp_f32 v140, v108\n" \
  "s_add_u32 s56, s48, 3072\n" \
  "buffer_load_dwordx2 v[176:177], v0, %2, s56 offen sc1\n" \
  "v_exp_f32 v141, v109\n" \
  "v_mfma_f32_16x16x32_bf16 v[88:91], v[208:211], v[188:191], v[88:91]\n" \
  "v_exp_f32 v142, v110\n" \
  "v_exp_f32 v143, v111\n" \
  "s_waitcnt lgkmcnt(0)\n" \
  "v_mfma_f32_32x32x16_bf16 v[96:111], v[144:147], v[244:247], 0\n" \
  "v_exp_f32 v128, v112\n" \
  "v_exp_f32 v129, v113\n" \
  "v_mfma_f32_16x16x32_bf16 v[92:95], v[208:211], v[192:195], v[92:95]\n" \
  "v_exp_f32 v130, v114\n" \
  "v_exp_f32 v131, v115\n" \
  "v_smfmac_f32_32x32x32_bf16 v[16:31], v[152:155], v[136:143], v161\n" \
  "v_exp_f32 v132, v116\n" \
  "v_exp_f32 v133, v117\n" \
  "s_add_u32 s56, s48, 4096\n" \
  "buffer_load_dwordx2 v[178:179], v0, %2, s56 offen sc1\n" \
  "v_exp_f32 v134, v118\n" \
  "v_mfma_f32_32x32x8_bf16 v[96:111], v[156:157], v[252:253], v[96:111]\n" \
  "v_exp_f32 v135, v119\n" \
  "v_exp_f32 v136, v120\n" \
  "v_exp_f32 v137, v121\n" \
  "s_waitcnt vmcnt(7)\n" \
  "v_mfma_f32_16x16x32_bf16 v[212:215], v[196:199], v[180:183], v[212:215]\n" \
  "v_exp_f32 v138, v122\n" \
  "v_exp_f32 v139, v123\n" \
  "v_smfmac_f32_32x32x32_bf16 v[32:47], v[148:151], v[128:135], v161\n" \
  "ds_read_b128 v[148:151], v160 offset:55296\n" \
  "v_exp_f32 v140, v124\n" \
  "v_exp_f32 v141, v125\n" \
  "v_mfma_f32_16x16x32_bf16 v[216:219], v[196:199], v[184:187], v[216:219]\n" \
  "v_exp_f32 v142, v126\n" \
  "v_exp_f32 v143, v127\n" \
  "s_setprio 1\n" \
  "v_mfma_f32_32x32x16_bf16 v[112:127], v[144:147], v[248:251], 0\n" \
  "ds_read_b128 v[144:147], v158 offset:58368\n" \
  "v_exp_f32 v128, v96\n" \
  "v_exp_f32 v129, v97\n" \
  "v_mfma_f32_16x16x32_bf16 v[220:223], v[196:199], v[188:191], v[220:223]\n" \
  "v_exp_f32 v130, v98\n" \
  "v_exp_f32 v131, v99\n" \
  "v_smfmac_f32_32x32x32_bf16 v[32:47], v[152:155], v[136:143], v161\n" \
  "ds_read_b128 v[152:155], v160 offset:56064\n" \
  "v_exp_f32 v132, v100\n" \
  "v_exp_f32 v133, v101\n" \
  "v_exp_f32 v134, v102\n" \
  "v_mfma_f32_32x32x8_bf16 v[112:127], v[156:157], v[254:255], v[112:127]\n" \
  "ds_read_b64 v[156:157], v159 offset:58368\n" \
  "v_exp_f32 v135, v103\n" \
  "v_exp_f32 v136, v104\n" \
  "v_exp_f32 v137, v105\n" \
  "v_mfma_f32_16x16x32_bf16 v[224:227], v[196:199], v[192:195], v[224:227]\n" \
  "v_exp_f32 v138, v106\n" \
  "v_exp_f32 v139, v107\n" \
  "s_waitcnt lgkmcnt(3)\n" \
  "v_smfmac_f32_32x32x32_bf16 v[16:31], v[148:151], v[128:135], v161\n" \
  "v_exp_f32 v140, v108\n" \
  "v_exp_f32 v141, v109\n" \
  "s_waitcnt vmcnt(5)\n" \
  "v_mfma_f32_16x16x32_bf16 v[228:231], v[200:203], v[180:183], v[228:231]\n" \
  "v_exp_f32 v142, v110\n" \
  "v_exp_f32 v143, v111\n" \
  "s_waitcnt lgkmcnt(0)\n" \
  "v_mfma_f32_32x32x16_bf16 v[96:111], v[144:147], v[244:247], 0\n" \
  "v_exp_f32 v128, v112\n" \
  "v_exp_f32 v129, v113\n" \
  "v_mfma_f32_16x16x32_bf16 v[232:235], v[200:203], v[184:187], v[232:235]\n" \
  "v_exp_f32 v130, v114\n" \
  "v_exp_f32 v131, v115\n" \
  "v_smfmac_f32_32x32x32_bf16 v[16:31], v[152:155], v[136:143], v161\n" \
  "v_exp_f32 v132, v116\n" \
  "v_exp_f32 v133, v117\n" \
  "v_exp_f32 v134, v118\n" \
  "v_mfma_f32_32x32x8_bf16 v[96:111], v[156:157], v[252:253], v[96:111]\n" \
  "v_exp_f32 v135, v119\n" \
  "v_exp_f32 v136, v120\n" \
  "v_exp_f32 v137, v121\n" \
  "v_mfma_f32_16x16x32_bf16 v[236:239], v[200:203], v[188:191], v[236:239]\n" \
  "v_exp_f32 v138, v122\n" \
  "v_exp_f32 v139, v123\n" \
  "v_smfmac_f32_32x32x32_bf16 v[32:47], v[148:151], v[128:135], v161\n" \
  "ds_read_b128 v[148:151], v160 offset:58368\n" \
  "v_exp_f32 v140, v124\n" \
  "v_exp_f32 v141, v125\n" \
  "v_mfma_f32_16x16x32_bf16 v[240:243], v[200:203], v[192:195], v[240:243]\n" \
  "v_exp_f32 v142, v126\n" \
  "v_exp_f32 v143, v127\n" \
  "s_setprio 0\n" \
  "v_mfma_f32_32x32x16_bf16 v[112:127], v[144:147], v[248:251], 0\n" \
  "v_exp_f32 v128, v96\n" \
  "v_exp_f32 v129, v97\n" \
  "v_exp_f32 v130, v98\n" \
  "v_exp_f32 v131, v99\n" \
  "v_smfmac_f32_32x32x32_bf16 v[32:47], v[152:155], v[136:143], v161\n" \
  "ds_read_b128 v[152:155], v160 offset:59136\n" \
  "v_exp_f32 v132, v100\n" \
  "v_exp_f32 v133, v101\n" \
  "v_exp_f32 v134, v102\n" \
  "v_mfma_f32_32x32x8_bf16 v[112:127], v[156:157], v[254:255], v[112:127]\n" \
  "v_exp_f32 v135, v103\n" \
  "v_exp_f32 v136, v104\n" \
  "v_exp_f32 v137, v105\n" \
  "v_exp_f32 v138, v106\n" \
  "v_exp_f32 v139, v107\n" \
  "s_waitcnt lgkmcnt(1)\n" \
  "v_smfmac_f32_32x32x32_bf16 v[16:31], v[148:151], v[128:135], v161\n" \
  "v_exp_f32 v140, v108\n" \
  "v_exp_f32 v141, v109\n" \
  "v_exp_f32 v142, v110\n" \
  "v_exp_f32 v143, v111\n" \
  "s_waitcnt lgkmcnt(0)\n" \
  "v_exp_f32 v128, v112\n" \
  "v_exp_f32 v129, v113\n" \
  "v_exp_f32 v130, v114\n" \
  "v_smfmac_f32_32x32x32_bf16 v[16:31], v[152:155], v[136:143], v161\n" \
  "v_exp_f32 v131, v115\n" \
  "v_mov_b32 v7, 0x7ffffff0\n" \
  "s_nop 0\n" \
  "v_writelane_b32 v7, s74, 0\n" \
  "v_exp_f32 v132, v116\n" \
  "v_exp_f32 v133, v117\n" \
  "v_exp_f32 v134, v118\n" \
  "v_exp_f32 v135, v119\n" \
  "buffer_store_dword v163, v7, %2, 0 offen sc1\n" \
  "v_exp_f32 v136, v120\n" \
  "v_exp_f32 v137, v121\n" \
  "v_exp_f32 v138, v122\n" \
  "v_exp_f32 v139, v123\n" \
  "v_smfmac_f32_32x32x32_bf16 v[32:47], v[148:151], v[128:135], v161\n" \
  "v_exp_f32 v140, v124\n" \
  "ds_write_b128 v2, v[16:19] offset:0\n" \
  "v_exp_f32 v141, v125\n" \
  "ds_write_b128 v2, v[20:23] offset:1024\n" \
  "v_exp_f32 v142, v126\n" \
  "ds_write_b128 v2, v[24:27] offset:2048\n" \
  "v_exp_f32 v143, v127\n" \
  "s_nop 1\n" \
  "v_smfmac_f32_32x32x32_bf16 v[32:47], v[152:155], v[136:143], v161\n" \
  "s_waitcnt lgkmcnt(0)\n" \
  "s_barrier\n" \
  "buffer_load_dword v13, v15, %2, s78 offen sc1\n" \
  "ds_read_b128 v[24:27], v3 offset:2048\n" \
  "ds_read_b128 v[16:19], v3 offset:0\n" \
  "ds_read_b128 v[20:23], v3 offset:1024\n" \
  "s_mov_b32 s56, 0x2b800000\n" \
  "s_mov_b32 s59, -2\n" \
  "s_mov_b32 s58, 0x40000\n" \
  "s_nop 3\n" \
  "s_waitcnt lgkmcnt(2)\n" \
  "v_pk_add_f32 v[40:41], v[40:41], v[24:25]\n" \
  "v_pk_add_f32 v[42:43], v[42:43], v[26:27]\n" \
  "v_mov_b32 v125, v40\n" \
  "v_mov_b32 v124, v40\n" \
  "s_waitcnt lgkmcnt(0)\n" \
  "v_pk_add_f32 v[32:33], v[32:33], v[16:17]\n" \
  "v_permlane32_swap_b32 v125, v124\n" \
  "v_pk_add_f32 v[34:35], v[34:35], v[18:19]\n" \
  "v_pk_add_f32 v[36:37], v[36:37], v[20:21]\n" \
  "v_pk_add_f32 v[38:39], v[38:39], v[22:23]\n" \
  "v_cmp_nle_f32 s[80:81], s56, v124\n" \
  "v_rcp_f32 v124, v124\n" \
  "v_pk_mul_f32 v[126:127], v[40:41], 0 op_sel_hi:[1,0]\n" \
  "v_pk_fma_f32 v[126:127], v[42:43], 0, v[126:127] op_sel_hi:[1,0,1]\n" \
  "v_pk_fma_f32 v[126:127], v[32:33], 0, v[126:127] op_sel_hi:[1,0,1]\n" \
  "v_pk_fma_f32 v[126:127], v[34:35], 0, v[126:127] op_sel_hi:[1,0,1]\n" \
  "v_pk_fma_f32 v[126:127], v[36:37], 0, v[126:127] op_sel_hi:[1,0,1]\n" \
  "v_pk_fma_f32 v[126:127], v[38:39], 0, v[126:127] op_sel_hi:[1,0,1]\n" \
  "v_cmp_u_f32 vcc, v126, v127\n" \
  "v_pk_mul_f32 v[40:41], v[40:41], v[124:125] op_sel_hi:[1,0]\n" \
  "v_pk_mul_f32 v[42:43], v[42:43], v[124:125] op_sel_hi:[1,0]\n" \
  "v_pk_mul_f32 v[32:33], v[32:33], v[124:125] op_sel_hi:[1,0]\n" \
  "v_pk_mul_f32 v[34:35], v[34:35], v[124:125] op_sel_hi:[1,0]\n" \
  "v_pk_mul_f32 v[36:37], v[36:37], v[124:125] op_sel_hi:[1,0]\n" \
  "v_pk_mul_f32 v[38:39], v[38:39], v[124:125] op_sel_hi:[1,0]\n" \
  "v_cvt_pk_bf16_f32 v20, v40, v41\n" \
  "v_cvt_pk_bf16_f32 v21, v42, v43\n" \
  "v_cvt_pk_bf16_f32 v16, v32, v33\n" \
  "v_cvt_pk_bf16_f32 v17, v34, v35\n" \
  "v_cvt_pk_bf16_f32 v18, v36, v37\n" \
  "v_cvt_pk_bf16_f32 v19, v38, v39\n" \
  "s_or_b64 vcc, vcc, s[80:81]\n" \
  "s_cbranch_scc1 30f\n" \
  "31:\n" \
  "buffer_store_dwordx4 v[16:19], v0, %2, s64 offen sc1\n" \
  "v_mov_b32_dpp v22, v20 quad_perm:[1,1,3,3] row_mask:0xf bank_mask:0xf\n" \
  "v_mov_b32_dpp v23, v21 quad_perm:[1,1,3,3] row_mask:0xf bank_mask:0xf\n" \
  "s_mov_b32 exec_hi, 0\n" \
  "s_mov_b32 exec_lo, 0x55555555\n" \
  "buffer_store_dwordx4 v[20:23], v12, %2, s65 offen sc1\n" \
  "s_mov_b64 exec, -1\n" \
  "s_setprio 0\n" \
  "s_waitcnt vmcnt(4)\n" \
  "v_mov_b32 v124, s97\n" \
  "v_add_u32 v2, s49, v0\n" \
  "s_add_u32 s56, s42, 90112\n" \
  "v_add_u32 v3, s56, v0\n" \
  "ds_read_b128 v[132:135], v2 offset:0\n" \
  "ds_read_b128 v[136:139], v2 offset:1024\n" \
  "ds_read_b128 v[140:143], v3 offset:0\n" \
  "ds_read_b128 v[144:147], v3 offset:1024\n" \
  "ds_read_b128 v[148:151], v3 offset:2048\n" \
  "ds_read_b128 v[152:155], v3 offset:24576\n" \
  "ds_read_b128 v[156:159], v3 offset:25600\n" \
  "ds_read_b128 v[160:163], v3 offset:26624\n" \
  "v_add_u32 v2, s71, v0\n" \
  "v_lshrrev_b32 v3, 5, v1\n" \
  "v_mul_u32_u24 v3, 3072, v3\n" \
  "v_add3_u32 v3, v3, v10, s72\n" \
  "s_waitcnt lgkmcnt(0)\n" \
  "s_barrier\n" \
  "s_waitcnt vmcnt(2)\n" \
  "v_cmp_ne_u32 vcc, v13, v124\n" \
  "s_nop 1\n" \
  "s_cbranch_vccz 34f\n" \
  "33:\n" \
  "buffer_load_dword v13, v15, %2, s78 offen sc1\n" \
  "s_waitcnt vmcnt(0)\n" \
  "v_cmp_ne_u32 vcc, v13, v124\n" \
  "s_sub_u32 s58, s58, 1\n" \
  "s_cbranch_scc1 34f\n" \
  "s_cbranch_vccz 34f\n" \
  "s_sleep 6\n" \
  "s_branch 33b\n" \
  "34:\n" \
  "s_mov_b32 s58, 0x40000\n" \
  "buffer_load_dwordx4 v[180:183], v10, %2, s66 offen offset:0 sc1\n" \
  "buffer_load_dwordx4 v[196:199], v10, %2, s66 offen offset:2048 sc1\n" \
  "s_add_u32 s56, s66, s79\n" \
  "buffer_load_dwordx2 v[96:97], v11, %2, s56 offen offset:0 sc1\n" \
  "buffer_load_dwordx4 v[184:187], v10, %2, s66 offen offset:256 sc1\n" \
  "buffer_load_dwordx4 v[200:203], v10, %2, s66 offen offset:2304 sc1\n" \
  "s_add_u32 s56, s66, s79\n" \
  "buffer_load_dwordx2 v[98:99], v11, %2, s56 offen offset:128 sc1\n" \
  "buffer_load_dwordx4 v[188:191], v10, %2, s67 offen offset:0 sc1\n" \
  "buffer_load_dwordx4 v[204:207], v10, %2, s67 offen offset:2048 sc1\n" \
  "s_add_u32 s56, s67, s79\n" \
  "buffer_load_dwordx2 v[100:101], v11, %2, s56 offen offset:0 sc1\n" \
  "buffer_load_dwordx4 v[192:195], v10, %2, s67 offen offset:256 sc1\n" \
  "buffer_load_dwordx4 v[208:211], v10, %2, s67 offen offset:2304 sc1\n" \
  "s_add_u32 s56, s67, s79\n" \
  "buffer_load_dwordx2 v[102:103], v11, %2, s56 offen offset:128 sc1\n" \
  "buffer_load_dwordx4 v[244:247], v6, %3, s68 offen\n" \
  "buffer_load_dwordx2 v[248:249], v4, %3, s69 offen\n" \
  "40:\n" \
  "s_waitcnt vmcnt(13)\n" \
  "v_max3_u32 v125, v180, v181, v182\n" \
  "v_max_u32 v125, v125, v183\n" \
  "v_cmp_lt_u32 vcc, s59, v125\n" \
  "s_nop 1\n" \
  "s_cbranch_vccnz 60f\n" \
  "v_mfma_f32_16x16x32_bf16 v[48:51], v[132:135], v[180:183], v[48:51]\n" \
  "v_mfma_f32_16x16x32_bf16 v[64:67], v[136:139], v[180:183], v[64:67]\n" \
  "v_mfma_f32_16x16x32_bf16 v[80:83], v[140:143], v[180:183], v[80:83]\n" \
  "v_mfma_f32_16x16x32_bf16 v[212:215], v[144:147], v[180:183], v[212:215]\n" \
  "v_mfma_f32_16x16x32_bf16 v[228:231], v[148:151], v[180:183], v[228:231]\n" \
  "44:\n" \
  "s_waitcnt vmcnt(12)\n" \
  "v_max3_u32 v125, v196, v197, v198\n" \
  "v_max_u32 v125, v125, v199\n" \
  "v_cmp_lt_u32 vcc, s59, v125\n" \
  "s_nop 1\n" \
  "s_cbranch_vccnz 64f\n" \
  "v_mfma_f32_16x16x32_bf16 v[48:51], v[152:155], v[196:199], v[48:51]\n" \
  "v_mfma_f32_16x16x32_bf16 v[64:67], v[156:159], v[196:199], v[64:67]\n" \
  "v_mfma_f32_16x16x32_bf16 v[80:83], v[160:163], v[196:199], v[80:83]\n" \
  "v_mfma_f32_16x16x32_bf16 v[212:215], v[164:167], v[196:199], v[212:215]\n" \
  "v_mfma_f32_16x16x32_bf16 v[228:231], v[168:171], v[196:199], v[228:231]\n" \
  "48:\n" \
  "s_waitcnt vmcnt(11)\n" \
  "v_max_u32 v125, v96, v97\n" \
  "v_cmp_lt_u32 vcc, s59, v125\n" \
  "s_nop 1\n" \
  "s_cbranch_vccnz 68f\n" \
  "v_mfma_f32_16x16x16_bf16 v[48:51], v[8:9], v[96:97], v[48:51]\n" \
  "v_mfma_f32_16x16x16_bf16 v[64:67], v[172:173], v[96:97], v[64:67]\n" \
  "v_mfma_f32_16x16x16_bf16 v[80:83], v[174:175], v[96:97], v[80:83]\n" \
  "v_mfma_f32_16x16x16_bf16 v[212:215], v[176:177], v[96:97], v[212:215]\n" \
  "v_mfma_f32_16x16x16_bf16 v[228:231], v[178:179], v[96:97], v[228:231]\n" \
  "41:\n" \
  "s_waitcnt vmcnt(10)\n" \
  "v_max3_u32 v125, v184, v185, v186\n" \
  "v_max_u32 v125, v125, v187\n" \
  "v_cmp_lt_u32 vcc, s59, v125\n" \
  "s_nop 1\n" \
  "s_cbranch_vccnz 61f\n" \
  "v_mfma_f32_16x16x32_bf16 v[52:55], v[132:135], v[184:187], v[52:55]\n" \
  "v_mfma_f32_16x16x32_bf16 v[68:71], v[136:139], v[184:187], v[68:71]\n" \
  "v_mfma_f32_16x16x32_bf16 v[84:87], v[140:143], v[184:187], v[84:87]\n" \
  "v_mfma_f32_16x16x32_bf16 v[216:219], v[144:147], v[184:187], v[216:219]\n" \
  "v_mfma_f32_16x16x32_bf16 v[232:235], v[148:151], v[184:187], v[232:235]\n" \
  "45:\n" \
  "s_waitcnt vmcnt(9)\n" \
  "v_max3_u32 v125, v200, v201, v202\n" \
  "v_max_u32 v125, v125, v203\n" \
  "v_cmp_lt_u32 vcc, s59, v125\n" \
  "s_nop 1\n" \
  "s_cbranch_vccnz 65f\n" \
  "v_mfma_f32_16x16x32_bf16 v[52:55], v[152:155], v[200:203], v[52:55]\n" \
  "ds_write_b128 v2, v[48:51] offset:0\n" \
  "v_mfma_f32_16x16x32_bf16 v[68:71], v[156:159], v[200:203], v[68:71]\n" \
  "ds_write_b128 v2, v[64:67] offset:4096\n" \
  "v_mfma_f32_16x16x32_bf16 v[84:87], v[160:163], v[200:203], v[84:87]\n" \
  "ds_write_b128 v2, v[80:83] offset:8192\n" \
  "v_mfma_f32_16x16x32_bf16 v[216:219], v[164:167], v[200:203], v[216:219]\n" \
  "ds_write_b128 v2, v[212:215] offset:12288\n" \
  "v_mfma_f32_16x16x32_bf16 v[232:235], v[168:171], v[200:203], v[232:235]\n" \
  "ds_write_b128 v2, v[228:231] offset:16384\n" \
  "49:\n" \
  "s_waitcnt vmcnt(8)\n" \
  "v_max_u32 v125, v98, v99\n" \
  "v_cmp_lt_u32 vcc, s59, v125\n" \
  "s_nop 1\n" \
  "s_cbranch_vccnz 69f\n" \
  "v_mfma_f32_16x16x16_bf16 v[52:55], v[8:9], v[98:99], v[52:55]\n" \
  "v_mfma_f32_16x16x16_bf16 v[68:71], v[172:173], v[98:99], v[68:71]\n" \
  "v_mfma_f32_16x16x16_bf16 v[84:87], v[174:175], v[98:99], v[84:87]\n" \
  "v_mfma_f32_16x16x16_bf16 v[216:219], v[176:177], v[98:99], v[216:219]\n" \
  "v_mfma_f32_16x16x16_bf16 v[232:235], v[178:179], v[98:99], v[232:235]\n" \
  "42:\n" \
  "s_waitcnt vmcnt(7)\n" \
  "v_max3_u32 v125, v188, v189, v190\n" \
  "v_max_u32 v125, v125, v191\n" \
  "v_cmp_lt_u32 vcc, s59, v125\n" \
  "s_nop 1\n" \
  "s_cbranch_vccnz 62f\n" \
  "v_mfma_f32_16x16x32_bf16 v[56:59], v[132:135], v[188:191], v[56:59]\n" \
  "v_mfma_f32_16x16x32_bf16 v[72:75], v[136:139], v[188:191], v[72:75]\n" \
  "v_mfma_f32_16x16x32_bf16 v[88:91], v[140:143], v[188:191], v[88:91]\n" \
  "v_mfma_f32_16x16x32_bf16 v[220:223], v[144:147], v[188:191], v[220:223]\n" \
  "v_mfma_f32_16x16x32_bf16 v[236:239], v[148:151], v[188:191], v[236:239]\n" \
  "46:\n" \
  "s_waitcnt vmcnt(6)\n" \
  "v_max3_u32 v125, v204, v205, v206\n" \
  "v_max_u32 v125, v125, v207\n" \
  "v_cmp_lt_u32 vcc, s59, v125\n" \
  "s_nop 1\n" \
  "s_cbranch_vccnz 66f\n" \
  "v_mfma_f32_16x16x32_bf16 v[56:59], v[152:155], v[204:207], v[56:59]\n" \
  "ds_write_b128 v2, v[52:55] offset:1024\n" \
  "v_mfma_f32_16x16x32_bf16 v[72:75], v[156:159], v[204:207], v[72:75]\n" \
  "ds_write_b128 v2, v[68:71] offset:5120\n" \
  "v_mfma_f32_16x16x32_bf16 v[88:91], v[160:163], v[204:207], v[88:91]\n" \
  "ds_write_b128 v2, v[84:87] offset:9216\n" \
  "v_mfma_f32_16x16x32_bf16 v[220:223], v[164:167], v[204:207], v[220:223]\n" \
  "ds_write_b128 v2, v[216:219] offset:13312\n" \
  "v_mfma_f32_16x16x32_bf16 v[236:239], v[168:171], v[204:207], v[236:239]\n" \
  "ds_write_b128 v2, v[232:235] offset:17408\n" \
  "50:\n" \
  "s_waitcnt vmcnt(5)\n" \
  "v_max_u32 v125, v100, v101\n" \
  "v_cmp_lt_u32 vcc, s59, v125\n" \
  "s_nop 1\n" \
  "s_cbranch_vccnz 70f\n" \
  "v_mfma_f32_16x16x16_bf16 v[56:59], v[8:9], v[100:101], v[56:59]\n" \
  "v_mfma_f32_16x16x16_bf16 v[72:75], v[172:173], v[100:101], v[72:75]\n" \
  "v_mfma_f32_16x16x16_bf16 v[88:91], v[174:175], v[100:101], v[88:91]\n" \
  "v_mfma_f32_16x16x16_bf16 v[220:223], v[176:177], v[100:101], v[220:223]\n" \
  "v_mfma_f32_16x16x16_bf16 v[236:239], v[178:179], v[100:101], v[236:239]\n" \
  "43:\n" \
  "s_waitcnt vmcnt(4)\n" \
  "v_max3_u32 v125, v192, v193, v194\n" \
  "v_max_u32 v125, v125, v195\n" \
  "v_cmp_lt_u32 vcc, s59, v125\n" \
  "s_nop 1\n" \
  "s_cbranch_vccnz 63f\n" \
  "v_mfma_f32_16x16x32_bf16 v[60:63], v[132:135], v[192:195], v[60:63]\n" \
  "v_mfma_f32_16x16x32_bf16 v[76:79], v[136:139], v[192:195], v[76:79]\n" \
  "v_mfma_f32_16x16x32_bf16 v[92:95], v[140:143], v[192:195], v[92:95]\n" \
  "v_mfma_f32_16x16x32_bf16 v[224:227], v[144:147], v[192:195], v[224:227]\n" \
  "v_mfma_f32_16x16x32_bf16 v[240:243], v[148:151], v[192:195], v[240:243]\n" \
  "47:\n" \
  "s_waitcnt vmcnt(3)\n" \
  "v_max3_u32 v125, v208, v209, v210\n" \
  "v_max_u32 v125, v125, v211\n" \
  "v_cmp_lt_u32 vcc, s59, v125\n" \
  "s_nop 1\n" \
  "s_cbranch_vccnz 67f\n" \
  "v_mfma_f32_16x16x32_bf16 v[60:63], v[152:155], v[208:211], v[60:63]\n" \
  "ds_write_b128 v2, v[56:59] offset:2048\n" \
  "v_mfma_f32_16x16x32_bf16 v[76:79], v[156:159], v[208:211], v[76:79]\n" \
  "ds_write_b128 v2, v[72:75] offset:6144\n" \
  "v_mfma_f32_16x16x32_bf16 v[92:95], v[160:163], v[208:211], v[92:95]\n" \
  "ds_write_b128 v2, v[88:91] offset:10240\n" \
  "v_mfma_f32_16x16x32_bf16 v[224:227], v[164:167], v[208:211], v[224:227]\n" \
  "ds_write_b128 v2, v[220:223] offset:14336\n" \
  "v_mfma_f32_16x16x32_bf16 v[240:243], v[168:171], v[208:211], v[240:243]\n" \
  "ds_write_b128 v2, v[236:239] offset:18432\n" \
  "51:\n" \
  "s_waitcnt vmcnt(2)\n" \
  "v_max_u32 v125, v102, v103\n" \
  "v_cmp_lt_u32 vcc, s59, v125\n" \
  "s_nop 1\n" \
  "s_cbranch_vccnz 71f\n" \
  "v_mfma_f32_16x16x16_bf16 v[60:63], v[8:9], v[102:103], v[60:63]\n" \
  "v_mfma_f32_16x16x16_bf16 v[76:79], v[172:173], v[102:103], v[76:79]\n" \
  "v_mfma_f32_16x16x16_bf16 v[92:95], v[174:175], v[102:103], v[92:95]\n" \
  "v_mfma_f32_16x16x16_bf16 v[224:227], v[176:177], v[102:103], v[224:227]\n" \
  "v_mfma_f32_16x16x16_bf16 v[240:243], v[178:179], v[102:103], v[240:243]\n" \
  "s_waitcnt vmcnt(0)\n" \
  "v_lshlrev_b32 v24, 16, v248\n" \
  "v_and_b32 v25, 0xffff0000, v248\n" \
  "v_lshlrev_b32 v26, 16, v249\n" \
  "v_and_b32 v27, 0xffff0000, v249\n" \
  "ds_write_b128 v2, v[60:63] offset:3072\n" \
  "ds_write_b128 v2, v[76:79] offset:7168\n" \
  "ds_write_b128 v2, v[92:95] offset:11264\n" \
  "ds_write_b128 v2, v[224:227] offset:15360\n" \
  "ds_write_b128 v2, v[240:243] offset:19456\n" \
  "v_lshlrev_b32 v16, 16, v244\n" \
  "v_and_b32 v17, 0xffff0000, v244\n" \
  "v_lshlrev_b32 v18, 16, v245\n" \
  "v_and_b32 v19, 0xffff0000, v245\n" \
  "v_lshlrev_b32 v20, 16, v246\n" \
  "v_and_b32 v21, 0xffff0000, v246\n" \
  "v_lshlrev_b32 v22, 16, v247\n" \
  "v_and_b32 v23, 0xffff0000, v247\n" \
  "v_add_u32 v8, 61440, v3\n" \
  "v_add_u32 v9, 122880, v3\n" \
  "s_lshr_b32 s56, s49, 1\n" \
  "s_add_u32 s56, s56, 16384\n" \
  "v_add_u32 v10, s56, v0\n" \
  "v_add_u32 v11, 61440, v10\n" \
  "v_add_u32 v12, 122880, v10\n" \
  "v_mov_b32 v7, 0x7ffffff0\n" \
  "s_nop 0\n" \
  "v_writelane_b32 v7, s70, 0\n" \
  "s_waitcnt lgkmcnt(0)\n" \
  "s_barrier\n" \
  "ds_read_b128 v[128:131], v3 offset:0\n" \
  "ds_read_b128 v[132:135], v3 offset:256\n" \
  "ds_read_b128 v[136:139], v3 offset:20480\n" \
  "ds_read_b128 v[140:143], v3 offset:20736\n" \
  "ds_read_b128 v[144:147], v3 offset:40960\n" \
  "ds_read_b128 v[148:151], v3 offset:41216\n" \
  "ds_read_b128 v[152:155], v8 offset:0\n" \
  "ds_read_b128 v[156:159], v8 offset:256\n" \
  "ds_read_b128 v[160:163], v8 offset:20480\n" \
  "ds_read_b128 v[164:167], v8 offset:20736\n" \
  "ds_read_b128 v[168:171], v8 offset:40960\n" \
  "ds_read_b128 v[172:175], v8 offset:41216\n" \
  "ds_read_b128 v[176:179], v9 offset:0\n" \
  "ds_read_b128 v[180:183], v9 offset:256\n" \
  "s_waitcnt lgkmcnt(12)\n" \
  "ds_read_b128 v[184:187], v9 offset:20480\n" \
  "ds_read_b128 v[188:191], v9 offset:20736\n" \
  "v_pk_add_f32 v[16:17], v[16:17], v[128:129]\n" \
  "v_pk_add_f32 v[18:19], v[18:19], v[130:131]\n" \
  "v_pk_add_f32 v[20:21], v[20:21], v[132:133]\n" \
  "v_pk_add_f32 v[22:23], v[22:23], v[134:135]\n" \
  "s_cmp_lg_u32 s52, 0\n" \
  "s_cbranch_scc1 35f\n" \
  "s_waitcnt lgkmcnt(12)\n" \
  "ds_read_b128 v[192:195], v10 offset:0\n" \
  "v_pk_add_f32 v[16:17], v[16:17], v[136:137]\n" \
  "v_pk_add_f32 v[18:19], v[18:19], v[138:139]\n" \
  "v_pk_add_f32 v[20:21], v[20:21], v[140:141]\n" \
  "v_pk_add_f32 v[22:23], v[22:23], v[142:143]\n" \
  "s_waitcnt lgkmcnt(11)\n" \
  "ds_read_b128 v[196:199], v10 offset:20480\n" \
  "v_pk_add_f32 v[16:17], v[16:17], v[144:145]\n" \
  "v_pk_add_f32 v[18:19], v[18:19], v[146:147]\n" \
  "v_pk_add_f32 v[20:21], v[20:21], v[148:149]\n" \
  "v_pk_add_f32 v[22:23], v[22:23], v[150:151]\n" \
  "s_waitcnt lgkmcnt(10)\n" \
  "ds_read_b128 v[200:203], v10 offset:40960\n" \
  "v_pk_add_f32 v[16:17], v[16:17], v[152:153]\n" \
  "v_pk_add_f32 v[18:19], v[18:19], v[154:155]\n" \
  "v_pk_add_f32 v[20:21], v[20:21], v[156:157]\n" \
  "v_pk_add_f32 v[22:23], v[22:23], v[158:159]\n" \
  "s_waitcnt lgkmcnt(9)\n" \
  "ds_read_b128 v[204:207], v11 offset:0\n" \
  "v_pk_add_f32 v[16:17], v[16:17], v[160:161]\n" \
  "v_pk_add_f32 v[18:19], v[18:19], v[162:163]\n" \
  "v_pk_add_f32 v[20:21], v[20:21], v[164:165]\n" \
  "v_pk_add_f32 v[22:23], v[22:23], v[166:167]\n" \
  "s_waitcnt lgkmcnt(8)\n" \
  "ds_read_b128 v[208:211], v11 offset:20480\n" \
  "v_pk_add_f32 v[16:17], v[16:17], v[168:169]\n" \
  "v_pk_add_f32 v[18:19], v[18:19], v[170:171]\n" \
  "v_pk_add_f32 v[20:21], v[20:21], v[172:173]\n" \
  "v_pk_add_f32 v[22:23], v[22:23], v[174:175]\n" \
  "s_waitcnt lgkmcnt(7)\n" \
  "ds_read_b128 v[212:215], v11 offset:40960\n" \
  "v_pk_add_f32 v[16:17], v[16:17], v[176:177]\n" \
  "v_pk_add_f32 v[18:19], v[18:19], v[178:179]\n" \
  "v_pk_add_f32 v[20:21], v[20:21], v[180:181]\n" \
  "v_pk_add_f32 v[22:23], v[22:23], v[182:183]\n" \
  "s_waitcnt lgkmcnt(6)\n" \
  "ds_read_b128 v[216:219], v12 offset:0\n" \
  "v_pk_add_f32 v[16:17], v[16:17], v[184:185]\n" \
  "v_pk_add_f32 v[18:19], v[18:19], v[186:187]\n" \
  "v_pk_add_f32 v[20:21], v[20:21], v[188:189]\n" \
  "v_pk_add_f32 v[22:23], v[22:23], v[190:191]\n" \
  "ds_read_b128 v[220:223], v12 offset:20480\n" \
  "v_cvt_pk_bf16_f32 v16, v16, v17\n" \
  "v_cvt_pk_bf16_f32 v17, v18, v19\n" \
  "v_cvt_pk_bf16_f32 v18, v20, v21\n" \
  "v_cvt_pk_bf16_f32 v19, v22, v23\n" \
  "buffer_store_dwordx4 v[16:19], v6, %4, s68 offen\n" \
  "s_waitcnt lgkmcnt(7)\n" \
  "v_pk_add_f32 v[24:25], v[24:25], v[192:193]\n" \
  "v_pk_add_f32 v[26:27], v[26:27], v[194:195]\n" \
  "s_waitcnt lgkmcnt(6)\n" \
  "v_pk_add_f32 v[24:25], v[24:25], v[196:197]\n" \
  "v_pk_add_f32 v[26:27], v[26:27], v[198:199]\n" \
  "s_waitcnt lgkmcnt(5)\n" \
  "v_pk_add_f32 v[24:25], v[24:25], v[200:201]\n" \
  "v_pk_add_f32 v[26:27], v[26:27], v[202:203]\n" \
  "s_waitcnt lgkmcnt(4)\n" \
  "v_pk_add_f32 v[24:25], v[24:25], v[204:205]\n" \
  "v_pk_add_f32 v[26:27], v[26:27], v[206:207]\n" \
  "s_waitcnt lgkmcnt(3)\n" \
  "v_pk_add_f32 v[24:25], v[24:25], v[208:209]\n" \
  "v_pk_add_f32 v[26:27], v[26:27], v[210:211]\n" \
  "s_waitcnt lgkmcnt(2)\n" \
  "v_pk_add_f32 v[24:25], v[24:25], v[212:213]\n" \
  "v_pk_add_f32 v[26:27], v[26:27], v[214:215]\n" \
  "s_waitcnt lgkmcnt(1)\n" \
  "v_pk_add_f32 v[24:25], v[24:25], v[216:217]\n" \
  "v_pk_add_f32 v[26:27], v[26:27], v[218:219]\n" \
  "s_waitcnt lgkmcnt(0)\n" \
  "v_pk_add_f32 v[24:25], v[24:25], v[220:221]\n" \
  "v_pk_add_f32 v[26:27], v[26:27], v[222:223]\n" \
  "v_cvt_pk_bf16_f32 v24, v24, v25\n" \
  "v_cvt_pk_bf16_f32 v25, v26, v27\n" \
  "buffer_store_dwordx2 v[24:25], v4, %4, s69 offen\n" \
  "buffer_store_dword v124, v7, %2, 0 offen sc1\n" \
  "s_branch 36f\n" \
  "35:\n" \
  "s_waitcnt lgkmcnt(12)\n" \
  "v_pk_add_f32 v[16:17], v[16:17], v[136:137]\n" \
  "v_pk_add_f32 v[18:19], v[18:19], v[138:139]\n" \
  "v_pk_add_f32 v[20:21], v[20:21], v[140:141]\n" \
  "v_pk_add_f32 v[22:23], v[22:23], v[142:143]\n" \
  "s_waitcnt lgkmcnt(10)\n" \
  "v_pk_add_f32 v[16:17], v[16:17], v[144:145]\n" \
  "v_pk_add_f32 v[18:19], v[18:19], v[146:147]\n" \
  "v_pk_add_f32 v[20:21], v[20:21], v[148:149]\n" \
  "v_pk_add_f32 v[22:23], v[22:23], v[150:151]\n" \
  "s_waitcnt lgkmcnt(8)\n" \
  "v_pk_add_f32 v[16:17], v[16:17], v[152:153]\n" \
  "v_pk_add_f32 v[18:19], v[18:19], v[154:155]\n" \
  "v_pk_add_f32 v[20:21], v[20:21], v[156:157]\n" \
  "v_pk_add_f32 v[22:23], v[22:23], v[158:159]\n" \
  "s_waitcnt lgkmcnt(6)\n" \
  "v_pk_add_f32 v[16:17], v[16:17], v[160:161]\n" \
  "v_pk_add_f32 v[18:19], v[18:19], v[162:163]\n" \
  "v_pk_add_f32 v[20:21], v[20:21], v[164:165]\n" \
  "v_pk_add_f32 v[22:23], v[22:23], v[166:167]\n" \
  "s_waitcnt lgkmcnt(4)\n" \
  "v_pk_add_f32 v[16:17], v[16:17], v[168:169]\n" \
  "v_pk_add_f32 v[18:19], v[18:19], v[170:171]\n" \
  "v_pk_add_f32 v[20:21], v[20:21], v[172:173]\n" \
  "v_pk_add_f32 v[22:23], v[22:23], v[174:175]\n" \
  "s_waitcnt lgkmcnt(2)\n" \
  "v_pk_add_f32 v[16:17], v[16:17], v[176:177]\n" \
  "v_pk_add_f32 v[18:19], v[18:19], v[178:179]\n" \
  "v_pk_add_f32 v[20:21], v[20:21], v[180:181]\n" \
  "v_pk_add_f32 v[22:23], v[22:23], v[182:183]\n" \
  "s_waitcnt lgkmcnt(0)\n" \
  "v_pk_add_f32 v[16:17], v[16:17], v[184:185]\n" \
  "v_pk_add_f32 v[18:19], v[18:19], v[186:187]\n" \
  "v_pk_add_f32 v[20:21], v[20:21], v[188:189]\n" \
  "v_pk_add_f32 v[22:23], v[22:23], v[190:191]\n" \
  "v_cvt_pk_bf16_f32 v16, v16, v17\n" \
  "v_cvt_pk_bf16_f32 v17, v18, v19\n" \
  "v_cvt_pk_bf16_f32 v18, v20, v21\n" \
  "v_cvt_pk_bf16_f32 v19, v22, v23\n" \
  "buffer_store_dwordx4 v[16:19], v6, %4, s68 offen\n" \
  "36:\n" \
  "s_branch 99f\n" \
  "81:\n" \
  "s_sleep 1\n" \
  "ds_read_b32 v162, v0 offset:42848\n" \
  "s_sub_u32 s58, s58, 1\n" \
  "s_waitcnt lgkmcnt(0)\n" \
  "s_cbranch_scc1 91b\n" \
  "v_readfirstlane_b32 s56, v162\n" \
  "s_nop 1\n" \
  "s_cmp_lt_u32 s56, 8\n" \
  "s_cbranch_scc1 81b\n" \
  "s_branch 91b\n" \
  "82:\n" \
  "s_sleep 1\n" \
  "ds_read_b32 v162, v0 offset:42848\n" \
  "s_sub_u32 s58, s58, 1\n" \
  "s_waitcnt lgkmcnt(0)\n" \
  "s_cbranch_scc1 92b\n" \
  "v_readfirstlane_b32 s56, v162\n" \
  "s_nop 1\n" \
  "s_cmp_lt_u32 s56, 16\n" \
  "s_cbranch_scc1 82b\n" \
  "s_branch 92b\n" \
  "83:\n" \
  "s_sleep 1\n" \
  "ds_read_b32 v162, v0 offset:42848\n" \
  "s_sub_u32 s58, s58, 1\n" \
  "s_waitcnt lgkmcnt(0)\n" \
  "s_cbranch_scc1 93b\n" \
  "v_readfirstlane_b32 s56, v162\n" \
  "s_nop 1\n" \
  "s_cmp_lt_u32 s56, 24\n" \
  "s_cbranch_scc1 83b\n" \
  "s_branch 93b\n" \
  "84:\n" \
  "s_sleep 1\n" \
  "ds_read_b32 v162, v0 offset:42848\n" \
  "s_sub_u32 s58, s58, 1\n" \
  "s_waitcnt lgkmcnt(0)\n" \
  "s_cbranch_scc1 94b\n" \
  "v_readfirstlane_b32 s56, v162\n" \
  "s_nop 1\n" \
  "s_cmp_lt_u32 s56, 32\n" \
  "s_cbranch_scc1 84b\n" \
  "s_branch 94b\n" \
  "60:\n" \
  "s_call_b64 s[80:81], 79f\n" \
  "s_branch 40b\n" \
  "61:\n" \
  "s_call_b64 s[80:81], 79f\n" \
  "s_branch 41b\n" \
  "62:\n" \
  "s_call_b64 s[80:81], 79f\n" \
  "s_branch 42b\n" \
  "63:\n" \
  "s_call_b64 s[80:81], 79f\n" \
  "s_branch 43b\n" \
  "64:\n" \
  "s_call_b64 s[80:81], 79f\n" \
  "s_branch 44b\n" \
  "65:\n" \
  "s_call_b64 s[80:81], 79f\n" \
  "s_branch 45b\n" \
  "66:\n" \
  "s_call_b64 s[80:81], 79f\n" \
  "s_branch 46b\n" \
  "67:\n" \
  "s_call_b64 s[80:81], 79f\n" \
  "s_branch 47b\n" \
  "68:\n" \
  "s_call_b64 s[80:81], 79f\n" \
  "s_branch 48b\n" \
  "69:\n" \
  "s_call_b64 s[80:81], 79f\n" \
  "s_branch 49b\n" \
  "70:\n" \
  "s_call_b64 s[80:81], 79f\n" \
  "s_branch 50b\n" \
  "71:\n" \
  "s_call_b64 s[80:81], 79f\n" \
  "s_branch 51b\n" \
  "79:\n" \
  "s_waitcnt vmcnt(0)\n" \
  "s_sleep 2\n" \
  "v_max3_u32 v125, v180, v181, v182\n" \
  "v_max_u32 v125, v125, v183\n" \
  "v_cmp_lt_u32 vcc, s59, v125\n" \
  "s_nop 1\n" \
  "s_cbranch_vccz 1f\n" \
  "buffer_load_dwordx4 v[180:183], v10, %2, s66 offen offset:0 sc1\n" \
  "1:\n" \
  "v_max3_u32 v125, v184, v185, v186\n" \
  "v_max_u32 v125, v125, v187\n" \
  "v_cmp_lt_u32 vcc, s59, v125\n" \
  "s_nop 1\n" \
  "s_cbranch_vccz 1f\n" \
  "buffer_load_dwordx4 v[184:187], v10, %2, s66 offen offset:256 sc1\n" \
  "1:\n" \
  "v_max3_u32 v125, v188, v189, v190\n" \
  "v_max_u32 v125, v125, v191\n" \
  "v_cmp_lt_u32 vcc, s59, v125\n" \
  "s_nop 1\n" \
  "s_cbranch_vccz 1f\n" \
  "buffer_load_dwordx4 v[188:191], v10, %2, s67 offen offset:0 sc1\n" \
  "1:\n" \
  "v_max3_u32 v125, v192, v193, v194\n" \
  "v_max_u32 v125, v125, v195\n" \
  "v_cmp_lt_u32 vcc, s59, v125\n" \
  "s_nop 1\n" \
  "s_cbranch_vccz 1f\n" \
  "buffer_load_dwordx4 v[192:195], v10, %2, s67 offen offset:256 sc1\n" \
  "1:\n" \
  "v_max3_u32 v125, v196, v197, v198\n" \
  "v_max_u32 v125, v125, v199\n" \
  "v_cmp_lt_u32 vcc, s59, v125\n" \
  "s_nop 1\n" \
  "s_cbranch_vccz 1f\n" \
  "buffer_load_dwordx4 v[196:199], v10, %2, s66 offen offset:2048 sc1\n" \
  "1:\n" \
  "v_max3_u32 v125, v200, v201, v202\n" \
  "v_max_u32 v125, v125, v203\n" \
  "v_cmp_lt_u32 vcc, s59, v125\n" \
  "s_nop 1\n" \
  "s_cbranch_vccz 1f\n" \
  "buffer_load_dwordx4 v[200:203], v10, %2, s66 offen offset:2304 sc1\n" \
  "1:\n" \
  "v_max3_u32 v125, v204, v205, v206\n" \
  "v_max_u32 v125, v125, v207\n" \
  "v_cmp_lt_u32 vcc, s59, v125\n" \
  "s_nop 1\n" \
  "s_cbranch_vccz 1f\n" \
  "buffer_load_dwordx4 v[204:207], v10, %2, s67 offen offset:2048 sc1\n" \
  "1:\n" \
  "v_max3_u32 v125, v208, v209, v210\n" \
  "v_max_u32 v125, v125, v211\n" \
  "v_cmp_lt_u32 vcc, s59, v125\n" \
  "s_nop 1\n" \
  "s_cbranch_vccz 1f\n" \
  "buffer_load_dwordx4 v[208:211], v10, %2, s67 offen offset:2304 sc1\n" \
  "1:\n" \
  "v_max_u32 v125, v96, v97\n" \
  "v_cmp_lt_u32 vcc, s59, v125\n" \
  "s_nop 1\n" \
  "s_cbranch_vccz 1f\n" \
  "s_add_u32 s56, s66, s79\n" \
  "buffer_load_dwordx2 v[96:97], v11, %2, s56 offen offset:0 sc1\n" \
  "1:\n" \
  "v_max_u32 v125, v98, v99\n" \
  "v_cmp_lt_u32 vcc, s59, v125\n" \
  "s_nop 1\n" \
  "s_cbranch_vccz 1f\n" \
  "s_add_u32 s56, s66, s79\n" \
  "buffer_load_dwordx2 v[98:99], v11, %2, s56 offen offset:128 sc1\n" \
  "1:\n" \
  "v_max_u32 v125, v100, v101\n" \
  "v_cmp_lt_u32 vcc, s59, v125\n" \
  "s_nop 1\n" \
  "s_cbranch_vccz 1f\n" \
  "s_add_u32 s56, s67, s79\n" \
  "buffer_load_dwordx2 v[100:101], v11, %2, s56 offen offset:0 sc1\n" \
  "1:\n" \
  "v_max_u32 v125, v102, v103\n" \
  "v_cmp_lt_u32 vcc, s59, v125\n" \
  "s_nop 1\n" \
  "s_cbranch_vccz 1f\n" \
  "s_add_u32 s56, s67, s79\n" \
  "buffer_load_dwordx2 v[102:103], v11, %2, s56 offen offset:128 sc1\n" \
  "1:\n" \
  "s_sub_u32 s58, s58, 1\n" \
  "s_cselect_b32 s59, -1, s59\n" \
  "s_waitcnt vmcnt(0)\n" \
  "s_setpc_b64 s[80:81]\n" \
  "30:\n" \
  "v_and_b32 v96, 31, v1\n" \
  "v_lshrrev_b32 v97, 5, v1\n" \
  "v_and_b32 v126, 19, v96\n" \
  "v_and_b32 v127, 4, v96\n" \
  "v_lshl_or_b32 v126, v127, 1, v126\n" \
  "v_bfe_u32 v127, v96, 3, 1\n" \
  "v_lshl_or_b32 v126, v127, 2, v126\n" \
  "v_lshlrev_b32 v127, 4, v126\n" \
  "v_add_u32 v130, s76, v127\n" \
  "v_lshl_add_u32 v129, v97, 9, v127\n" \
  "v_add_u32 v129, s75, v129\n" \
  "v_and_b32 v128, 19, v96\n" \
  "v_cmp_gt_u32 vcc, 16, v96\n" \
  "s_nop 1\n" \
  "v_cndmask_b32 v127, v128, v126, vcc\n" \
  "v_mul_u32_u24 v131, 320, v97\n" \
  "v_lshl_add_u32 v131, v127, 4, v131\n" \
  "v_add_u32 v131, s77, v131\n" \
  "v_mov_b32 v98, 0\n" \
  "v_mov_b32 v125, 0x3f803f80\n" \
  "v_cmp_eq_u32 vcc, 20, v96\n" \
  "s_nop 1\n" \
  "v_cndmask_b32 v98, v98, v125, vcc\n" \
  "v_cmp_gt_u32 s[60:61], 20, v96\n" \
  "s_mov_b32 exec_lo, 0\n" \
  "v_mov_b32 v254, 0\n" \
  "s_mov_b32 exec_lo, -1\n" \
  "v_mov_b32 v32, 0\n" \
  "v_mov_b32 v33, 0\n" \
  "v_mov_b32 v34, 0\n" \
  "v_mov_b32 v35, 0\n" \
  "v_mov_b32 v36, 0\n" \
  "v_mov_b32 v37, 0\n" \
  "v_mov_b32 v38, 0\n" \
  "v_mov_b32 v39, 0\n" \
  "v_mov_b32 v40, 0\n" \
  "v_mov_b32 v41, 0\n" \
  "v_mov_b32 v42, 0\n" \
  "v_mov_b32 v43, 0\n" \
  "v_mov_b32 v44, 0\n" \
  "v_mov_b32 v45, 0\n" \
  "v_mov_b32 v46, 0\n" \
  "v_mov_b32 v47, 0\n" \
  "v_mov_b32 v160, 0xff800000\n" \
  "s_mov_b32 s57, 2097152\n" \
  "32:\n" \
  "buffer_load_dwordx4 v[148:151], v129, %2, s57 offen sc1\n" \
  "buffer_load_dwordx2 v[162:163], v130, %2, s57 offen sc1\n" \
  "buffer_load_dwordx4 v[152:155], v131, %2, s57 offen sc1\n" \
  "buffer_load_dwordx4 v[156:159], v131, %2, s57 offen offset:640 sc1\n" \
  "s_waitcnt vmcnt(2)\n" \
  "v_mfma_f32_32x32x16_bf16 v[16:31], v[148:151], v[248:251], 0\n" \
  "v_mfma_f32_32x32x8_bf16 v[16:31], v[162:163], v[254:255], v[16:31]\n" \
  "s_waitcnt vmcnt(0)\n" \
  "v_cndmask_b32 v152, v98, v152, s[60:61]\n" \
  "v_cndmask_b32 v153, v98, v153, s[60:61]\n" \
  "v_cndmask_b32 v154, v98, v154, s[60:61]\n" \
  "v_cndmask_b32 v155, v98, v155, s[60:61]\n" \
  "v_cndmask_b32 v156, v98, v156, s[60:61]\n" \
  "v_cndmask_b32 v157, v98, v157, s[60:61]\n" \
  "v_cndmask_b32 v158, v98, v158, s[60:61]\n" \
  "v_cndmask_b32 v159, v98, v159, s[60:61]\n" \
  "s_nop 7\n" \
  "v_max3_f32 v126, v160, v16, v17\n" \
  "v_max3_f32 v126, v126, v18, v19\n" \
  "v_max3_f32 v126, v126, v20, v21\n" \
  "v_max3_f32 v126, v126, v22, v23\n" \
  "v_max3_f32 v126, v126, v24, v25\n" \
  "v_max3_f32 v126, v126, v26, v27\n" \
  "v_max3_f32 v126, v126, v28, v29\n" \
  "v_max3_f32 v126, v126, v30, v31\n" \
  "v_mov_b32 v127, v126\n" \
  "s_nop 1\n" \
  "v_permlane32_swap_b32 v126, v127\n" \
  "s_nop 1\n" \
  "v_max_f32 v126, v126, v127\n" \
  "v_sub_f32 v127, v160, v126\n" \
  "v_exp_f32 v127, v127\n" \
  "v_mov_b32 v160, v126\n" \
  "v_sub_f32 v16, v16, v126\n" \
  "v_sub_f32 v17, v17, v126\n" \
  "v_sub_f32 v18, v18, v126\n" \
  "v_sub_f32 v19, v19, v126\n" \
  "v_sub_f32 v20, v20, v126\n" \
  "v_sub_f32 v21, v21, v126\n" \
  "v_sub_f32 v22, v22, v126\n" \
  "v_sub_f32 v23, v23, v126\n" \
  "v_sub_f32 v24, v24, v126\n" \
  "v_sub_f32 v25, v25, v126\n" \
  "v_sub_f32 v26, v26, v126\n" \
  "v_sub_f32 v27, v27, v126\n" \
  "v_sub_f32 v28, v28, v126\n" \
  "v_sub_f32 v29, v29, v126\n" \
  "v_sub_f32 v30, v30, v126\n" \
  "v_sub_f32 v31, v31, v126\n" \
  "v_exp_f32 v132, v16\n" \
  "v_exp_f32 v133, v17\n" \
  "v_exp_f32 v134, v18\n" \
  "v_exp_f32 v135, v19\n" \
  "v_exp_f32 v136, v20\n" \
  "v_exp_f32 v137, v21\n" \
  "v_exp_f32 v138, v22\n" \
  "v_exp_f32 v139, v23\n" \
  "v_exp_f32 v140, v24\n" \
  "v_exp_f32 v141, v25\n" \
  "v_exp_f32 v142, v26\n" \
  "v_exp_f32 v143, v27\n" \
  "v_exp_f32 v144, v28\n" \
  "v_exp_f32 v145, v29\n" \
  "v_exp_f32 v146, v30\n" \
  "v_exp_f32 v147, v31\n" \
  "v_mul_f32 v32, v32, v127\n" \
  "v_mul_f32 v33, v33, v127\n" \
  "v_mul_f32 v34, v34, v127\n" \
  "v_mul_f32 v35, v35, v127\n" \
  "v_mul_f32 v36, v36, v127\n" \
  "v_mul_f32 v37, v37, v127\n" \
  "v_mul_f32 v38, v38, v127\n" \
  "v_mul_f32 v39, v39, v127\n" \
  "v_mul_f32 v40, v40, v127\n" \
  "v_mul_f32 v41, v41, v127\n" \
  "v_mul_f32 v42, v42, v127\n" \
  "v_mul_f32 v43, v43, v127\n" \
  "v_mul_f32 v44, v44, v127\n" \
  "v_mul_f32 v45, v45, v127\n" \
  "v_mul_f32 v46, v46, v127\n" \
  "v_mul_f32 v47, v47, v127\n" \
  "s_nop 1\n" \
  "v_smfmac_f32_32x32x32_bf16 v[32:47], v[152:155], v[132:139], v161\n" \
  "v_smfmac_f32_32x32x32_bf16 v[32:47], v[156:159], v[140:147], v161\n" \
  "s_add_u32 s57, s57, 122880\n" \
  "s_cmp_lt_u32 s57, 9961472\n" \
  "s_cbranch_scc1 32b\n" \
  "s_nop 15\n" \
  "v_mov_b32 v125, v40\n" \
  "v_mov_b32 v124, v40\n" \
  "s_nop 1\n" \
  "v_permlane32_swap_b32 v125, v124\n" \
  "s_nop 1\n" \
  "v_rcp_f32 v124, v124\n" \
  "s_nop 1\n" \
  "v_pk_mul_f32 v[40:41], v[40:41], v[124:125] op_sel_hi:[1,0]\n" \
  "v_pk_mul_f32 v[42:43], v[42:43], v[124:125] op_sel_hi:[1,0]\n" \
  "v_pk_mul_f32 v[32:33], v[32:33], v[124:125] op_sel_hi:[1,0]\n" \
  "v_pk_mul_f32 v[34:35], v[34:35], v[124:125] op_sel_hi:[1,0]\n" \
  "v_pk_mul_f32 v[36:37], v[36:37], v[124:125] op_sel_hi:[1,0]\n" \
  "v_pk_mul_f32 v[38:39], v[38:39], v[124:125] op_sel_hi:[1,0]\n" \
  "v_cvt_pk_bf16_f32 v20, v40, v41\n" \
  "v_cvt_pk_bf16_f32 v21, v42, v43\n" \
  "v_cvt_pk_bf16_f32 v16, v32, v33\n" \
  "v_cvt_pk_bf16_f32 v17, v34, v35\n" \
  "v_cvt_pk_bf16_f32 v18, v36, v37\n" \
  "v_cvt_pk_bf16_f32 v19, v38, v39\n" \
  "v_min_u32 v16, -2, v16\n" \
  "v_min_u32 v17, -2, v17\n" \
  "v_min_u32 v18, -2, v18\n" \
  "v_min_u32 v19, -2, v19\n" \
  "v_min_u32 v20, -2, v20\n" \
  "v_min_u32 v21, -2, v21\n" \
  "s_nop 1\n" \
  "s_branch 31b\n" \
  "99:\n"
#define ATTN_CLOB "v0", "v1", "v2", "v3", "v4", "v5", "v6", "v7", "v8", "v9", "v10", "v11", "v12", "v13", "v14", "v15", "v16", "v17", "v18", "v19", "v20", "v21", "v22", "v23", "v24", "v25", "v26", "v27", "v28", "v29", "v30", "v31", "v32", "v33", "v34", "v35", "v36", "v37", "v38", "v39", "v40", "v41", "v42", "v43", "v44", "v45", "v46", "v47", "v48", "v49", "v50", "v51", "v52", "v53", "v54", "v55", "v56", "v57", "v58", "v59", "v60", "v61", "v62", "v63", "v64", "v65", "v66", "v67", "v68", "v69", "v70", "v71", "v72", "v73", "v74", "v75", "v76", "v77", "v78", "v79", "v80", "v81", "v82", "v83", "v84", "v85", "v86", "v87", "v88", "v89", "v90", "v91", "v92", "v93", "v94", "v95", "v96", "v97", "v98", "v99", "v100", "v101", "v102", "v103", "v104", "v105", "v106", "v107", "v108", "v109", "v110", "v111", "v112", "v113", "v114", "v115", "v116", "v117", "v118", "v119", "v120", "v121", "v122", "v123", "v124", "v125", "v126", "v127", "v128", "v129", "v130", "v131", "v132", "v133", "v134", "v135", "v136", "v137", "v138", "v139", "v140", "v141", "v142", "v143", "v144", "v145", "v146", "v147", "v148", "v149", "v150", "v151", "v152", "v153", "v154", "v155", "v156", "v157", "v158", "v159", "v160", "v161", "v162", "v163", "v196", "v197", "v198", "v199", "v200", "v201", "v202", "v203", "v204", "v205", "v206", "v207", "v208", "v209", "v210", "v211", "v212", "v213", "v214", "v215", "v216", "v217", "v218", "v219", "v220", "v221", "v222", "v223", "v224", "v225", "v226", "v227", "v228", "v229", "v230", "v231", "v232", "v233", "v234", "v235", "v236", "v237", "v238", "v239", "v240", "v241", "v242", "v243", "v244", "v245", "v246", "v247", "v248", "v249", "v250", "v251", "v252", "v253", "v254", "v255", "s56", "s57", "s58", "s59", "s60", "s61", "s62", "s63", "s80", "s81", "vcc", "scc", "memory"

constexpr float QSCALE = 0.22360679774997896f * 1.4426950408889634f;

#define LD_OFFSETS(l) unsigned vx[2] = {(unsigned)(((l) >> 3) * 1280 + ((((l) & 7) ^ ((l) >> 4)) << 4)), (unsigned)((8 + ((l) >> 3)) * 1280 + ((((l) & 7) ^ ((l) >> 4) ^ 4) << 4))}; \
    unsigned vw = ((l) >> 4) * 3840 + ((((l) & 15) ^ (((l) >> 4) << 2)) << 4); \
    unsigned vg[3] = {(unsigned)(((l) / 12) * 3072 + ((l) % 12) * 16), (unsigned)(((64 + (l)) / 12) * 3072 + ((64 + (l)) % 12) * 16), (unsigned)(((128 + (l)) / 12) * 3072 + ((128 + (l)) % 12) * 16)};
#define LD_OX ((128 * rb + 16 * lw) * 1280)
#define LD_OQ (8 * lw * 3840 + min(cg, 14) * 256)
#define LD_OG (16 * (lw & 1) * 3072 + cg * 192)

__global__ __launch_bounds__(512, 2) void fused(Params p) {
  extern __shared__ __attribute__((aligned(16))) char smem[];
  const lptr lds = (lptr)smem;
  const unsigned lds0 = (unsigned)(uintptr_t)lds;
  const int wid = __builtin_amdgcn_readfirstlane(threadIdx.x >> 6), wid0 = wid;
  const int b = blockIdx.x, xcd = b & 7, j = b >> 3;
  const __amdgpu_buffer_rsrc_t wsr = __builtin_amdgcn_make_buffer_rsrc(p.ws, 0, WS_END, 0x00020000);
  STAMP(0);
#define LTS(i)
  unsigned long long pc; unsigned clen;
  asm volatile("s_getpc_b64 %0\n.Lcode_a:\n\ts_mov_b32 %1, .Lcode_z-.Lcode_a\n\ts_max_u32 %1, %1, .Lcode_y-.Lcode_a" : "=s"(pc), "=s"(clen) :: "scc");
  u32x4 s0, s1; u32x2 s2; unsigned gword, junk;
  {
    const int l = threadIdx.x & 63, lw = wid & 3, rb = (xcd & 1) * 8 + (j & 7), cg = (xcd >> 1) * 4 + (j >> 3);
    const unsigned off = ROT ? ((rb + cg) & 1) * 5 : 0;
    unsigned co = min((unsigned)(j * 4 + wid) * 512u, clen - 512u) & ~3u;
    unsigned va = (l >> 5) * 1280 + (l & 31) * 16 + (b * 8 + 2 * lw) * 1280, vb = va - (l & 31) * 8;
    LD_OFFSETS(l)
    unsigned e0 = LD_OX + off * 128, e1 = LD_OQ + off * 64 * 3840, e2 = LD_OG + off * 64 * 3072, l0 = lw * 2048, l1 = (lw >> 1) * WG_U + (lw & 1) * 3072;
    asm volatile("" : "+v"(va), "+v"(vb), "+v"(vx[0]), "+v"(vx[1]), "+v"(vw), "+v"(vg[0]), "+v"(vg[1]), "+v"(vg[2]), "+s"(co), "+s"(e0), "+s"(e1), "+s"(e2), "+s"(l0), "+s"(l1));
    asm volatile("s_cmp_gt_u32 %11, 3\n\ts_cbranch_scc1 1f\n\t"
                 "global_load_dwordx4 %0, %5, %7\n\tglobal_load_dwordx4 %1, %5, %7 offset:512\n\tglobal_load_dwordx2 %2, %6, %7 offset:1024\n\tv_mov_b32 %3, 0\n\tglobal_load_dword %3, %3, %8 sc1\n\t"
                 "s_load_dword %4, %9, %10\n\ts_load_dword %4, %9, %10 offset:64\n\ts_load_dword %4, %9, %10 offset:128\n\ts_load_dword %4, %9, %10 offset:192\n\t"
                 "s_load_dword %4, %9, %10 offset:256\n\ts_load_dword %4, %9, %10 offset:320\n\ts_load_dword %4, %9, %10 offset:384\n\ts_load_dword %4, %9, %10 offset:448\n\t"
                 "s_branch 2f\n1:\n\t" P1_FIRST "2:"
                 : "=&{v[20:23]}"(s0), "=&{v[24:27]}"(s1), "=&{v[28:29]}"(s2), "=&{v30}"(gword), "=&{s96}"(junk)
                 : "v"(va), "v"(vb), "s"(p.x), "s"(p.ws), "s"(pc), "s"(co), "s"(wid),
                   "v"(vx[0]), "v"(vx[1]), "v"(vw), "v"(vg[0]), "v"(vg[1]), "v"(vg[2]), "s"(make_srd(p.x, 2048 * 1280)), "s"(make_srd(p.wqkv, 640 * 3840)), "s"(make_srd((lw >> 1) ? p.wu : p.wg, 640 * 3072)),
                   "s"(e0), "s"(e1), "s"(e2), "s"(l0), "s"(l1) : "memory", "scc");
  }
#define BLOCK_IDS int b = blockIdx.x, wid = wid0; asm volatile("" : "+s"(b), "+s"(wid)); __builtin_assume(b >= 0 && b < 256 && wid >= 0 && wid < 8); \
    const int xcd = b & 7, j = b >> 3, head = xcd * 4 + (j & 3), qb = j >> 2, odd = head & 1, m5 = (head >> 1) * 5, qg = wid & 3, kh = wid >> 2, lw = qg; \
    const int rt = xcd * 4 + (j >> 3), cg8 = j & 7, rb = (xcd & 1) * 8 + (j & 7), cg = (xcd >> 1) * 4 + (j >> 3), cgq = cg < 15 ? cg : 14;
  unsigned tag;
  u32x16 ring[2];

  u32x16 prm, pr2;
#define SET_PARAMS     { \
      const unsigned wt0 = WS_WT + cg8 * 68 * 5120, a0 = WS_AH + 2 * rt * AH_REC, orec = WS_AH + (qb * 8 + 2 * qg + kh) * AH_REC + (head >> 3) * 10240, g0 = rt * 64 * 1280 + cg8 * 160; \
      prm[0] = WS_QKV + (((1 + qb) & 7) * 8 + wid) * QKV_REC; prm[1] = kh ? 2048 : AH_REC; prm[2] = wid * KVT; prm[3] = 0; \
      prm[4] = wt0 + (20 + 6 * wid) * 5120; prm[5] = a0 + (80 + 24 * wid) * 512; prm[6] = prm[5] + AH_REC; prm[7] = wt0 + 2 * wid * 5120; prm[8] = wt0 + (16 + (wid >> 1)) * 5120 + (wid & 1) * 8; \
      prm[9] = wid * 2048; prm[10] = wid == 0 ? WS_F2 + b * 4 : 0x7ffffff0u; prm[11] = kh ? (b < 136 ? WS_WT + (((b * 4 + qg) & 7) * 68 + ((b * 4 + qg) >> 3)) * 5120 : 0x7f000000u) : WS_AH + (4 * rb + 2 * (wid >> 1)) * AH_REC + (80 + 12 * cg + 6 * (wid & 1)) * 512; prm[12] = kh * 4 * KVT; prm[13] = L_QST + qg * QSTR; prm[14] = 0; prm[15] = 0; \
      pr2[0] = orec + (head & 7) * 1024; pr2[1] = orec + 8192 + (head & 7) * 256; \
      pr2[2] = a0 + (wid >> 1) * 10240 + (wid & 1) * 4096; pr2[3] = pr2[2] + AH_REC; \
      pr2[4] = g0 + (wid >> 1) * 16 * 1280 + (wid & 1) * 64; pr2[5] = g0 + qg * 16 * 1280 + 128; \
      pr2[6] = (b == 0 && wid == 0) ? WS_G : 0x7ffffff0u; \
      pr2[7] = wid * 20480; pr2[8] = (8 * (wid & 1) + (wid >> 1)) * 1024; pr2[9] = L_OX + (wid ^ 4) * KVT; \
      pr2[10] = wid == 0 ? WS_HINT + (qb * 4 + (head >> 3)) * HINT_STR + (head & 7) * 4 : 0x7ffffff0u; \
      pr2[11] = (80 + (odd ? m5 + 3 : m5)) * 512; pr2[12] = (80 + m5 + 2) * 512 + odd * 8; pr2[13] = 81920 + head * 1280; \
      pr2[14] = WS_HINT + ((rt >> 2) * 4 + (wid >> 1)) * HINT_STR + (wid & 1) * 16; pr2[15] = 8192 - (wid & 1) * 3072; \
    } \
    _Pragma("unroll") for (int i = 0; i < 16; ++i) { prm[i] = __builtin_amdgcn_readfirstlane(prm[i]); pr2[i] = __builtin_amdgcn_readfirstlane(pr2[i]); }

  {
#define LANE_IDS int lane; asm volatile("v_mbcnt_lo_u32_b32 %0, -1, 0\n\tv_mbcnt_hi_u32_b32 %0, -1, %0" : "=v"(lane)); __builtin_assume(lane >= 0 && lane < 64); \
    const int r32 = lane & 31, hh = lane >> 5, i16 = lane & 15, q4 = i16 >> 2, p4 = i16 & 3, gq = (lane >> 4) & 1, pperm = ((p4 & 1) << 1) | (p4 >> 1);
    if (__builtin_expect(wid >= 4, 1)) {
      __builtin_amdgcn_s_setprio(3);
      LANE_IDS BLOCK_IDS
      const unsigned off = ROT ? ((rb + cg) & 1) * 5 : 0;
      const u32x4 rx = make_srd(p.x, 2048 * 1280), rq = make_srd(p.wqkv, 640 * 3840), rg = make_srd((lw >> 1) ? p.wu : p.wg, 640 * 3072);
      const unsigned ox[2] = {LD_OX + off * 128, LD_OX - off * 128}, oq[2] = {LD_OQ + off * 64 * 3840, LD_OQ - off * 64 * 3840}, og[2] = {LD_OG + off * 64 * 3072, LD_OG - off * 64 * 3072};
      const unsigned lx = lds0 + lw * 2048, lg = lds0 + (lw >> 1) * WG_U + (lw & 1) * 3072;
      LD_OFFSETS(lane)
      const char* ta; unsigned tstr;
      lptr cq, cgp, cgm; f32x4 zq = {}, zg = {}; bf16x8 sel, selm; int sidx; u32x4 ptr[5]; unsigned vpt; float csg, csq; u32x2 og2, oq2;
      const int u = b * 4 + lw; const bool pt = u < 544;
      const int pks = min(u >> 3, 67), pcg = u & 7, ptl = L_WG - 4096 + lw * 1024;
      unsigned vkv[3], vf1, vq1, vq2; u32x2 stg; u32x4 f1;
#define LX(L, G, K, H) dma_b<L, G>(vx[K], rx, lx, ox[H])
#define LQ(L, G, H) dma_b<L, G>(vw, rq, lx, oq[H])
#define LG(L, G, K, H) dma_b<L, G>(vg[K], rg, lg, og[H])
#define LT(W) asm volatile("s_mov_b32 m0, %1\n\ts_nop 0\n\tglobal_load_lds_dword %0, off" :: "v"(ta + (size_t)(W) * tstr), "s"(lds0 + L_JUNK) : "memory")
#define CSRQ(N, O) const bf16x8 fa##N = lds_tr(cq + (O), 1024), fb##N = lds_tr(cq + (O) + 4096, 1024)
#define CSRG(N, O) const bf16x8 fa##N = lds_tr(cgp + (O), 768), fb##N = lds_tr(cgp + (O) + 3072, 768), fc##N = lds_tr(cgm + (O), 768), fd##N = lds_tr(cgm + (O) + 3072, 768)
#define CSMQ(N) zq = cs_two(zq, fa##N, fb##N, sel, sidx)
#define CSMG(N) zg = cs_two(zg, fa##N, fb##N, sel, sidx); zg = cs_two(zg, fc##N, fd##N, selm, sidx)
#define CSPIN PIN(zq); PIN(zg); SBAR();
#define HOOK_SETUP1 { const int id = j * 40 + lw * 10 + min(lane, 9), row = (xcd & 1) * 320 + 64 + id / 20, k = id % 20, c4 = xcd >> 1; \
        ta = k < 8 ? p.wqkv + (size_t)row * 3840 + min(c4 * 8 + k, 29) * 128 : (k < 14 ? p.wg : p.wu - 768) + (size_t)row * 3072 + (c4 * 6 + k - 8) * 128; \
        tstr = k < 8 ? 64 * 3840 : 64 * 3072; }
#define HOOK_SETUP2 { cq = lds + L_WQ + ((8 * hh + q4) * 256 + ((lw ^ q4) << 6) + gq * 32 + p4 * 8); \
        cgp = lds + L_WG + (gq * WG_U + (8 * hh + q4) * 192 + lw * 32 + p4 * 8); cgm = cgp + (4 + (lw & 1) - lw) * 32; \
        const unsigned one = 0x3f803f80u, lo = (i16 >> 1) == 0 ? one : 0u, hi = (i16 >> 1) == 2 ? one : 0u, lm = (i16 >> 1) == 4 ? one : 0u, hm = (i16 >> 1) == 6 ? one : 0u; \
        sel = __builtin_bit_cast(bf16x8, u32x4{lo, lo, hi, hi}); selm = __builtin_bit_cast(bf16x8, u32x4{lm, lm, hm, hm}); sidx = (lane & 1) ? 0xeeee : 0x4444; STAMP(1); }
#define HOOK_AFTER_INIT tag = __builtin_amdgcn_readfirstlane(*(LAS unsigned*)(lds + L_TAG));
#define HOOK_STATS_LOAD asm volatile("global_load_dwordx2 %0, %1, %2 sc1" : "=v"(stg) : "v"(WS_ST + (128 * rb + 32 * lw + r32) * 8), "s"(p.ws));
#define HOOK_STATS_USE { PIN(stg); \
        _Pragma("unroll 1") for (unsigned spins = 0; spins < SPIN_CAP && !__all(stg[1] == tag); ++spins) \
          asm volatile("s_sleep 1\n\tglobal_load_dwordx2 %0, %1, %2 sc1\n\ts_waitcnt vmcnt(0)" : "=v"(stg) : "v"(WS_ST + (128 * rb + 32 * lw + r32) * 8), "s"(p.ws) : "memory"); \
        unsigned nm = ((stg[0] & 0xffffu) ^ 0x8000u) * 0x10001u, rs = __float_as_uint(__builtin_amdgcn_rcpf(__uint_as_float(stg[0] & 0xffff0000u))); PIN(nm); PIN(rs); \
        *(LAS unsigned*)(lds + L_MU + hh * 512 + (32 * lw + r32) * 4) = hh ? rs : nm; }
#define CS_LOADS "global_load_dwordx2 %0, %2, %3 sc1\n\tglobal_load_dwordx2 %1, %2, %3 offset:512 sc1"
#define HOOK_CS_SEND { csg = zg[0] + zg[1]; csq = zq[0] + zq[1]; \
        __builtin_amdgcn_raw_buffer_store_b64(u32x2{__float_as_uint(csg), tag}, wsr, lane * 8, WS_CS + (b * 4 + lw) * 1024, 16); \
        __builtin_amdgcn_raw_buffer_store_b64(u32x2{__float_as_uint(csq), tag}, wsr, lane * 8 + 512, WS_CS + (b * 4 + lw) * 1024, 16); asm volatile("" ::: "memory"); }
#define HOOK_CS_RECV asm volatile(CS_LOADS : "=&v"(og2), "=&v"(oq2) : "v"(WS_CS + ((b ^ 8) * 4 + lw) * 1024 + lane * 8), "s"(p.ws));
#define HOOK_CS_PUB { PIN(og2); PIN(oq2); \
        _Pragma("unroll 1") for (unsigned spins = 0; spins < SPIN_CAP && !__all(og2[1] == tag && oq2[1] == tag); ++spins) \
          asm volatile("s_sleep 4\n\t" CS_LOADS "\n\ts_waitcnt vmcnt(0)" : "=&v"(og2), "=&v"(oq2) : "v"(WS_CS + ((b ^ 8) * 4 + lw) * 1024 + lane * 8), "s"(p.ws) : "memory"); \
        if (lane < 32) *(LAS u32x2*)(lds + L_CSQ + (32 * lw + lane) * 8) = split3(csq + __uint_as_float(oq2[0])); \
        *(LAS u32x2*)(lds + L_CSG + (gq * 96 + (hh ? 64 + 16 * (lw & 1) : 16 * lw) + i16) * 8) = split3(csg + __uint_as_float(og2[0])); }
#define HOOK_PT_TOUCH(K, N) if (j % (N) == (K)) { const int id = min(u * 22 + min(lane, 21), 21759); \
        asm volatile("s_mov_b32 m0, %1\n\ts_nop 0\n\tglobal_load_lds_dword %0, off" :: "v"(id < 6400 ? p.wo + id * 128 : p.wd + (id - 6400) * 128), "s"(lds0 + L_JUNK) : "memory"); }
#define HOOK_PT_LOAD { const int rr = lane >> 1;         \
        const int row = pks >= 20 ? 32 * (pks - 20) + rr : (pks < 16 ? (2 * pks + (rr >> 4)) * 20 + (rr & 15) : (8 * (pks - 16) + 4 * ((rr >> 2) & 1) + (rr >> 3)) * 20 + 16 + (rr & 3)); \
        vpt = pt ? row * 1280 + (lane & 1) * 16 + pcg * 160 : 0; } \
        asm volatile("global_load_dwordx4 %0, %5, %6\n\tglobal_load_dwordx4 %1, %5, %6 offset:32\n\tglobal_load_dwordx4 %2, %5, %6 offset:64\n\t" \
                     "global_load_dwordx4 %3, %5, %6 offset:96\n\tglobal_load_dwordx4 %4, %5, %6 offset:128" \
                     : "=&v"(ptr[0]), "=&v"(ptr[1]), "=&v"(ptr[2]), "=&v"(ptr[3]), "=&v"(ptr[4]) : "v"(vpt), "s"(pks < 20 ? p.wo : p.wd));
#define HOOK_PT_TILE { lptr a = lds + ptl + ((8 * (lane >> 4) + q4) * 32 + p4 * 8); \
        _Pragma("unroll") for (int t = 0; t < 5; ++t) { PIN(ptr[t]); *(LAS u32x4*)(lds + ptl + lane * 16) = ptr[t]; asm volatile("" ::: "memory"); \
          const u32x4 f = __builtin_bit_cast(u32x4, lds_tr(a, 128)); asm volatile("" ::: "memory");         \
          _Pragma("unroll") for (int e = 0; e < 4; ++e) { const int k = t < 3 ? t : t + 1; ring[k >> 2][4 * (k & 3) + e] = f[e]; if (t == 2) ring[0][12 + e] = f[e]; } } \
        PIN(ring[0]); PIN(ring[1]); }
#define HOOK_SETUP3 { _Pragma("unroll") for (int n = 0; n < 3; ++n) { \
          const int o = 64 * n + lane; \
          if (o < 96) { \
            const int kp = o >> 5, sl = o & 31, gp = odd ? (kp == 2 ? m5 + 2 : m5 + 3 + kp) : m5 + kp; \
            const int key = (sl & ~12) | ((sl & 4) << 1) | ((sl & 8) >> 1); \
            vkv[n] = (80 + gp) * 512 + key * 16; \
          } else { \
            const int v = o - 96, blk = v / 24, i = v - blk * 24; \
            const int dd = i < 16 ? 8 * ((i >> 2) & 1) + 4 * (i >> 3) + (i & 3) : i; \
            vkv[n] = 81920 + head * 1280 + blk * 320 + dd * 16; \
          } } \
          \
        vf1 = WS_F1 + ((min(lane >> 3, 2) * 640 + head * 20 + (((lane >> 2) & 1) ? 19 : 0)) >> 7) * F1_STR + (lane & 3) * 16; \
        vq1 = (odd ? m5 + 3 : m5) * 512 + lane * 16; vq2 = hh * QKV_REC + (m5 + 2) * 512 + r32 * 16; \
        PIN(vkv[0]); PIN(vkv[1]); PIN(vkv[2]); PIN(vf1); PIN(vq1); PIN(vq2); }
#define HOOK_F1_SET __builtin_amdgcn_raw_buffer_store_b32(tag, wsr, (cg < 15 && lw == 0 && lane == 0) ? WS_F1 + cg * F1_STR + rb * 4 : 0x7ffffff0u, 0, 16); asm volatile("" ::: "memory");
#define HOOK_PARAMS { BLOCK_IDS SET_PARAMS } asm volatile("" : "+{s[40:55]}"(prm), "+{s[64:79]}"(pr2));
#define HOOK_F1_LOAD asm volatile("buffer_load_dwordx4 %0, %1, %2, 0 offen sc1" : "=v"(f1) : "v"(lw == 0 ? vf1 : 0x7f000000u), "s"(make_srd(p.ws, WS_END)));
      P1_LOADER
      STAMP(3); LTS(3)
#define PSLEEP 6
      if (lw == 0) *(LAS u32x4*)(lds + L_TAB + lane * 16) = u32x4{vkv[0], vkv[1], vkv[2], 0u};
      if (lane < 32) { const unsigned ov = (lane & 1) ? 0u : 0x3f803f80u;
        *(LAS u32x4*)(lds + kvs(0) + (lane >> 4) * 8 * KVT + (2 * lw + ((lane >> 3) & 1)) * KVT + 1536 + ((lane >> 1) & 3) * 384 + 320 + (lane & 1) * 16) = u32x4{ov, ov, ov, ov}; }
      if (lw == 0) *(LAS unsigned*)(lds + kvs(0) + 1536 + 22 * 16) = 0u;
      unsigned dq = L_QST + lw * QSTR, dk = kvs(0) + 2 * lw * KVT, src0 = WS_QKV + (qb * 8 + 2 * lw) * QKV_REC; u32x4 srdw = make_srd(p.ws, WS_END);
      asm volatile("" : "+s"(dq), "+s"(dk), "+s"(src0), "+s"(srdw));
#define PDELAY 0
      if (lw == 0) {
        vmwait<0>(); PIN(f1);
        if (!__all(f1[0] == tag && f1[1] == tag && f1[2] == tag && f1[3] == tag)) {
#pragma unroll 1
          for (unsigned spins = 0; spins < SPIN_CAP; ++spins) {
            __builtin_amdgcn_s_sleep(PSLEEP);
            asm volatile("global_load_dwordx4 %0, %1, %2 sc1\n\ts_waitcnt vmcnt(0)" : "=v"(f1) : "v"(vf1), "s"(p.ws) : "memory");
            if (__all(f1[0] == tag && f1[1] == tag && f1[2] == tag && f1[3] == tag)) break;
          }
        }
        *(volatile LAS unsigned*)(lds + L_TAG + 4) = 1u;
      } else {
#pragma unroll 1
        for (unsigned spins = 0; spins < 64u * SPIN_CAP && !__builtin_amdgcn_readfirstlane(*(volatile LAS unsigned*)(lds + L_TAG + 4)); ++spins) __builtin_amdgcn_s_sleep(1);
      }
      asm volatile("" ::: "memory");
      STAMP(4); LTS(4)
      asm volatile("s_mov_b32 m0, %6\n\ts_add_u32 %0, %8, 122880\n\tbuffer_load_dwordx4 %1, %10, %8 offen sc1 lds\n\t"
                   "s_add_u32 m0, m0, 1024\n\ts_nop 0\n\tbuffer_load_dwordx4 %1, %10, %0 offen sc1 lds\n\t"
                   "s_add_u32 m0, m0, 1024\n\ts_nop 0\n\tbuffer_load_dwordx4 %2, %10, %8 offen sc1 lds\n\t"
                   "s_mov_b32 m0, %7\n\ts_nop 0\n\tbuffer_load_dwordx4 %3, %10, %8 offen sc1 lds\n\t"
                   "s_add_u32 m0, m0, 3072\n\ts_nop 0\n\tbuffer_load_dwordx4 %3, %10, %0 offen sc1 lds\n\t"
                   "s_add_u32 m0, %7, 1024\n\ts_mov_b32 exec_hi, %11\n\tbuffer_load_dwordx4 %4, %10, %8 offen sc1 lds\n\t"
                   "s_add_u32 m0, m0, 3072\n\ts_nop 0\n\tbuffer_load_dwordx4 %4, %10, %0 offen sc1 lds\n\t"
                   "s_add_u32 m0, %7, 2048\n\ts_mov_b64 exec, %9\n\tbuffer_load_dwordx4 %5, %10, %8 offen sc1 lds\n\t"
                   "s_add_u32 m0, m0, 3072\n\ts_nop 0\n\tbuffer_load_dwordx4 %5, %10, %0 offen sc1 lds\n\ts_mov_b64 exec, -1"
                   : "=&s"(junk) : "v"(vq1), "v"(vq2), "v"(vkv[0]), "v"(vkv[1]), "v"(vkv[2]), "s"(dq), "s"(dk), "s"(src0), "s"(0x0fffff0fffff0fffull), "s"(srdw), "s"(0xff0fffffu) : "memory", "scc");
      __builtin_amdgcn_s_setprio(0);
      HOOK_PT_TILE
    } else {
      LANE_IDS BLOCK_IDS
      const int wr = wid >> 1, wc = wid & 1;
      const bool vtype = cgq >= 10;
      u32x16 vin; u32x4 sp;
      {
#pragma unroll
      for (int a = 0; a < 4; ++a) vin[a] = L_X + (64 * wr + r32) * 128 + (((2 * a + hh) ^ ((r32 >> 1) & 7)) << 4);
#pragma unroll
      for (int t = 0; t < 2; ++t) vin[4 + t] = L_WQ + (8 * hh + q4) * 256 + (((2 * wc + t) ^ q4) << 6) + gq * 32 + (vtype ? p4 : pperm) * 8;
      vin[6] = L_WG + gq * WG_U + (8 * hh + q4) * 192 + wc * 96 + pperm * 8;
      const int sw = (r32 & ~12) | ((r32 & 4) << 1) | ((r32 & 8) >> 1);
#pragma unroll
      for (int t = 0; t < 2; ++t) { const int col = 128 * (cg - 10) + 64 * wc + 32 * t + r32, hd = col / 20, d = col - hd * 20;
        vin[7 + t] = cg == 15 ? 0x7f000000u : (vtype ? 81920 + hd * 1280 + hh * 320 + d * 16 : (16 * cg + 8 * wc) * 512 + lane * 16); }
      vin[9] = L_CSQ + (64 * wc + (vtype ? r32 : sw)) * 8;
      vin[10] = L_CSG + ((sw >> 4) * 96 + 48 * wc + (sw & 15)) * 8;
      vin[11] = L_MU + (64 * wr + r32) * 4;
      vin[12] = L_RS + (64 * wr + 4 * hh) * 4;
      vin[13] = 0;
      SET_PARAMS
      asm volatile("" : "+{s[40:55]}"(prm), "+{s[64:79]}"(pr2));
      vin[14] = 0; vin[15] = 0;
        PIN(vin);
        asm volatile("s_waitcnt vmcnt(0)" : "+{v[20:23]}"(s0), "+{v[24:27]}"(s1), "+{v[28:29]}"(s2), "+{v30}"(gword) :: "memory");
        float e[20];
#pragma unroll
        for (int i = 0; i < 4; ++i) { e[2 * i] = __uint_as_float(s0[i] << 16); e[2 * i + 1] = __uint_as_float(s0[i] & 0xffff0000u); e[8 + 2 * i] = __uint_as_float(s1[i] << 16); e[9 + 2 * i] = __uint_as_float(s1[i] & 0xffff0000u); }
#pragma unroll
        for (int i = 0; i < 2; ++i) { e[16 + 2 * i] = __uint_as_float(s2[i] << 16); e[17 + 2 * i] = __uint_as_float(s2[i] & 0xffff0000u); }
        float s = 0.f;
#pragma unroll
        for (int i = 0; i < 20; ++i) s += e[i];
        const float mean = half_sum(s) * (1.0f / 640.0f);
        float q = 0.f;
#pragma unroll
        for (int i = 0; i < 20; ++i) { const float d = e[i] - mean; q += d * d; }
        const float var = half_sum(q) * (1.0f / 640.0f);
        const float sd = __builtin_sqrtf(bf_round(bf_round(var) + 1e-5f));
        tag = __builtin_amdgcn_readfirstlane(gword) * 1664525u + 1013904223u;
        if (r32 == 0) __hip_atomic_store((GAS unsigned long long*)(p.ws + WS_ST + (b * 8 + 2 * wid + hh) * 8), ((unsigned long long)tag << 32) | pk2(mean, sd), RLX_AGENT);
      }
      *(LAS u32x4*)(lds + L_TAG) = u32x4{tag, 0u, 0u, 0u};
      sp[0] = WS_QKV + (4 * rb + 2 * wr) * QKV_REC; sp[1] = sp[0] + QKV_REC; sp[2] = __builtin_amdgcn_readfirstlane(cgq >= 10 ? 1 : 0); sp[3] = __builtin_amdgcn_readfirstlane(__float_as_uint(cg < 5 ? QSCALE : 1.0f));
      asm volatile(P1_ASM : "=&{v[164:179]}"(ring[0]), "=&{v[180:195]}"(ring[1]), "+{v[0:15]}"(vin), "+{s[40:55]}"(prm), "+{s[64:79]}"(pr2) : "s"(make_srd(p.ws, WS_END)), "{s[92:95]}"(sp), "{s96}"(junk) : P1_CLOB);
      asm volatile(".Lcode_y:");
    }
  }

  {
    asm volatile(ATTN_ASM
                 : "+{v[164:179]}"(ring[0]), "+{v[180:195]}"(ring[1])
                 : "s"(make_srd(p.ws, WS_END)), "s"(make_srd(p.x, 2048 * 1280)), "s"(make_srd(p.out, 2048 * 1280)), "{s[40:55]}"(prm), "{s[64:79]}"(pr2), "{s97}"(__builtin_amdgcn_readfirstlane(tag))

                 : ATTN_CLOB);
    STAMP(14);
  }
#define PBLK -1
  if (PROBE >= 0 && (PBLK < 0 || blockIdx.x == PBLK)) asm volatile("s_add_u32 s100, s100, 8000\n1:\n\ts_sleep 1\n\ts_memrealtime s[98:99]\n\ts_waitcnt lgkmcnt(0)\n\ts_sub_u32 s98, s98, s100\n\ts_cmp_lt_i32 s98, 0\n\ts_cbranch_scc1 1b" ::: "s98", "s99", "s100", "s101", "scc", "memory");
  asm volatile(".Lcode_z:");
}

extern "C" void kernel_launch(void* const* d_in, const int* in_sizes, int n_in, void* d_out, int out_size, void* d_ws, size_t ws_size, hipStream_t stream) {
  Params p;
  p.x = (const char*)d_in[0]; p.wqkv = (const char*)d_in[1]; p.wo = (const char*)d_in[2];
  p.wg = (const char*)d_in[4]; p.wu = (const char*)d_in[5]; p.wd = (const char*)d_in[6];
  p.out = (char*)d_out; p.ws = (char*)d_ws;
  (void)hipFuncSetAttribute((const void*)fused, hipFuncAttributeMaxDynamicSharedMemorySize, LDS_BYTES);
  hipLaunchKernelGGL(fused, dim3(256), dim3(512), LDS_BYTES, stream, p);
}
```
